```python
import torch

def _softmax(x, axis=-1):
    return torch.softmax(x, dim=axis)
def _gelu(x):
    return torch.nn.functional.gelu(x, approximate="tanh")
def _silu(x):
    return torch.nn.functional.silu(x)
def _sigmoid(x):
    return torch.sigmoid(x)
def _layernorm(x, axis=-1, eps=1e-5):
    m = x.mean(dim=axis, keepdim=True)
    v = x.var(dim=axis, keepdim=True, unbiased=False)
    return (x - m) / torch.sqrt(v + eps)
def _scatter_add(tgt, idx, src, axis=0):
    return tgt.clone().index_add_(axis, idx, src)
def _segment_sum(src, seg, n):
    out = torch.zeros((n,) + src.shape[1:], dtype=src.dtype)
    return out.index_add_(0, seg, src)
def _pad(x, pw):
    flat = tuple(v for pair in reversed(pw) for v in pair)
    return torch.nn.functional.pad(x, flat)

kernel_name = 'sequential_preLN__sliding_local_dilated__dense_activation_gelu__prompt'

def setup_inputs():

    _w = (lambda a: (lambda t: t.to(torch.bfloat16) if t.is_floating_point() else t)(torch.as_tensor(a)))
    def _A_setup():
        B, S, D = 1, 2048, 640
        return {
            "x": _w((lambda *s: torch.randn(*s, dtype=torch.bfloat16) * 0.05)(B, S, D)),
            "wqkv": _w((lambda *s: torch.randn(*s, dtype=torch.bfloat16) * 0.05)(D, 3 * D)),
            "wo": _w((lambda *s: torch.randn(*s, dtype=torch.bfloat16) * 0.05)(D, D)),
        }
    _A_in = _A_setup()

    def _B_setup():
        B, S, D, F = 1, 2048, 640, 1728
        return {
            "x": _w((lambda *s: torch.randn(*s, dtype=torch.bfloat16) * 0.05)(B, S, D)),
            "w1": _w((lambda *s: torch.randn(*s, dtype=torch.bfloat16) * 0.8)(D, F) * (D ** -0.5)),
            "b1": _w((lambda *s: torch.randn(*s, dtype=torch.bfloat16) * 0.8)(F) * 0.01),
            "w2": _w((lambda *s: torch.randn(*s, dtype=torch.bfloat16) * 0.8)(F, D) * (F ** -0.5)),
            "b2": _w((lambda *s: torch.randn(*s, dtype=torch.bfloat16) * 0.8)(D) * 0.01),
        }
    _B_in = _B_setup()

    return {
        "A_x": _A_in["x"],
        "A_wqkv": _A_in["wqkv"],
        "A_wo": _A_in["wo"],
        "B_x": _B_in["x"],
        "B_w1": _B_in["w1"],
        "B_b1": _B_in["b1"],
        "B_w2": _B_in["w2"],
        "B_b2": _B_in["b2"],
    }

def reference(A_x, A_wqkv, A_wo, B_x, B_w1, B_b1, B_w2, B_b2):
    def _A_ref(x, wqkv, wo):
        B, S, D = x.shape
        H = 32; Dh = D // H
        W, dil = 8, 2
        qkv = (lambda x, *d: x.permute(*d))((x @ wqkv).reshape(B, S, 3, H, Dh), 2, 0, 3, 1, 4)
        q, k, v = qkv[0], qkv[1], qkv[2]
        scale = Dh ** -0.5
        pos = torch.arange(S)
        off = torch.arange(W) * dil
        idx = pos[:, None] - off[None, :]
        mask = idx >= 0
        idx_c = torch.where(mask, idx, 0).reshape(-1)
        kg = k[:, :, idx_c, :].reshape(B, H, S, W, Dh)
        vg = v[:, :, idx_c, :].reshape(B, H, S, W, Dh)
        logits = torch.einsum("bhsd,bhswd->bhsw", q, kg) * scale
        logits = torch.where(mask[None, None, :, :], logits, -1e9)
        attn = _softmax(logits, axis=-1)
        o = torch.einsum("bhsw,bhswd->bhsd", attn, vg)
        return (lambda x, *d: x.permute(*d))(o, 0, 2, 1, 3).reshape(B, S, D) @ wo

    def _B_ref(x, w1, b1, w2, b2):
        h = torch.einsum("bsd,df->bsf", x, w1) + b1
        h = _gelu(h)
        return torch.einsum("bsf,fd->bsd", h, w2) + b2

    x = A_x
    h = x + _A_ref(_layernorm(x, axis=-1), A_wqkv, A_wo)
    h2 = h + _B_ref(_layernorm(h, axis=-1), B_w1, B_b1, B_w2, B_b2)
    return h2
```

```cpp
#include <hip/hip_runtime.h>
#include <hip/hip_bf16.h>
typedef unsigned short u16;
__device__ __forceinline__ float b2f(u16 b){ return __uint_as_float(((unsigned)b)<<16); }
__device__ __forceinline__ u16 f2b(float f){ __hip_bfloat16 h = __float2bfloat16(f); return *(u16*)&h; }
constexpr int S=2048,D=640,F=1728,H=32,DH=20;
__global__ void ln_k(const u16* x,u16* y){
  int r=blockIdx.x; __shared__ float red[64];
  float s=0; for(int i=threadIdx.x;i<D;i+=64) s+=b2f(x[r*D+i]);
  for(int o=32;o;o>>=1) s+=__shfl_xor(s,o);
  float m=s/D; float v=0; for(int i=threadIdx.x;i<D;i+=64){float d=b2f(x[r*D+i])-m; v+=d*d;}
  for(int o=32;o;o>>=1) v+=__shfl_xor(v,o);
  float rs=rsqrtf(v/D+1e-5f);
  for(int i=threadIdx.x;i<D;i+=64) y[r*D+i]=f2b((b2f(x[r*D+i])-m)*rs);
}
template<int MODE> __global__ void gemm_k(const u16*A,const u16*B,u16*C,int N,int K,const u16*bias,const u16*res){
  int n=blockIdx.x*64+threadIdx.x; int m0=blockIdx.y*8; if(n>=N) return;
  float acc[8]={};
  for(int k=0;k<K;++k){ float b=b2f(B[k*N+n]);
    for(int i=0;i<8;++i) acc[i]+=b2f(A[(m0+i)*K+k])*b; }
  for(int i=0;i<8;++i){ float v=acc[i];
    if(MODE==2||MODE==3) v+=b2f(bias[n]);
    if(MODE==2){ float u=0.7978845608028654f*(v+0.044715f*v*v*v); v=0.5f*v*(1.f+tanhf(u)); }
    if(MODE==1||MODE==3) v+=b2f(res[(m0+i)*N+n]);
    C[(m0+i)*N+n]=f2b(v);}
}
__global__ void attn_k(const u16*qkv,u16*o){
  int s=blockIdx.x; int h=threadIdx.x; if(h>=H) return;
  float l[8]; float mx=-1e30f;
  for(int w=0;w<8;++w){int t=s-2*w; float a=-1e9f; if(t>=0){a=0; for(int d=0;d<DH;++d) a+=b2f(qkv[s*1920+h*DH+d])*b2f(qkv[t*1920+640+h*DH+d]); a*=0.22360679774997896f;} l[w]=a; mx=fmaxf(mx,a);}
  float sum=0; for(int w=0;w<8;++w){l[w]=__expf(l[w]-mx); sum+=l[w];}
  for(int d=0;d<DH;++d){float a=0; for(int w=0;w<8;++w){int t=s-2*w; if(t<0)t=0; a+=l[w]*b2f(qkv[t*1920+1280+h*DH+d]);} o[s*D+h*DH+d]=f2b(a/sum);}
}
extern "C" void kernel_launch(void* const* d_in, const int* in_sizes, int n_in, void* d_out, int out_size, void* d_ws, size_t ws_size, hipStream_t stream) {
  const u16*x=(const u16*)d_in[0],*wqkv=(const u16*)d_in[1],*wo=(const u16*)d_in[2],*w1=(const u16*)d_in[4],*b1=(const u16*)d_in[5],*w2=(const u16*)d_in[6],*b2=(const u16*)d_in[7];
  u16*ws=(u16*)d_ws; u16*ln=ws; u16*qkv=ln+S*D; u16*o=qkv+S*1920; u16*h=o+S*D; u16*g=h+S*D;
  ln_k<<<S,64,0,stream>>>(x,ln);
  gemm_k<0><<<dim3(30,S/8),64,0,stream>>>(ln,wqkv,qkv,1920,D,nullptr,nullptr);
  attn_k<<<S,64,0,stream>>>(qkv,o);
  gemm_k<1><<<dim3(10,S/8),64,0,stream>>>(o,wo,h,D,D,nullptr,x);
  ln_k<<<S,64,0,stream>>>(h,ln);
  gemm_k<2><<<dim3(27,S/8),64,0,stream>>>(ln,w1,g,F,D,b1,nullptr);
  gemm_k<3><<<dim3(10,S/8),64,0,stream>>>(g,w2,(u16*)d_out,D,F,b2,h);
}
```

```cpp
#include <hip/hip_runtime.h>

__device__ const unsigned retile_units[1024 * 40] = {
0,44,1233580,76,1198668,0,0,0,2883584,3702784,5778688,5996544,6955520,0,0,0,2884608,3703808,5779712,5997568,6956544,0,0,0,2885632,3704832,5917952,5998592,6957568,0,0,0,2886656,3705856,5918976,5999616,6958592,0,0,0,
128,172,1233708,204,1198796,0,0,0,2887680,3706880,5920000,6000640,7230976,0,0,0,2985984,3707904,5921024,6273024,7232000,0,0,0,2987008,3708928,5922048,6274048,7233024,0,0,0,2988032,3846144,5923072,6275072,7234048,0,0,0,
256,300,1244204,332,1198924,0,0,0,2989056,3847168,3767808,6276096,7235072,0,0,0,2990080,3848192,3768832,6277120,7507456,0,0,0,3088384,3849216,3769856,6549504,7508480,0,0,0,3089408,3850240,3770880,6550528,7509504,0,0,0,
384,428,1244332,460,1199052,0,0,0,3090432,3851264,3771904,6551552,7510528,0,0,0,3091456,3852288,3772928,6552576,7511552,0,0,0,3092480,3989504,3773952,6553600,7783936,0,0,0,3190784,3990528,3911168,6825984,7784960,0,0,0,
512,556,1244460,588,1199180,0,0,0,3191808,3991552,3912192,6827008,7785984,0,0,0,3192832,3992576,3913216,6828032,7787008,0,0,0,3193856,3993600,3914240,6829056,7788032,0,0,0,3194880,3994624,3915264,6830080,8060416,0,0,0,
640,684,1244588,716,1199308,0,0,0,3293184,3995648,3916288,7102464,8061440,0,0,0,3294208,4132864,3917312,7103488,8062464,0,0,0,3295232,4133888,4054528,7104512,8063488,0,0,0,3296256,4134912,4055552,7105536,8064512,0,0,0,
768,812,1244716,844,1203276,0,0,0,3297280,4135936,4056576,7106560,6125312,0,0,0,3395584,4136960,4057600,7378944,6126336,0,0,0,3396608,4137984,4058624,7379968,6127360,0,0,0,3397632,4139008,4059648,7380992,6128384,0,0,0,
896,940,1244844,972,1203404,0,0,0,3398656,4276224,4060672,7382016,6129408,0,0,0,3399680,4277248,4197888,7383040,6401792,0,0,0,3497984,4278272,4198912,7655424,6402816,0,0,0,3499008,4279296,4199936,7656448,6403840,0,0,0,
1024,1068,1244972,1100,1203532,0,0,0,3500032,4280320,4200960,7657472,6404864,0,0,0,3501056,4281344,4201984,7658496,6405888,0,0,0,3502080,4282368,4203008,7659520,6678272,0,0,0,3600384,4419584,4204032,7931904,6679296,0,0,0,
1152,1196,1245100,1228,1203660,0,0,0,3601408,4420608,4341248,7932928,6680320,0,0,0,3602432,4421632,4342272,7933952,6681344,0,0,0,3603456,4422656,4343296,7934976,6682368,0,0,0,3604480,4423680,4344320,7936000,6954752,0,0,0,
10240,1324,1245228,5196,1203788,0,0,0,2883840,4424704,4345344,5996800,6955776,0,0,0,2884864,4425728,4346368,5997824,6956800,0,0,0,2885888,4562944,4347392,5998848,6957824,0,0,0,2886912,4563968,4484608,5999872,6958848,0,0,0,
10368,1452,1245356,5324,1203916,0,0,0,2887936,4564992,4485632,6000896,7231232,0,0,0,2986240,4566016,4486656,6273280,7232256,0,0,0,2987264,4567040,4487680,6274304,7233280,0,0,0,2988288,4568064,4488704,6275328,7234304,0,0,0,
10496,1580,1245484,5452,1204044,0,0,0,2989312,4569088,4489728,6276352,7235328,0,0,0,2990336,4706304,4490752,6277376,7507712,0,0,0,3088640,4707328,4627968,6549760,7508736,0,0,0,3089664,4708352,4628992,6550784,7509760,0,0,0,
10624,1708,1245612,5580,1204172,0,0,0,3090688,4709376,4630016,6551808,7510784,0,0,0,3091712,4710400,4631040,6552832,7511808,0,0,0,3092736,4711424,4632064,6553856,7784192,0,0,0,3191040,4712448,4633088,6826240,7785216,0,0,0,
10752,1836,1245740,5708,1204300,0,0,0,3192064,4849664,4634112,6827264,7786240,0,0,0,3193088,4850688,4771328,6828288,7787264,0,0,0,3194112,4851712,4772352,6829312,7788288,0,0,0,3195136,4852736,4773376,6830336,8060672,0,0,0,
10880,1964,1245868,5836,1204428,0,0,0,3293440,4853760,4774400,7102720,8061696,0,0,0,3294464,4854784,4775424,7103744,8062720,0,0,0,3295488,4855808,4776448,7104768,8063744,0,0,0,3296512,4993024,4777472,7105792,8064768,0,0,0,
11008,2092,1245996,5964,1228876,0,0,0,3297536,4994048,4914688,7106816,6129664,0,0,0,3395840,4995072,4915712,7379200,6130688,0,0,0,3396864,4996096,4916736,7380224,6131712,0,0,0,3397888,4997120,4917760,7381248,6132736,0,0,0,
11136,2220,1246124,6092,1229004,0,0,0,3398912,4998144,4918784,7382272,6133760,0,0,0,3399936,4999168,4919808,7383296,6406144,0,0,0,3498240,5136384,4920832,7655680,6407168,0,0,0,3499264,5137408,5058048,7656704,6408192,0,0,0,
11264,2348,1246252,6220,1229132,0,0,0,3500288,5138432,5059072,7657728,6409216,0,0,0,3501312,5139456,5060096,7658752,6410240,0,0,0,3502336,5140480,5061120,7659776,6682624,0,0,0,3600640,5141504,5062144,7932160,6683648,0,0,0,
11392,2476,1246380,6348,1229260,0,0,0,3601664,5142528,5063168,7933184,6684672,0,0,0,3602688,5279744,5064192,7934208,6685696,0,0,0,3603712,5280768,5201408,7935232,6686720,0,0,0,3604736,5281792,5202432,7936256,6959104,0,0,0,
25600,2604,1246508,10316,1229388,0,0,0,2884096,5282816,5203456,5997056,6960128,0,0,0,2885120,5283840,5204480,5998080,6961152,0,0,0,2886144,5284864,5205504,5999104,6962176,0,0,0,2887168,5285888,5206528,6000128,6963200,0,0,0,
25728,2732,1246636,10444,1229516,0,0,0,2888192,5423104,5207552,6001152,7235584,0,0,0,2986496,5424128,5344768,6273536,7236608,0,0,0,2987520,5425152,5345792,6274560,7237632,0,0,0,2988544,5426176,5346816,6275584,7238656,0,0,0,
25856,2860,1246764,10572,1229644,0,0,0,2989568,5427200,5347840,6276608,7239680,0,0,0,2990592,5428224,5348864,6277632,7512064,0,0,0,3088896,5566464,5349888,6550016,7513088,0,0,0,3089920,5567488,5350912,6551040,7514112,0,0,0,
25984,2988,1246892,10700,1229772,0,0,0,3090944,5568512,5488128,6552064,7515136,0,0,0,3091968,5569536,5489152,6553088,7516160,0,0,0,3092992,5570560,5490176,6554112,7788544,0,0,0,3191296,5571584,5491200,6826496,7789568,0,0,0,
26112,3116,1247020,10828,1229900,0,0,0,3192320,5709824,5492224,6827520,7790592,0,0,0,3193344,5710848,5493248,6828544,7791616,0,0,0,3194368,5711872,5631488,6829568,7792640,0,0,0,3195392,5712896,5632512,6830592,8065024,0,0,0,
26240,3244,1247148,10956,1230028,0,0,0,3293696,5713920,5633536,7102976,8066048,0,0,0,3294720,5714944,5634560,7104000,8067072,0,0,0,3295744,5853184,5635584,7105024,8068096,0,0,0,3296768,5854208,5636608,7106048,8069120,0,0,0,
26368,3372,1247276,11084,1233996,0,0,0,3297792,5855232,5774848,7107072,6129920,0,0,0,3396096,5856256,5775872,7379456,6130944,0,0,0,3397120,5857280,5776896,7380480,6131968,0,0,0,3398144,5858304,5777920,7381504,6132992,0,0,0,
26496,13868,1247404,11212,1234124,0,0,0,3399168,3703040,5778944,7382528,6134016,0,0,0,3400192,3704064,5779968,7383552,6406400,0,0,0,3498496,3705088,5918208,7655936,6407424,0,0,0,3499520,3706112,5919232,7656960,6408448,0,0,0,
26624,13996,1247532,11340,1234252,0,0,0,3500544,3707136,5920256,7657984,6409472,0,0,0,3501568,3708160,5921280,7659008,6410496,0,0,0,3502592,3709184,5922304,7660032,6682880,0,0,0,3600896,3846400,5923328,7932416,6683904,0,0,0,
26752,14124,1258028,11468,1234380,0,0,0,3601920,3847424,3768064,7933440,6684928,0,0,0,3602944,3848448,3769088,7934464,6685952,0,0,0,3603968,3849472,3770112,7935488,6686976,0,0,0,3604992,3850496,3771136,7936512,6959360,0,0,0,
35840,14252,1258156,15436,1234508,0,0,0,2884352,3851520,3772160,5997312,6960384,0,0,0,2885376,3852544,3773184,5998336,6961408,0,0,0,2886400,3989760,3774208,5999360,6962432,0,0,0,2887424,3990784,3911424,6000384,6963456,0,0,0,
35968,14380,1258284,15564,1234636,0,0,0,2888448,3991808,3912448,6001408,7235840,0,0,0,2986752,3992832,3913472,6273792,7236864,0,0,0,2987776,3993856,3914496,6274816,7237888,0,0,0,2988800,3994880,3915520,6275840,7238912,0,0,0,
36096,14508,1258412,15692,1234764,0,0,0,2989824,3995904,3916544,6276864,7239936,0,0,0,2990848,4133120,3917568,6277888,7512320,0,0,0,3089152,4134144,4054784,6550272,7513344,0,0,0,3090176,4135168,4055808,6551296,7514368,0,0,0,
36224,14636,1258540,15820,1234892,0,0,0,3091200,4136192,4056832,6552320,7515392,0,0,0,3092224,4137216,4057856,6553344,7516416,0,0,0,3093248,4138240,4058880,6554368,7788800,0,0,0,3191552,4139264,4059904,6826752,7789824,0,0,0,
36352,14764,1258668,15948,1235020,0,0,0,3192576,4276480,4060928,6827776,7790848,0,0,0,3193600,4277504,4198144,6828800,7791872,0,0,0,3194624,4278528,4199168,6829824,7792896,0,0,0,3195648,4279552,4200192,6830848,8065280,0,0,0,
36480,14892,1258796,16076,1235148,0,0,0,3293952,4280576,4201216,7103232,8066304,0,0,0,3294976,4281600,4202240,7104256,8067328,0,0,0,3296000,4282624,4203264,7105280,8068352,0,0,0,3297024,4419840,4204288,7106304,8069376,0,0,0,
36608,15020,1258924,16204,1239116,0,0,0,3298048,4420864,4341504,7107328,6130176,0,0,0,3396352,4421888,4342528,7379712,6131200,0,0,0,3397376,4422912,4343552,7380736,6132224,0,0,0,3398400,4423936,4344576,7381760,6133248,0,0,0,
36736,15148,1259052,16332,1239244,0,0,0,3399424,4424960,4345600,7382784,6134272,0,0,0,3400448,4425984,4346624,7383808,6406656,0,0,0,3498752,4563200,4347648,7656192,6407680,0,0,0,3499776,4564224,4484864,7657216,6408704,0,0,0,
36864,15276,1259180,16460,1239372,0,0,0,3500800,4565248,4485888,7658240,6409728,0,0,0,3501824,4566272,4486912,7659264,6410752,0,0,0,3502848,4567296,4487936,7660288,6683136,0,0,0,3601152,4568320,4488960,7932672,6684160,0,0,0,
36992,15404,1259308,16588,1239500,0,0,0,3602176,4569344,4489984,7933696,6685184,0,0,0,3603200,4706560,4491008,7934720,6686208,0,0,0,3604224,4707584,4628224,7935744,6687232,0,0,0,3605248,4708608,4629248,7936768,6959616,0,0,0,
51200,15532,1259436,41036,1239628,0,0,0,2888704,4709632,4630272,6001664,6960640,0,0,0,2889728,4710656,4631296,6002688,6961664,0,0,0,2890752,4711680,4632320,6003712,6962688,0,0,0,2891776,4712704,4633344,6004736,6963712,0,0,0,
51328,15660,1259564,41164,1239756,0,0,0,2892800,4849920,4634368,6005760,7236096,0,0,0,2991104,4850944,4771584,6278144,7237120,0,0,0,2992128,4851968,4772608,6279168,7238144,0,0,0,2993152,4852992,4773632,6280192,7239168,0,0,0,
51456,15788,1259692,41292,1239884,0,0,0,2994176,4854016,4774656,6281216,7240192,0,0,0,2995200,4855040,4775680,6282240,7512576,0,0,0,3093504,4856064,4776704,6554624,7513600,0,0,0,3094528,4993280,4777728,6555648,7514624,0,0,0,
51584,15916,1259820,41420,1240012,0,0,0,3095552,4994304,4914944,6556672,7515648,0,0,0,3096576,4995328,4915968,6557696,7516672,0,0,0,3097600,4996352,4916992,6558720,7789056,0,0,0,3195904,4997376,4918016,6831104,7790080,0,0,0,
51712,16044,1259948,41548,1240140,0,0,0,3196928,4998400,4919040,6832128,7791104,0,0,0,3197952,4999424,4920064,6833152,7792128,0,0,0,3198976,5136640,4921088,6834176,7793152,0,0,0,3200000,5137664,5058304,6835200,8065536,0,0,0,
51840,16172,1260076,41676,1240268,0,0,0,3298304,5138688,5059328,7107584,8066560,0,0,0,3299328,5139712,5060352,7108608,8067584,0,0,0,3300352,5140736,5061376,7109632,8068608,0,0,0,3301376,5141760,5062400,7110656,8069632,0,0,0,
51968,16300,1260204,41804,1244236,0,0,0,3302400,5142784,5063424,7111680,6130432,0,0,0,3400704,5280000,5064448,7384064,6131456,0,0,0,3401728,5281024,5201664,7385088,6132480,0,0,0,3402752,5282048,5202688,7386112,6133504,0,0,0,
52096,16428,1260332,41932,1244364,0,0,0,3403776,5283072,5203712,7387136,6134528,0,0,0,3404800,5284096,5204736,7388160,6406912,0,0,0,3503104,5285120,5205760,7660544,6407936,0,0,0,3504128,5286144,5206784,7661568,6408960,0,0,0,
52224,16556,1260460,42060,1244492,0,0,0,3505152,5423360,5207808,7662592,6409984,0,0,0,3506176,5424384,5345024,7663616,6411008,0,0,0,3507200,5425408,5346048,7664640,6683392,0,0,0,3605504,5426432,5347072,7937024,6684416,0,0,0,
52352,16684,1260588,42188,1244620,0,0,0,3606528,5427456,5348096,7938048,6685440,0,0,0,3607552,5428480,5349120,7939072,6686464,0,0,0,3608576,5566720,5350144,7940096,6687488,0,0,0,3609600,5567744,5351168,7941120,6959872,0,0,0,
61440,16812,1260716,46156,1244748,0,0,0,2888960,5568768,5488384,6001920,6960896,0,0,0,2889984,5569792,5489408,6002944,6961920,0,0,0,2891008,5570816,5490432,6003968,6962944,0,0,0,2892032,5571840,5491456,6004992,6963968,0,0,0,
61568,16940,1260844,46284,1244876,0,0,0,2893056,5710080,5492480,6006016,7236352,0,0,0,2991360,5711104,5493504,6278400,7237376,0,0,0,2992384,5712128,5631744,6279424,7238400,0,0,0,2993408,5713152,5632768,6280448,7239424,0,0,0,
61696,17068,1260972,46412,1245004,0,0,0,2994432,5714176,5633792,6281472,7240448,0,0,0,2995456,5715200,5634816,6282496,7512832,0,0,0,3093760,5853440,5635840,6554880,7513856,0,0,0,3094784,5854464,5636864,6555904,7514880,0,0,0,
61824,17196,1261100,46540,1245132,0,0,0,3095808,5855488,5775104,6556928,7515904,0,0,0,3096832,5856512,5776128,6557952,7516928,0,0,0,3097856,5857536,5777152,6558976,7789312,0,0,0,3196160,5858560,5778176,6831360,7790336,0,0,0,
61952,27692,1261228,46668,1245260,0,0,0,3197184,3703296,5779200,6832384,7791360,0,0,0,3198208,3704320,5780224,6833408,7792384,0,0,0,3199232,3705344,5918464,6834432,7793408,0,0,0,3200256,3706368,5919488,6835456,8065792,0,0,0,
62080,27820,1261356,46796,1245388,0,0,0,3298560,3707392,5920512,7107840,8066816,0,0,0,3299584,3708416,5921536,7108864,8067840,0,0,0,3300608,3709440,5922560,7109888,8068864,0,0,0,3301632,3846656,5923584,7110912,8069888,0,0,0,
62208,27948,1382444,46924,1290316,0,0,0,3302656,3847680,3774464,7111936,6134784,0,0,0,3400960,3848704,3775488,7384320,6135808,0,0,0,3401984,3849728,3776512,7385344,6136832,0,0,0,3403008,3850752,3777536,7386368,6137856,0,0,0,
62336,28076,1382572,47052,1290444,0,0,0,3404032,3851776,3778560,7387392,6138880,0,0,0,3405056,3852800,3779584,7388416,6411264,0,0,0,3503360,3990016,3780608,7660800,6412288,0,0,0,3504384,3991040,3917824,7661824,6413312,0,0,0,
62464,28204,1382700,47180,1290572,0,0,0,3505408,3992064,3918848,7662848,6414336,0,0,0,3506432,3993088,3919872,7663872,6415360,0,0,0,3507456,3994112,3920896,7664896,6687744,0,0,0,3605760,3995136,3921920,7937280,6688768,0,0,0,
62592,28332,1382828,47308,1290700,0,0,0,3606784,3996160,3922944,7938304,6689792,0,0,0,3607808,4133376,3923968,7939328,6690816,0,0,0,3608832,4134400,4061184,7940352,6691840,0,0,0,3609856,4135424,4062208,7941376,6964224,0,0,0,
76800,28460,1382956,51276,1290828,0,0,0,2889216,4136448,4063232,6002176,6965248,0,0,0,2890240,4137472,4064256,6003200,6966272,0,0,0,2891264,4138496,4065280,6004224,6967296,0,0,0,2892288,4139520,4066304,6005248,6968320,0,0,0,
76928,28588,1383084,51404,1290956,0,0,0,2893312,4276736,4067328,6006272,7240704,0,0,0,2991616,4277760,4204544,6278656,7241728,0,0,0,2992640,4278784,4205568,6279680,7242752,0,0,0,2993664,4279808,4206592,6280704,7243776,0,0,0,
77056,28716,1383212,51532,1291084,0,0,0,2994688,4280832,4207616,6281728,7244800,0,0,0,2995712,4281856,4208640,6282752,7517184,0,0,0,3094016,4282880,4209664,6555136,7518208,0,0,0,3095040,4420096,4210688,6556160,7519232,0,0,0,
77184,28844,1383340,51660,1291212,0,0,0,3096064,4421120,4347904,6557184,7520256,0,0,0,3097088,4422144,4348928,6558208,7521280,0,0,0,3098112,4423168,4349952,6559232,7793664,0,0,0,3196416,4424192,4350976,6831616,7794688,0,0,0,
77312,28972,1383468,51788,1291340,0,0,0,3197440,4425216,4352000,6832640,7795712,0,0,0,3198464,4426240,4353024,6833664,7796736,0,0,0,3199488,4563456,4354048,6834688,7797760,0,0,0,3200512,4564480,4491264,6835712,8070144,0,0,0,
77440,29100,1383596,51916,1291468,0,0,0,3298816,4565504,4492288,7108096,8071168,0,0,0,3299840,4566528,4493312,7109120,8072192,0,0,0,3300864,4567552,4494336,7110144,8073216,0,0,0,3301888,4568576,4495360,7111168,8074240,0,0,0,
77568,29228,1383724,52044,1295436,0,0,0,3302912,4569600,4496384,7112192,6135040,0,0,0,3401216,4706816,4497408,7384576,6136064,0,0,0,3402240,4707840,4634624,7385600,6137088,0,0,0,3403264,4708864,4635648,7386624,6138112,0,0,0,
77696,29356,1383852,52172,1295564,0,0,0,3404288,4709888,4636672,7387648,6139136,0,0,0,3405312,4710912,4637696,7388672,6411520,0,0,0,3503616,4711936,4638720,7661056,6412544,0,0,0,3504640,4712960,4639744,7662080,6413568,0,0,0,
77824,29484,1383980,52300,1295692,0,0,0,3505664,4850176,4640768,7663104,6414592,0,0,0,3506688,4851200,4777984,7664128,6415616,0,0,0,3507712,4852224,4779008,7665152,6688000,0,0,0,3606016,4853248,4780032,7937536,6689024,0,0,0,
77952,29612,1384108,52428,1295820,0,0,0,3607040,4854272,4781056,7938560,6690048,0,0,0,3608064,4855296,4782080,7939584,6691072,0,0,0,3609088,4856320,4783104,7940608,6692096,0,0,0,3610112,4993536,4784128,7941632,6964480,0,0,0,
87040,29740,1384236,56396,1295948,0,0,0,2889472,4994560,4921344,6002432,6965504,0,0,0,2890496,4995584,4922368,6003456,6966528,0,0,0,2891520,4996608,4923392,6004480,6967552,0,0,0,2892544,4997632,4924416,6005504,6968576,0,0,0,
87168,29868,1384364,56524,1296076,0,0,0,2893568,4998656,4925440,6006528,7240960,0,0,0,2991872,4999680,4926464,6278912,7241984,0,0,0,2992896,5136896,4927488,6279936,7243008,0,0,0,2993920,5137920,5064704,6280960,7244032,0,0,0,
87296,29996,1384492,56652,1296204,0,0,0,2994944,5138944,5065728,6281984,7245056,0,0,0,2995968,5139968,5066752,6283008,7517440,0,0,0,3094272,5140992,5067776,6555392,7518464,0,0,0,3095296,5142016,5068800,6556416,7519488,0,0,0,
87424,30124,1384620,56780,1296332,0,0,0,3096320,5143040,5069824,6557440,7520512,0,0,0,3097344,5280256,5070848,6558464,7521536,0,0,0,3098368,5281280,5208064,6559488,7793920,0,0,0,3196672,5282304,5209088,6831872,7794944,0,0,0,
87552,30252,1384748,56908,1296460,0,0,0,3197696,5283328,5210112,6832896,7795968,0,0,0,3198720,5284352,5211136,6833920,7796992,0,0,0,3199744,5285376,5212160,6834944,7798016,0,0,0,3200768,5286400,5213184,6835968,8070400,0,0,0,
87680,30380,1384876,57036,1296588,0,0,0,3299072,5423616,5214208,7108352,8071424,0,0,0,3300096,5424640,5351424,7109376,8072448,0,0,0,3301120,5425664,5352448,7110400,8073472,0,0,0,3302144,5426688,5353472,7111424,8074496,0,0,0,
87808,30508,1385004,57164,1300556,0,0,0,3303168,5427712,5354496,7112448,6135296,0,0,0,3401472,5428736,5355520,7384832,6136320,0,0,0,3402496,5566976,5356544,7385856,6137344,0,0,0,3403520,5568000,5357568,7386880,6138368,0,0,0,
87936,30636,1385132,57292,1300684,0,0,0,3404544,5569024,5494784,7387904,6139392,0,0,0,3405568,5570048,5495808,7388928,6411776,0,0,0,3503872,5571072,5496832,7661312,6412800,0,0,0,3504896,5572096,5497856,7662336,6413824,0,0,0,
88064,30764,1385260,57420,1300812,0,0,0,3505920,5710336,5498880,7663360,6414848,0,0,0,3506944,5711360,5499904,7664384,6415872,0,0,0,3507968,5712384,5638144,7665408,6688256,0,0,0,3606272,5713408,5639168,7937792,6689280,0,0,0,
88192,30892,1385388,57548,1300940,0,0,0,3607296,5714432,5640192,7938816,6690304,0,0,0,3608320,5715456,5641216,7939840,6691328,0,0,0,3609344,5853696,5642240,7940864,6692352,0,0,0,3610368,5854720,5643264,7941888,6964736,0,0,0,
102400,31020,1385516,81996,1301068,0,0,0,2893824,5855744,5781504,6006784,6965760,0,0,0,2894848,5856768,5782528,6007808,6966784,0,0,0,2895872,5857792,5783552,6008832,6967808,0,0,0,2896896,5858816,5784576,6009856,6968832,0,0,0,
102528,41516,1385644,82124,1301196,0,0,0,2897920,3703552,5785600,6010880,7241216,0,0,0,2996224,3704576,5786624,6283264,7242240,0,0,0,2997248,3705600,5924864,6284288,7243264,0,0,0,2998272,3706624,5925888,6285312,7244288,0,0,0,
102656,41644,1385772,82252,1301324,0,0,0,2999296,3707648,5926912,6286336,7245312,0,0,0,3000320,3708672,5927936,6287360,7517696,0,0,0,3098624,3709696,5928960,6559744,7518720,0,0,0,3099648,3846912,5929984,6560768,7519744,0,0,0,
102784,41772,1396268,82380,1301452,0,0,0,3100672,3847936,3774720,6561792,7520768,0,0,0,3101696,3848960,3775744,6562816,7521792,0,0,0,3102720,3849984,3776768,6563840,7794176,0,0,0,3201024,3851008,3777792,6836224,7795200,0,0,0,
102912,41900,1396396,82508,1301580,0,0,0,3202048,3852032,3778816,6837248,7796224,0,0,0,3203072,3853056,3779840,6838272,7797248,0,0,0,3204096,3990272,3780864,6839296,7798272,0,0,0,3205120,3991296,3918080,6840320,8070656,0,0,0,
103040,42028,1396524,82636,1301708,0,0,0,3303424,3992320,3919104,7112704,8071680,0,0,0,3304448,3993344,3920128,7113728,8072704,0,0,0,3305472,3994368,3921152,7114752,8073728,0,0,0,3306496,3995392,3922176,7115776,8074752,0,0,0,
103168,42156,1396652,82764,1305676,0,0,0,3307520,3996416,3923200,7116800,6135552,0,0,0,3405824,4133632,3924224,7389184,6136576,0,0,0,3406848,4134656,4061440,7390208,6137600,0,0,0,3407872,4135680,4062464,7391232,6138624,0,0,0,
103296,42284,1396780,82892,1305804,0,0,0,3408896,4136704,4063488,7392256,6139648,0,0,0,3409920,4137728,4064512,7393280,6412032,0,0,0,3508224,4138752,4065536,7665664,6413056,0,0,0,3509248,4139776,4066560,7666688,6414080,0,0,0,
103424,42412,1396908,83020,1305932,0,0,0,3510272,4276992,4067584,7667712,6415104,0,0,0,3511296,4278016,4204800,7668736,6416128,0,0,0,3512320,4279040,4205824,7669760,6688512,0,0,0,3610624,4280064,4206848,7942144,6689536,0,0,0,
103552,42540,1397036,83148,1306060,0,0,0,3611648,4281088,4207872,7943168,6690560,0,0,0,3612672,4282112,4208896,7944192,6691584,0,0,0,3613696,4283136,4209920,7945216,6692608,0,0,0,3614720,4420352,4210944,7946240,6964992,0,0,0,
112640,42668,1397164,87116,1306188,0,0,0,2894080,4421376,4348160,6007040,6966016,0,0,0,2895104,4422400,4349184,6008064,6967040,0,0,0,2896128,4423424,4350208,6009088,6968064,0,0,0,2897152,4424448,4351232,6010112,6969088,0,0,0,
112768,42796,1397292,87244,1306316,0,0,0,2898176,4425472,4352256,6011136,7241472,0,0,0,2996480,4426496,4353280,6283520,7242496,0,0,0,2997504,4563712,4354304,6284544,7243520,0,0,0,2998528,4564736,4491520,6285568,7244544,0,0,0,
112896,42924,1397420,87372,1306444,0,0,0,2999552,4565760,4492544,6286592,7245568,0,0,0,3000576,4566784,4493568,6287616,7517952,0,0,0,3098880,4567808,4494592,6560000,7518976,0,0,0,3099904,4568832,4495616,6561024,7520000,0,0,0,
113024,43052,1397548,87500,1306572,0,0,0,3100928,4569856,4496640,6562048,7521024,0,0,0,3101952,4707072,4497664,6563072,7522048,0,0,0,3102976,4708096,4634880,6564096,7794432,0,0,0,3201280,4709120,4635904,6836480,7795456,0,0,0,
113152,43180,1397676,87628,1306700,0,0,0,3202304,4710144,4636928,6837504,7796480,0,0,0,3203328,4711168,4637952,6838528,7797504,0,0,0,3204352,4712192,4638976,6839552,7798528,0,0,0,3205376,4713216,4640000,6840576,8070912,0,0,0,
113280,43308,1397804,87756,1306828,0,0,0,3303680,4850432,4641024,7112960,8071936,0,0,0,3304704,4851456,4778240,7113984,8072960,0,0,0,3305728,4852480,4779264,7115008,8073984,0,0,0,3306752,4853504,4780288,7116032,8075008,0,0,0,
113408,43436,1397932,87884,1331276,0,0,0,3307776,4854528,4781312,7117056,6139904,0,0,0,3406080,4855552,4782336,7389440,6140928,0,0,0,3407104,4856576,4783360,7390464,6141952,0,0,0,3408128,4993792,4784384,7391488,6142976,0,0,0,
113536,43564,1398060,88012,1331404,0,0,0,3409152,4994816,4921600,7392512,6144000,0,0,0,3410176,4995840,4922624,7393536,6416384,0,0,0,3508480,4996864,4923648,7665920,6417408,0,0,0,3509504,4997888,4924672,7666944,6418432,0,0,0,
113664,43692,1398188,88140,1331532,0,0,0,3510528,4998912,4925696,7667968,6419456,0,0,0,3511552,4999936,4926720,7668992,6420480,0,0,0,3512576,5137152,4927744,7670016,6692864,0,0,0,3610880,5138176,5064960,7942400,6693888,0,0,0,
113792,43820,1398316,88268,1331660,0,0,0,3611904,5139200,5065984,7943424,6694912,0,0,0,3612928,5140224,5067008,7944448,6695936,0,0,0,3613952,5141248,5068032,7945472,6696960,0,0,0,3614976,5142272,5069056,7946496,6969344,0,0,0,
128000,43948,1398444,92236,1331788,0,0,0,2894336,5143296,5070080,6007296,6970368,0,0,0,2895360,5280512,5071104,6008320,6971392,0,0,0,2896384,5281536,5208320,6009344,6972416,0,0,0,2897408,5282560,5209344,6010368,6973440,0,0,0,
128128,44076,1398572,92364,1331916,0,0,0,2898432,5283584,5210368,6011392,7245824,0,0,0,2996736,5284608,5211392,6283776,7246848,0,0,0,2997760,5285632,5212416,6284800,7247872,0,0,0,2998784,5286656,5213440,6285824,7248896,0,0,0,
128256,44204,1398700,92492,1332044,0,0,0,2999808,5423872,5214464,6286848,7249920,0,0,0,3000832,5424896,5351680,6287872,7522304,0,0,0,3099136,5425920,5352704,6560256,7523328,0,0,0,3100160,5426944,5353728,6561280,7524352,0,0,0,
128384,44332,1398828,92620,1332172,0,0,0,3101184,5427968,5354752,6562304,7525376,0,0,0,3102208,5428992,5355776,6563328,7526400,0,0,0,3103232,5567232,5356800,6564352,7798784,0,0,0,3201536,5568256,5357824,6836736,7799808,0,0,0,
128512,44460,1398956,92748,1332300,0,0,0,3202560,5569280,5495040,6837760,7800832,0,0,0,3203584,5570304,5496064,6838784,7801856,0,0,0,3204608,5571328,5497088,6839808,7802880,0,0,0,3205632,5572352,5498112,6840832,8075264,0,0,0,
128640,44588,1399084,92876,1332428,0,0,0,3303936,5710592,5499136,7113216,8076288,0,0,0,3304960,5711616,5500160,7114240,8077312,0,0,0,3305984,5712640,5638400,7115264,8078336,0,0,0,3307008,5713664,5639424,7116288,8079360,0,0,0,
128768,44716,1399212,93004,1336396,0,0,0,3308032,5714688,5640448,7117312,6140160,0,0,0,3406336,5715712,5641472,7389696,6141184,0,0,0,3407360,5853952,5642496,7390720,6142208,0,0,0,3408384,5854976,5643520,7391744,6143232,0,0,0,
128896,44844,1399340,93132,1336524,0,0,0,3409408,5856000,5781760,7392768,6144256,0,0,0,3410432,5857024,5782784,7393792,6416640,0,0,0,3508736,5858048,5783808,7666176,6417664,0,0,0,3509760,5859072,5784832,7667200,6418688,0,0,0,
129024,110636,1399468,93260,1336652,0,0,0,3510784,3709952,5785856,7668224,6419712,0,0,0,3511808,3710976,5786880,7669248,6420736,0,0,0,3512832,3712000,5925120,7670272,6693120,0,0,0,3611136,3713024,5926144,7942656,6694144,0,0,0,
129152,110764,1399596,93388,1336780,0,0,0,3612160,3714048,5927168,7943680,6695168,0,0,0,3613184,3715072,5928192,7944704,6696192,0,0,0,3614208,3716096,5929216,7945728,6697216,0,0,0,3615232,3853312,5930240,7946752,6969600,0,0,0,
138240,110892,1410092,97356,1336908,0,0,0,2894592,3854336,3774976,6007552,6970624,0,0,0,2895616,3855360,3776000,6008576,6971648,0,0,0,2896640,3856384,3777024,6009600,6972672,0,0,0,2897664,3857408,3778048,6010624,6973696,0,0,0,
138368,111020,1410220,97484,1337036,0,0,0,2898688,3858432,3779072,6011648,7246080,0,0,0,2996992,3859456,3780096,6284032,7247104,0,0,0,2998016,3996672,3781120,6285056,7248128,0,0,0,2999040,3997696,3918336,6286080,7249152,0,0,0,
138496,111148,1410348,97612,1337164,0,0,0,3000064,3998720,3919360,6287104,7250176,0,0,0,3001088,3999744,3920384,6288128,7522560,0,0,0,3099392,4000768,3921408,6560512,7523584,0,0,0,3100416,4001792,3922432,6561536,7524608,0,0,0,
138624,111276,1410476,97740,1337292,0,0,0,3101440,4002816,3923456,6562560,7525632,0,0,0,3102464,4140032,3924480,6563584,7526656,0,0,0,3103488,4141056,4061696,6564608,7799040,0,0,0,3201792,4142080,4062720,6836992,7800064,0,0,0,
138752,111404,1410604,97868,1337420,0,0,0,3202816,4143104,4063744,6838016,7801088,0,0,0,3203840,4144128,4064768,6839040,7802112,0,0,0,3204864,4145152,4065792,6840064,7803136,0,0,0,3205888,4146176,4066816,6841088,8075520,0,0,0,
138880,111532,1410732,97996,1337548,0,0,0,3304192,4283392,4067840,7113472,8076544,0,0,0,3305216,4284416,4205056,7114496,8077568,0,0,0,3306240,4285440,4206080,7115520,8078592,0,0,0,3307264,4286464,4207104,7116544,8079616,0,0,0,
139008,111660,1410860,98124,1341516,0,0,0,3308288,4287488,4208128,7117568,6140416,0,0,0,3406592,4288512,4209152,7389952,6141440,0,0,0,3407616,4289536,4210176,7390976,6142464,0,0,0,3408640,4426752,4211200,7392000,6143488,0,0,0,
139136,111788,1410988,98252,1341644,0,0,0,3409664,4427776,4348416,7393024,6144512,0,0,0,3410688,4428800,4349440,7394048,6416896,0,0,0,3508992,4429824,4350464,7666432,6417920,0,0,0,3510016,4430848,4351488,7667456,6418944,0,0,0,
139264,111916,1411116,98380,1341772,0,0,0,3511040,4431872,4352512,7668480,6419968,0,0,0,3512064,4432896,4353536,7669504,6420992,0,0,0,3513088,4570112,4354560,7670528,6693376,0,0,0,3611392,4571136,4491776,7942912,6694400,0,0,0,
139392,112044,1411244,98508,1341900,0,0,0,3612416,4572160,4492800,7943936,6695424,0,0,0,3613440,4573184,4493824,7944960,6696448,0,0,0,3614464,4574208,4494848,7945984,6697472,0,0,0,3615488,4575232,4495872,7947008,6969856,0,0,0,
153600,112172,1411372,143436,1342028,0,0,0,2898944,4576256,4496896,6011904,6970880,0,0,0,2899968,4713472,4497920,6012928,6971904,0,0,0,2900992,4714496,4635136,6013952,6972928,0,0,0,2902016,4715520,4636160,6014976,6973952,0,0,0,
153728,112300,1411500,143564,1342156,0,0,0,2903040,4716544,4637184,6016000,7246336,0,0,0,3001344,4717568,4638208,6288384,7247360,0,0,0,3002368,4718592,4639232,6289408,7248384,0,0,0,3003392,4719616,4640256,6290432,7249408,0,0,0,
153856,112428,1411628,143692,1342284,0,0,0,3004416,4856832,4641280,6291456,7250432,0,0,0,3005440,4857856,4778496,6292480,7522816,0,0,0,3103744,4858880,4779520,6564864,7523840,0,0,0,3104768,4859904,4780544,6565888,7524864,0,0,0,
153984,112556,1411756,143820,1342412,0,0,0,3105792,4860928,4781568,6566912,7525888,0,0,0,3106816,4861952,4782592,6567936,7526912,0,0,0,3107840,4862976,4783616,6568960,7799296,0,0,0,3206144,5000192,4784640,6841344,7800320,0,0,0,
154112,112684,1411884,143948,1342540,0,0,0,3207168,5001216,4921856,6842368,7801344,0,0,0,3208192,5002240,4922880,6843392,7802368,0,0,0,3209216,5003264,4923904,6844416,7803392,0,0,0,3210240,5004288,4924928,6845440,8075776,0,0,0,
154240,112812,1412012,144076,1342668,0,0,0,3308544,5005312,4925952,7117824,8076800,0,0,0,3309568,5006336,4926976,7118848,8077824,0,0,0,3310592,5143552,4928000,7119872,8078848,0,0,0,3311616,5144576,5065216,7120896,8079872,0,0,0,
154368,112940,1412140,144204,1346636,0,0,0,3312640,5145600,5066240,7121920,6140672,0,0,0,3410944,5146624,5067264,7394304,6141696,0,0,0,3411968,5147648,5068288,7395328,6142720,0,0,0,3412992,5148672,5069312,7396352,6143744,0,0,0,
154496,113068,1412268,144332,1346764,0,0,0,3414016,5149696,5070336,7397376,6144768,0,0,0,3415040,5286912,5071360,7398400,6417152,0,0,0,3513344,5287936,5208576,7670784,6418176,0,0,0,3514368,5288960,5209600,7671808,6419200,0,0,0,
154624,113196,1412396,144460,1346892,0,0,0,3515392,5289984,5210624,7672832,6420224,0,0,0,3516416,5291008,5211648,7673856,6421248,0,0,0,3517440,5292032,5212672,7674880,6693632,0,0,0,3615744,5293056,5213696,7947264,6694656,0,0,0,
154752,113324,1412524,144588,1347020,0,0,0,3616768,5430272,5214720,7948288,6695680,0,0,0,3617792,5431296,5351936,7949312,6696704,0,0,0,3618816,5432320,5352960,7950336,6697728,0,0,0,3619840,5433344,5353984,7951360,6970112,0,0,0,
163840,113452,1412652,148556,1347148,0,0,0,2899200,5434368,5355008,6012160,6971136,0,0,0,2900224,5435392,5356032,6013184,6972160,0,0,0,2901248,5573632,5357056,6014208,6973184,0,0,0,2902272,5574656,5358080,6015232,6974208,0,0,0,
163968,113580,1412780,148684,1347276,0,0,0,2903296,5575680,5495296,6016256,7246592,0,0,0,3001600,5576704,5496320,6288640,7247616,0,0,0,3002624,5577728,5497344,6289664,7248640,0,0,0,3003648,5578752,5498368,6290688,7249664,0,0,0,
164096,113708,1412908,148812,1347404,0,0,0,3004672,5716992,5499392,6291712,7250688,0,0,0,3005696,5718016,5500416,6292736,7523072,0,0,0,3104000,5719040,5638656,6565120,7524096,0,0,0,3105024,5720064,5639680,6566144,7525120,0,0,0,
164224,113836,1413036,148940,1347532,0,0,0,3106048,5721088,5640704,6567168,7526144,0,0,0,3107072,5722112,5641728,6568192,7527168,0,0,0,3108096,5860352,5642752,6569216,7799552,0,0,0,3206400,5861376,5643776,6841600,7800576,0,0,0,
164352,113964,1413164,149068,1347660,0,0,0,3207424,5862400,5782016,6842624,7801600,0,0,0,3208448,5863424,5783040,6843648,7802624,0,0,0,3209472,5864448,5784064,6844672,7803648,0,0,0,3210496,5865472,5785088,6845696,8076032,0,0,0,
164480,124460,1413292,149196,1347788,0,0,0,3308800,3710208,5786112,7118080,8077056,0,0,0,3309824,3711232,5787136,7119104,8078080,0,0,0,3310848,3712256,5925376,7120128,8079104,0,0,0,3311872,3713280,5926400,7121152,8080128,0,0,0,
164608,124588,1413420,149324,1372236,0,0,0,3312896,3714304,5927424,7122176,6145024,0,0,0,3411200,3715328,5928448,7394560,6146048,0,0,0,3412224,3716352,5929472,7395584,6147072,0,0,0,3413248,3853568,5930496,7396608,6148096,0,0,0,
164736,124716,1423916,149452,1372364,0,0,0,3414272,3854592,3775232,7397632,6149120,0,0,0,3415296,3855616,3776256,7398656,6421504,0,0,0,3513600,3856640,3777280,7671040,6422528,0,0,0,3514624,3857664,3778304,7672064,6423552,0,0,0,
164864,124844,1424044,149580,1372492,0,0,0,3515648,3858688,3779328,7673088,6424576,0,0,0,3516672,3859712,3780352,7674112,6425600,0,0,0,3517696,3996928,3781376,7675136,6697984,0,0,0,3616000,3997952,3918592,7947520,6699008,0,0,0,
164992,124972,1424172,149708,1372620,0,0,0,3617024,3998976,3919616,7948544,6700032,0,0,0,3618048,4000000,3920640,7949568,6701056,0,0,0,3619072,4001024,3921664,7950592,6702080,0,0,0,3620096,4002048,3922688,7951616,6974464,0,0,0,
179200,125100,1424300,153676,1372748,0,0,0,2899456,4003072,3923712,6012416,6975488,0,0,0,2900480,4140288,3924736,6013440,6976512,0,0,0,2901504,4141312,4061952,6014464,6977536,0,0,0,2902528,4142336,4062976,6015488,6978560,0,0,0,
179328,125228,1424428,153804,1372876,0,0,0,2903552,4143360,4064000,6016512,7250944,0,0,0,3001856,4144384,4065024,6288896,7251968,0,0,0,3002880,4145408,4066048,6289920,7252992,0,0,0,3003904,4146432,4067072,6290944,7254016,0,0,0,
179456,125356,1424556,153932,1373004,0,0,0,3004928,4283648,4068096,6291968,7255040,0,0,0,3005952,4284672,4205312,6292992,7527424,0,0,0,3104256,4285696,4206336,6565376,7528448,0,0,0,3105280,4286720,4207360,6566400,7529472,0,0,0,
179584,125484,1424684,154060,1373132,0,0,0,3106304,4287744,4208384,6567424,7530496,0,0,0,3107328,4288768,4209408,6568448,7531520,0,0,0,3108352,4289792,4210432,6569472,7803904,0,0,0,3206656,4427008,4211456,6841856,7804928,0,0,0,
179712,125612,1424812,154188,1373260,0,0,0,3207680,4428032,4348672,6842880,7805952,0,0,0,3208704,4429056,4349696,6843904,7806976,0,0,0,3209728,4430080,4350720,6844928,7808000,0,0,0,3210752,4431104,4351744,6845952,8080384,0,0,0,
179840,125740,1424940,154316,1373388,0,0,0,3309056,4432128,4352768,7118336,8081408,0,0,0,3310080,4433152,4353792,7119360,8082432,0,0,0,3311104,4570368,4354816,7120384,8083456,0,0,0,3312128,4571392,4492032,7121408,8084480,0,0,0,
179968,125868,1425068,154444,1377356,0,0,0,3313152,4572416,4493056,7122432,6145280,0,0,0,3411456,4573440,4494080,7394816,6146304,0,0,0,3412480,4574464,4495104,7395840,6147328,0,0,0,3413504,4575488,4496128,7396864,6148352,0,0,0,
180096,125996,1425196,154572,1377484,0,0,0,3414528,4576512,4497152,7397888,6149376,0,0,0,3415552,4713728,4498176,7398912,6421760,0,0,0,3513856,4714752,4635392,7671296,6422784,0,0,0,3514880,4715776,4636416,7672320,6423808,0,0,0,
180224,126124,1425324,154700,1377612,0,0,0,3515904,4716800,4637440,7673344,6424832,0,0,0,3516928,4717824,4638464,7674368,6425856,0,0,0,3517952,4718848,4639488,7675392,6698240,0,0,0,3616256,4719872,4640512,7947776,6699264,0,0,0,
180352,126252,1425452,154828,1377740,0,0,0,3617280,4857088,4641536,7948800,6700288,0,0,0,3618304,4858112,4778752,7949824,6701312,0,0,0,3619328,4859136,4779776,7950848,6702336,0,0,0,3620352,4860160,4780800,7951872,6974720,0,0,0,
189440,126380,1425580,158796,1377868,0,0,0,2899712,4861184,4781824,6012672,6975744,0,0,0,2900736,4862208,4782848,6013696,6976768,0,0,0,2901760,4863232,4783872,6014720,6977792,0,0,0,2902784,5000448,4784896,6015744,6978816,0,0,0,
189568,126508,1425708,158924,1377996,0,0,0,2903808,5001472,4922112,6016768,7251200,0,0,0,3002112,5002496,4923136,6289152,7252224,0,0,0,3003136,5003520,4924160,6290176,7253248,0,0,0,3004160,5004544,4925184,6291200,7254272,0,0,0,
189696,126636,1425836,159052,1378124,0,0,0,3005184,5005568,4926208,6292224,7255296,0,0,0,3006208,5006592,4927232,6293248,7527680,0,0,0,3104512,5143808,4928256,6565632,7528704,0,0,0,3105536,5144832,5065472,6566656,7529728,0,0,0,
189824,126764,1425964,159180,1378252,0,0,0,3106560,5145856,5066496,6567680,7530752,0,0,0,3107584,5146880,5067520,6568704,7531776,0,0,0,3108608,5147904,5068544,6569728,7804160,0,0,0,3206912,5148928,5069568,6842112,7805184,0,0,0,
189952,126892,1426092,159308,1378380,0,0,0,3207936,5149952,5070592,6843136,7806208,0,0,0,3208960,5287168,5071616,6844160,7807232,0,0,0,3209984,5288192,5208832,6845184,7808256,0,0,0,3211008,5289216,5209856,6846208,8080640,0,0,0,
190080,127020,1426220,159436,1378508,0,0,0,3309312,5290240,5210880,7118592,8081664,0,0,0,3310336,5291264,5211904,7119616,8082688,0,0,0,3311360,5292288,5212928,7120640,8083712,0,0,0,3312384,5293312,5213952,7121664,8084736,0,0,0,
190208,127148,1426348,159564,1382476,0,0,0,3313408,5430528,5214976,7122688,6145536,0,0,0,3411712,5431552,5352192,7395072,6146560,0,0,0,3412736,5432576,5353216,7396096,6147584,0,0,0,3413760,5433600,5354240,7397120,6148608,0,0,0,
190336,127276,1426476,159692,1382604,0,0,0,3414784,5434624,5355264,7398144,6149632,0,0,0,3415808,5435648,5356288,7399168,6422016,0,0,0,3514112,5573888,5357312,7671552,6423040,0,0,0,3515136,5574912,5358336,7672576,6424064,0,0,0,
190464,127404,1426604,159820,1382732,0,0,0,3516160,5575936,5495552,7673600,6425088,0,0,0,3517184,5576960,5496576,7674624,6426112,0,0,0,3518208,5577984,5497600,7675648,6698496,0,0,0,3616512,5579008,5498624,7948032,6699520,0,0,0,
190592,127532,1426732,159948,1382860,0,0,0,3617536,5717248,5499648,7949056,6700544,0,0,0,3618560,5718272,5500672,7950080,6701568,0,0,0,3619584,5719296,5638912,7951104,6702592,0,0,0,3620608,5720320,5639936,7952128,6974976,0,0,0,
204800,127660,1426860,184396,1382988,0,0,0,2904064,5721344,5640960,6017024,6976000,0,0,0,2905088,5722368,5641984,6018048,6977024,0,0,0,2906112,5860608,5643008,6019072,6978048,0,0,0,2907136,5861632,5644032,6020096,6979072,0,0,0,
204928,127788,1426988,184524,1383116,0,0,0,2908160,5862656,5782272,6021120,7251456,0,0,0,3006464,5863680,5783296,6293504,7252480,0,0,0,3007488,5864704,5784320,6294528,7253504,0,0,0,3008512,5865728,5785344,6295552,7254528,0,0,0,
205056,138284,1427116,184652,1383244,0,0,0,3009536,3710464,5786368,6296576,7255552,0,0,0,3010560,3711488,5787392,6297600,7527936,0,0,0,3108864,3712512,5925632,6569984,7528960,0,0,0,3109888,3713536,5926656,6571008,7529984,0,0,0,
205184,138412,1427244,184780,1383372,0,0,0,3110912,3714560,5927680,6572032,7531008,0,0,0,3111936,3715584,5928704,6573056,7532032,0,0,0,3112960,3716608,5929728,6574080,7804416,0,0,0,3211264,3853824,5930752,6846464,7805440,0,0,0,
205312,138540,1493036,184908,1383500,0,0,0,3212288,3854848,3781632,6847488,7806464,0,0,0,3213312,3855872,3782656,6848512,7807488,0,0,0,3214336,3856896,3783680,6849536,7808512,0,0,0,3215360,3857920,3784704,6850560,8080896,0,0,0,
205440,138668,1493164,185036,1383628,0,0,0,3313664,3858944,3785728,7122944,8081920,0,0,0,3314688,3859968,3786752,7123968,8082944,0,0,0,3315712,3997184,3787776,7124992,8083968,0,0,0,3316736,3998208,3924992,7126016,8084992,0,0,0,
205568,138796,1493292,185164,1387596,0,0,0,3317760,3999232,3926016,7127040,6145792,0,0,0,3416064,4000256,3927040,7399424,6146816,0,0,0,3417088,4001280,3928064,7400448,6147840,0,0,0,3418112,4002304,3929088,7401472,6148864,0,0,0,
205696,138924,1493420,185292,1387724,0,0,0,3419136,4003328,3930112,7402496,6149888,0,0,0,3420160,4140544,3931136,7403520,6422272,0,0,0,3518464,4141568,4068352,7675904,6423296,0,0,0,3519488,4142592,4069376,7676928,6424320,0,0,0,
205824,139052,1493548,185420,1387852,0,0,0,3520512,4143616,4070400,7677952,6425344,0,0,0,3521536,4144640,4071424,7678976,6426368,0,0,0,3522560,4145664,4072448,7680000,6698752,0,0,0,3620864,4146688,4073472,7952384,6699776,0,0,0,
205952,139180,1493676,185548,1387980,0,0,0,3621888,4283904,4074496,7953408,6700800,0,0,0,3622912,4284928,4211712,7954432,6701824,0,0,0,3623936,4285952,4212736,7955456,6702848,0,0,0,3624960,4286976,4213760,7956480,6975232,0,0,0,
215040,139308,1493804,189516,1388108,0,0,0,2904320,4288000,4214784,6017280,6976256,0,0,0,2905344,4289024,4215808,6018304,6977280,0,0,0,2906368,4290048,4216832,6019328,6978304,0,0,0,2907392,4427264,4217856,6020352,6979328,0,0,0,
215168,139436,1493932,189644,1388236,0,0,0,2908416,4428288,4355072,6021376,7251712,0,0,0,3006720,4429312,4356096,6293760,7252736,0,0,0,3007744,4430336,4357120,6294784,7253760,0,0,0,3008768,4431360,4358144,6295808,7254784,0,0,0,
215296,139564,1494060,189772,1388364,0,0,0,3009792,4432384,4359168,6296832,7255808,0,0,0,3010816,4433408,4360192,6297856,7528192,0,0,0,3109120,4570624,4361216,6570240,7529216,0,0,0,3110144,4571648,4498432,6571264,7530240,0,0,0,
215424,139692,1494188,189900,1388492,0,0,0,3111168,4572672,4499456,6572288,7531264,0,0,0,3112192,4573696,4500480,6573312,7532288,0,0,0,3113216,4574720,4501504,6574336,7804672,0,0,0,3211520,4575744,4502528,6846720,7805696,0,0,0,
215552,139820,1494316,190028,1388620,0,0,0,3212544,4576768,4503552,6847744,7806720,0,0,0,3213568,4713984,4504576,6848768,7807744,0,0,0,3214592,4715008,4641792,6849792,7808768,0,0,0,3215616,4716032,4642816,6850816,8081152,0,0,0,
215680,139948,1494444,190156,1388748,0,0,0,3313920,4717056,4643840,7123200,8082176,0,0,0,3314944,4718080,4644864,7124224,8083200,0,0,0,3315968,4719104,4645888,7125248,8084224,0,0,0,3316992,4720128,4646912,7126272,8085248,0,0,0,
215808,140076,1494572,190284,1433676,0,0,0,3318016,4857344,4647936,7127296,6150144,0,0,0,3416320,4858368,4785152,7399680,6151168,0,0,0,3417344,4859392,4786176,7400704,6152192,0,0,0,3418368,4860416,4787200,7401728,6153216,0,0,0,
215936,140204,1494700,190412,1433804,0,0,0,3419392,4861440,4788224,7402752,6154240,0,0,0,3420416,4862464,4789248,7403776,6426624,0,0,0,3518720,4863488,4790272,7676160,6427648,0,0,0,3519744,5000704,4791296,7677184,6428672,0,0,0,
216064,140332,1494828,190540,1433932,0,0,0,3520768,5001728,4928512,7678208,6429696,0,0,0,3521792,5002752,4929536,7679232,6430720,0,0,0,3522816,5003776,4930560,7680256,6703104,0,0,0,3621120,5004800,4931584,7952640,6704128,0,0,0,
216192,140460,1494956,190668,1434060,0,0,0,3622144,5005824,4932608,7953664,6705152,0,0,0,3623168,5006848,4933632,7954688,6706176,0,0,0,3624192,5144064,4934656,7955712,6707200,0,0,0,3625216,5145088,5071872,7956736,6979584,0,0,0,
230400,140588,1495084,194636,1434188,0,0,0,2904576,5146112,5072896,6017536,6980608,0,0,0,2905600,5147136,5073920,6018560,6981632,0,0,0,2906624,5148160,5074944,6019584,6982656,0,0,0,2907648,5149184,5075968,6020608,6983680,0,0,0,
230528,140716,1495212,194764,1434316,0,0,0,2908672,5150208,5076992,6021632,7256064,0,0,0,3006976,5287424,5078016,6294016,7257088,0,0,0,3008000,5288448,5215232,6295040,7258112,0,0,0,3009024,5289472,5216256,6296064,7259136,0,0,0,
230656,140844,1495340,194892,1434444,0,0,0,3010048,5290496,5217280,6297088,7260160,0,0,0,3011072,5291520,5218304,6298112,7532544,0,0,0,3109376,5292544,5219328,6570496,7533568,0,0,0,3110400,5293568,5220352,6571520,7534592,0,0,0,
230784,140972,1495468,195020,1434572,0,0,0,3111424,5430784,5221376,6572544,7535616,0,0,0,3112448,5431808,5358592,6573568,7536640,0,0,0,3113472,5432832,5359616,6574592,7809024,0,0,0,3211776,5433856,5360640,6846976,7810048,0,0,0,
230912,141100,1495596,195148,1434700,0,0,0,3212800,5434880,5361664,6848000,7811072,0,0,0,3213824,5435904,5362688,6849024,7812096,0,0,0,3214848,5574144,5363712,6850048,7813120,0,0,0,3215872,5575168,5364736,6851072,8085504,0,0,0,
231040,141228,1495724,195276,1434828,0,0,0,3314176,5576192,5501952,7123456,8086528,0,0,0,3315200,5577216,5502976,7124480,8087552,0,0,0,3316224,5578240,5504000,7125504,8088576,0,0,0,3317248,5579264,5505024,7126528,8089600,0,0,0,
231168,141356,1495852,195404,1438796,0,0,0,3318272,5717504,5506048,7127552,6150400,0,0,0,3416576,5718528,5507072,7399936,6151424,0,0,0,3417600,5719552,5645312,7400960,6152448,0,0,0,3418624,5720576,5646336,7401984,6153472,0,0,0,
231296,141484,1495980,195532,1438924,0,0,0,3419648,5721600,5647360,7403008,6154496,0,0,0,3420672,5722624,5648384,7404032,6426880,0,0,0,3518976,5860864,5649408,7676416,6427904,0,0,0,3520000,5861888,5650432,7677440,6428928,0,0,0,
231424,141612,1496108,195660,1439052,0,0,0,3521024,5862912,5788672,7678464,6429952,0,0,0,3522048,5863936,5789696,7679488,6430976,0,0,0,3523072,5864960,5790720,7680512,6703360,0,0,0,3621376,5865984,5791744,7952896,6704384,0,0,0,
231552,152108,1496236,195788,1439180,0,0,0,3622400,3710720,5792768,7953920,6705408,0,0,0,3623424,3711744,5793792,7954944,6706432,0,0,0,3624448,3712768,5932032,7955968,6707456,0,0,0,3625472,3713792,5933056,7956992,6979840,0,0,0,
240640,152236,1496364,199756,1439308,0,0,0,2904832,3714816,5934080,6017792,6980864,0,0,0,2905856,3715840,5935104,6018816,6981888,0,0,0,2906880,3716864,5936128,6019840,6982912,0,0,0,2907904,3854080,5937152,6020864,6983936,0,0,0,
240768,152364,1506860,199884,1439436,0,0,0,2908928,3855104,3781888,6021888,7256320,0,0,0,3007232,3856128,3782912,6294272,7257344,0,0,0,3008256,3857152,3783936,6295296,7258368,0,0,0,3009280,3858176,3784960,6296320,7259392,0,0,0,
240896,152492,1506988,200012,1439564,0,0,0,3010304,3859200,3785984,6297344,7260416,0,0,0,3011328,3860224,3787008,6298368,7532800,0,0,0,3109632,3997440,3788032,6570752,7533824,0,0,0,3110656,3998464,3925248,6571776,7534848,0,0,0,
241024,152620,1507116,200140,1439692,0,0,0,3111680,3999488,3926272,6572800,7535872,0,0,0,3112704,4000512,3927296,6573824,7536896,0,0,0,3113728,4001536,3928320,6574848,7809280,0,0,0,3212032,4002560,3929344,6847232,7810304,0,0,0,
241152,152748,1507244,200268,1439820,0,0,0,3213056,4003584,3930368,6848256,7811328,0,0,0,3214080,4140800,3931392,6849280,7812352,0,0,0,3215104,4141824,4068608,6850304,7813376,0,0,0,3216128,4142848,4069632,6851328,8085760,0,0,0,
241280,152876,1507372,200396,1439948,0,0,0,3314432,4143872,4070656,7123712,8086784,0,0,0,3315456,4144896,4071680,7124736,8087808,0,0,0,3316480,4145920,4072704,7125760,8088832,0,0,0,3317504,4146944,4073728,7126784,8089856,0,0,0,
241408,153004,1507500,200524,1443916,0,0,0,3318528,4284160,4074752,7127808,6150656,0,0,0,3416832,4285184,4211968,7400192,6151680,0,0,0,3417856,4286208,4212992,7401216,6152704,0,0,0,3418880,4287232,4214016,7402240,6153728,0,0,0,
241536,153132,1507628,200652,1444044,0,0,0,3419904,4288256,4215040,7403264,6154752,0,0,0,3420928,4289280,4216064,7404288,6427136,0,0,0,3519232,4290304,4217088,7676672,6428160,0,0,0,3520256,4427520,4218112,7677696,6429184,0,0,0,
241664,153260,1507756,200780,1444172,0,0,0,3521280,4428544,4355328,7678720,6430208,0,0,0,3522304,4429568,4356352,7679744,6431232,0,0,0,3523328,4430592,4357376,7680768,6703616,0,0,0,3621632,4431616,4358400,7953152,6704640,0,0,0,
241792,153388,1507884,200908,1444300,0,0,0,3622656,4432640,4359424,7954176,6705664,0,0,0,3623680,4433664,4360448,7955200,6706688,0,0,0,3624704,4570880,4361472,7956224,6707712,0,0,0,3625728,4571904,4498688,7957248,6980096,0,0,0,
256000,153516,1508012,225356,1444428,0,0,0,2909184,4572928,4499712,6022144,6981120,0,0,0,2910208,4573952,4500736,6023168,6982144,0,0,0,2911232,4574976,4501760,6024192,6983168,0,0,0,2912256,4576000,4502784,6025216,6984192,0,0,0,
256128,153644,1508140,225484,1444556,0,0,0,2913280,4577024,4503808,6026240,7256576,0,0,0,3011584,4714240,4504832,6298624,7257600,0,0,0,3012608,4715264,4642048,6299648,7258624,0,0,0,3013632,4716288,4643072,6300672,7259648,0,0,0,
256256,153772,1508268,225612,1444684,0,0,0,3014656,4717312,4644096,6301696,7260672,0,0,0,3015680,4718336,4645120,6302720,7533056,0,0,0,3113984,4719360,4646144,6575104,7534080,0,0,0,3115008,4720384,4647168,6576128,7535104,0,0,0,
256384,153900,1508396,225740,1444812,0,0,0,3116032,4857600,4648192,6577152,7536128,0,0,0,3117056,4858624,4785408,6578176,7537152,0,0,0,3118080,4859648,4786432,6579200,7809536,0,0,0,3216384,4860672,4787456,6851584,7810560,0,0,0,
256512,154028,1508524,225868,1444940,0,0,0,3217408,4861696,4788480,6852608,7811584,0,0,0,3218432,4862720,4789504,6853632,7812608,0,0,0,3219456,4863744,4790528,6854656,7813632,0,0,0,3220480,5000960,4791552,6855680,8086016,0,0,0,
256640,154156,1508652,225996,1445068,0,0,0,3318784,5001984,4928768,7128064,8087040,0,0,0,3319808,5003008,4929792,7129088,8088064,0,0,0,3320832,5004032,4930816,7130112,8089088,0,0,0,3321856,5005056,4931840,7131136,8090112,0,0,0,
256768,154284,1508780,226124,1449036,0,0,0,3322880,5006080,4932864,7132160,6150912,0,0,0,3421184,5007104,4933888,7404544,6151936,0,0,0,3422208,5144320,4934912,7405568,6152960,0,0,0,3423232,5145344,5072128,7406592,6153984,0,0,0,
256896,154412,1508908,226252,1449164,0,0,0,3424256,5146368,5073152,7407616,6155008,0,0,0,3425280,5147392,5074176,7408640,6427392,0,0,0,3523584,5148416,5075200,7681024,6428416,0,0,0,3524608,5149440,5076224,7682048,6429440,0,0,0,
257024,154540,1509036,226380,1449292,0,0,0,3525632,5150464,5077248,7683072,6430464,0,0,0,3526656,5287680,5078272,7684096,6431488,0,0,0,3527680,5288704,5215488,7685120,6703872,0,0,0,3625984,5289728,5216512,7957504,6704896,0,0,0,
257152,154668,1509164,226508,1449420,0,0,0,3627008,5290752,5217536,7958528,6705920,0,0,0,3628032,5291776,5218560,7959552,6706944,0,0,0,3629056,5292800,5219584,7960576,6707968,0,0,0,3630080,5293824,5220608,7961600,6980352,0,0,0,
266240,154796,1509292,230476,1449548,0,0,0,2909440,5431040,5221632,6022400,6981376,0,0,0,2910464,5432064,5358848,6023424,6982400,0,0,0,2911488,5433088,5359872,6024448,6983424,0,0,0,2912512,5434112,5360896,6025472,6984448,0,0,0,
266368,154924,1509420,230604,1449676,0,0,0,2913536,5435136,5361920,6026496,7256832,0,0,0,3011840,5436160,5362944,6298880,7257856,0,0,0,3012864,5574400,5363968,6299904,7258880,0,0,0,3013888,5575424,5364992,6300928,7259904,0,0,0,
266496,155052,1509548,230732,1449804,0,0,0,3014912,5576448,5502208,6301952,7260928,0,0,0,3015936,5577472,5503232,6302976,7533312,0,0,0,3114240,5578496,5504256,6575360,7534336,0,0,0,3115264,5579520,5505280,6576384,7535360,0,0,0,
266624,155180,1509676,230860,1449932,0,0,0,3116288,5717760,5506304,6577408,7536384,0,0,0,3117312,5718784,5507328,6578432,7537408,0,0,0,3118336,5719808,5645568,6579456,7809792,0,0,0,3216640,5720832,5646592,6851840,7810816,0,0,0,
266752,155308,1509804,230988,1450060,0,0,0,3217664,5721856,5647616,6852864,7811840,0,0,0,3218688,5722880,5648640,6853888,7812864,0,0,0,3219712,5861120,5649664,6854912,7813888,0,0,0,3220736,5862144,5650688,6855936,8086272,0,0,0,
266880,155436,1509932,231116,1450188,0,0,0,3319040,5863168,5788928,7128320,8087296,0,0,0,3320064,5864192,5789952,7129344,8088320,0,0,0,3321088,5865216,5790976,7130368,8089344,0,0,0,3322112,5866240,5792000,7131392,8090368,0,0,0,
267008,276524,1510060,231244,1474636,0,0,0,3323136,3717120,5793024,7132416,6155264,0,0,0,3421440,3718144,5794048,7404800,6156288,0,0,0,3422464,3719168,5932288,7405824,6157312,0,0,0,3423488,3720192,5933312,7406848,6158336,0,0,0,
267136,276652,1510188,231372,1474764,0,0,0,3424512,3721216,5934336,7407872,6159360,0,0,0,3425536,3722240,5935360,7408896,6431744,0,0,0,3523840,3723264,5936384,7681280,6432768,0,0,0,3524864,3860480,5937408,7682304,6433792,0,0,0,
267264,276780,1520684,231500,1474892,0,0,0,3525888,3861504,3782144,7683328,6434816,0,0,0,3526912,3862528,3783168,7684352,6435840,0,0,0,3527936,3863552,3784192,7685376,6708224,0,0,0,3626240,3864576,3785216,7957760,6709248,0,0,0,
267392,276908,1520812,231628,1475020,0,0,0,3627264,3865600,3786240,7958784,6710272,0,0,0,3628288,3866624,3787264,7959808,6711296,0,0,0,3629312,4003840,3788288,7960832,6712320,0,0,0,3630336,4004864,3925504,7961856,6984704,0,0,0,
281600,277036,1520940,235596,1475148,0,0,0,2909696,4005888,3926528,6022656,6985728,0,0,0,2910720,4006912,3927552,6023680,6986752,0,0,0,2911744,4007936,3928576,6024704,6987776,0,0,0,2912768,4008960,3929600,6025728,6988800,0,0,0,
281728,277164,1521068,235724,1475276,0,0,0,2913792,4009984,3930624,6026752,7261184,0,0,0,3012096,4147200,3931648,6299136,7262208,0,0,0,3013120,4148224,4068864,6300160,7263232,0,0,0,3014144,4149248,4069888,6301184,7264256,0,0,0,
281856,277292,1521196,235852,1475404,0,0,0,3015168,4150272,4070912,6302208,7265280,0,0,0,3016192,4151296,4071936,6303232,7537664,0,0,0,3114496,4152320,4072960,6575616,7538688,0,0,0,3115520,4153344,4073984,6576640,7539712,0,0,0,
281984,277420,1521324,235980,1475532,0,0,0,3116544,4290560,4075008,6577664,7540736,0,0,0,3117568,4291584,4212224,6578688,7541760,0,0,0,3118592,4292608,4213248,6579712,7814144,0,0,0,3216896,4293632,4214272,6852096,7815168,0,0,0,
282112,277548,1521452,236108,1475660,0,0,0,3217920,4294656,4215296,6853120,7816192,0,0,0,3218944,4295680,4216320,6854144,7817216,0,0,0,3219968,4296704,4217344,6855168,7818240,0,0,0,3220992,4433920,4218368,6856192,8090624,0,0,0,
282240,277676,1521580,236236,1475788,0,0,0,3319296,4434944,4355584,7128576,8091648,0,0,0,3320320,4435968,4356608,7129600,8092672,0,0,0,3321344,4436992,4357632,7130624,8093696,0,0,0,3322368,4438016,4358656,7131648,8094720,0,0,0,
282368,277804,1521708,236364,1479756,0,0,0,3323392,4439040,4359680,7132672,6155520,0,0,0,3421696,4440064,4360704,7405056,6156544,0,0,0,3422720,4577280,4361728,7406080,6157568,0,0,0,3423744,4578304,4498944,7407104,6158592,0,0,0,
282496,277932,1521836,236492,1479884,0,0,0,3424768,4579328,4499968,7408128,6159616,0,0,0,3425792,4580352,4500992,7409152,6432000,0,0,0,3524096,4581376,4502016,7681536,6433024,0,0,0,3525120,4582400,4503040,7682560,6434048,0,0,0,
282624,278060,1521964,236620,1480012,0,0,0,3526144,4583424,4504064,7683584,6435072,0,0,0,3527168,4720640,4505088,7684608,6436096,0,0,0,3528192,4721664,4642304,7685632,6708480,0,0,0,3626496,4722688,4643328,7958016,6709504,0,0,0,
282752,278188,1522092,236748,1480140,0,0,0,3627520,4723712,4644352,7959040,6710528,0,0,0,3628544,4724736,4645376,7960064,6711552,0,0,0,3629568,4725760,4646400,7961088,6712576,0,0,0,3630592,4726784,4647424,7962112,6984960,0,0,0,
291840,278316,1522220,240716,1480268,0,0,0,2909952,4864000,4648448,6022912,6985984,0,0,0,2910976,4865024,4785664,6023936,6987008,0,0,0,2912000,4866048,4786688,6024960,6988032,0,0,0,2913024,4867072,4787712,6025984,6989056,0,0,0,
291968,278444,1522348,240844,1480396,0,0,0,2914048,4868096,4788736,6027008,7261440,0,0,0,3012352,4869120,4789760,6299392,7262464,0,0,0,3013376,4870144,4790784,6300416,7263488,0,0,0,3014400,5007360,4791808,6301440,7264512,0,0,0,
292096,278572,1522476,240972,1480524,0,0,0,3015424,5008384,4929024,6302464,7265536,0,0,0,3016448,5009408,4930048,6303488,7537920,0,0,0,3114752,5010432,4931072,6575872,7538944,0,0,0,3115776,5011456,4932096,6576896,7539968,0,0,0,
292224,278700,1522604,241100,1480652,0,0,0,3116800,5012480,4933120,6577920,7540992,0,0,0,3117824,5013504,4934144,6578944,7542016,0,0,0,3118848,5150720,4935168,6579968,7814400,0,0,0,3217152,5151744,5072384,6852352,7815424,0,0,0,
292352,278828,1522732,241228,1480780,0,0,0,3218176,5152768,5073408,6853376,7816448,0,0,0,3219200,5153792,5074432,6854400,7817472,0,0,0,3220224,5154816,5075456,6855424,7818496,0,0,0,3221248,5155840,5076480,6856448,8090880,0,0,0,
292480,278956,1522860,241356,1480908,0,0,0,3319552,5156864,5077504,7128832,8091904,0,0,0,3320576,5294080,5078528,7129856,8092928,0,0,0,3321600,5295104,5215744,7130880,8093952,0,0,0,3322624,5296128,5216768,7131904,8094976,0,0,0,
292608,279084,1522988,241484,1484876,0,0,0,3323648,5297152,5217792,7132928,6155776,0,0,0,3421952,5298176,5218816,7405312,6156800,0,0,0,3422976,5299200,5219840,7406336,6157824,0,0,0,3424000,5300224,5220864,7407360,6158848,0,0,0,
292736,279212,1523116,241612,1485004,0,0,0,3425024,5437440,5221888,7408384,6159872,0,0,0,3426048,5438464,5359104,7409408,6432256,0,0,0,3524352,5439488,5360128,7681792,6433280,0,0,0,3525376,5440512,5361152,7682816,6434304,0,0,0,
292864,279340,1523244,241740,1485132,0,0,0,3526400,5441536,5362176,7683840,6435328,0,0,0,3527424,5442560,5363200,7684864,6436352,0,0,0,3528448,5580800,5364224,7685888,6708736,0,0,0,3626752,5581824,5365248,7958272,6709760,0,0,0,
292992,279468,1523372,241868,1485260,0,0,0,3627776,5582848,5502464,7959296,6710784,0,0,0,3628800,5583872,5503488,7960320,6711808,0,0,0,3629824,5584896,5504512,7961344,6712832,0,0,0,3630848,5585920,5505536,7962368,6985216,0,0,0,
307200,279596,1523500,286796,1485388,0,0,0,2914304,5724160,5506560,6027264,6986240,0,0,0,2915328,5725184,5507584,6028288,6987264,0,0,0,2916352,5726208,5645824,6029312,6988288,0,0,0,2917376,5727232,5646848,6030336,6989312,0,0,0,
307328,279724,1523628,286924,1485516,0,0,0,2918400,5728256,5647872,6031360,7261696,0,0,0,3016704,5729280,5648896,6303744,7262720,0,0,0,3017728,5867520,5649920,6304768,7263744,0,0,0,3018752,5868544,5650944,6305792,7264768,0,0,0,
307456,279852,1523756,287052,1485644,0,0,0,3019776,5869568,5789184,6306816,7265792,0,0,0,3020800,5870592,5790208,6307840,7538176,0,0,0,3119104,5871616,5791232,6580224,7539200,0,0,0,3120128,5872640,5792256,6581248,7540224,0,0,0,
307584,290348,1523884,287180,1485772,0,0,0,3121152,3717376,5793280,6582272,7541248,0,0,0,3122176,3718400,5794304,6583296,7542272,0,0,0,3123200,3719424,5932544,6584320,7814656,0,0,0,3221504,3720448,5933568,6856704,7815680,0,0,0,
307712,290476,1524012,287308,1485900,0,0,0,3222528,3721472,5934592,6857728,7816704,0,0,0,3223552,3722496,5935616,6858752,7817728,0,0,0,3224576,3723520,5936640,6859776,7818752,0,0,0,3225600,3860736,5937664,6860800,8091136,0,0,0,
307840,290604,1534508,287436,1486028,0,0,0,3323904,3861760,3782400,7133184,8092160,0,0,0,3324928,3862784,3783424,7134208,8093184,0,0,0,3325952,3863808,3784448,7135232,8094208,0,0,0,3326976,3864832,3785472,7136256,8095232,0,0,0,
307968,290732,1534636,287564,1489996,0,0,0,3328000,3865856,3786496,7137280,6156032,0,0,0,3426304,3866880,3787520,7409664,6157056,0,0,0,3427328,4004096,3788544,7410688,6158080,0,0,0,3428352,4005120,3925760,7411712,6159104,0,0,0,
308096,290860,1534764,287692,1490124,0,0,0,3429376,4006144,3926784,7412736,6160128,0,0,0,3430400,4007168,3927808,7413760,6432512,0,0,0,3528704,4008192,3928832,7686144,6433536,0,0,0,3529728,4009216,3929856,7687168,6434560,0,0,0,
308224,290988,1534892,287820,1490252,0,0,0,3530752,4010240,3930880,7688192,6435584,0,0,0,3531776,4147456,3931904,7689216,6436608,0,0,0,3532800,4148480,4069120,7690240,6708992,0,0,0,3631104,4149504,4070144,7962624,6710016,0,0,0,
308352,291116,1535020,287948,1490380,0,0,0,3632128,4150528,4071168,7963648,6711040,0,0,0,3633152,4151552,4072192,7964672,6712064,0,0,0,3634176,4152576,4073216,7965696,6713088,0,0,0,3635200,4153600,4074240,7966720,6985472,0,0,0,
317440,291244,1535148,291916,1490508,0,0,0,2914560,4290816,4075264,6027520,6986496,0,0,0,2915584,4291840,4212480,6028544,6987520,0,0,0,2916608,4292864,4213504,6029568,6988544,0,0,0,2917632,4293888,4214528,6030592,6989568,0,0,0,
317568,291372,1535276,292044,1490636,0,0,0,2918656,4294912,4215552,6031616,7261952,0,0,0,3016960,4295936,4216576,6304000,7262976,0,0,0,3017984,4296960,4217600,6305024,7264000,0,0,0,3019008,4434176,4218624,6306048,7265024,0,0,0,
317696,291500,1535404,292172,1490764,0,0,0,3020032,4435200,4355840,6307072,7266048,0,0,0,3021056,4436224,4356864,6308096,7538432,0,0,0,3119360,4437248,4357888,6580480,7539456,0,0,0,3120384,4438272,4358912,6581504,7540480,0,0,0,
317824,291628,1535532,292300,1490892,0,0,0,3121408,4439296,4359936,6582528,7541504,0,0,0,3122432,4440320,4360960,6583552,7542528,0,0,0,3123456,4577536,4361984,6584576,7814912,0,0,0,3221760,4578560,4499200,6856960,7815936,0,0,0,
317952,291756,1535660,292428,1491020,0,0,0,3222784,4579584,4500224,6857984,7816960,0,0,0,3223808,4580608,4501248,6859008,7817984,0,0,0,3224832,4581632,4502272,6860032,7819008,0,0,0,3225856,4582656,4503296,6861056,8091392,0,0,0,
318080,291884,1535788,292556,1491148,0,0,0,3324160,4583680,4504320,7133440,8092416,0,0,0,3325184,4720896,4505344,7134464,8093440,0,0,0,3326208,4721920,4642560,7135488,8094464,0,0,0,3327232,4722944,4643584,7136512,8095488,0,0,0,
318208,292012,1535916,292684,1515596,0,0,0,3328256,4723968,4644608,7137536,6160384,0,0,0,3426560,4724992,4645632,7409920,6161408,0,0,0,3427584,4726016,4646656,7410944,6162432,0,0,0,3428608,4727040,4647680,7411968,6163456,0,0,0,
318336,292140,1536044,292812,1515724,0,0,0,3429632,4864256,4648704,7412992,6164480,0,0,0,3430656,4865280,4785920,7414016,6436864,0,0,0,3528960,4866304,4786944,7686400,6437888,0,0,0,3529984,4867328,4787968,7687424,6438912,0,0,0,
318464,292268,1536172,292940,1515852,0,0,0,3531008,4868352,4788992,7688448,6439936,0,0,0,3532032,4869376,4790016,7689472,6440960,0,0,0,3533056,4870400,4791040,7690496,6713344,0,0,0,3631360,5007616,4792064,7962880,6714368,0,0,0,
318592,292396,1536300,293068,1515980,0,0,0,3632384,5008640,4929280,7963904,6715392,0,0,0,3633408,5009664,4930304,7964928,6716416,0,0,0,3634432,5010688,4931328,7965952,6717440,0,0,0,3635456,5011712,4932352,7966976,6989824,0,0,0,
332800,292524,1536428,297036,1516108,0,0,0,2914816,5012736,4933376,6027776,6990848,0,0,0,2915840,5013760,4934400,6028800,6991872,0,0,0,2916864,5150976,4935424,6029824,6992896,0,0,0,2917888,5152000,5072640,6030848,6993920,0,0,0,
332928,292652,1536556,297164,1516236,0,0,0,2918912,5153024,5073664,6031872,7266304,0,0,0,3017216,5154048,5074688,6304256,7267328,0,0,0,3018240,5155072,5075712,6305280,7268352,0,0,0,3019264,5156096,5076736,6306304,7269376,0,0,0,
333056,292780,1536684,297292,1516364,0,0,0,3020288,5157120,5077760,6307328,7270400,0,0,0,3021312,5294336,5078784,6308352,7542784,0,0,0,3119616,5295360,5216000,6580736,7543808,0,0,0,3120640,5296384,5217024,6581760,7544832,0,0,0,
333184,292908,1536812,297420,1516492,0,0,0,3121664,5297408,5218048,6582784,7545856,0,0,0,3122688,5298432,5219072,6583808,7546880,0,0,0,3123712,5299456,5220096,6584832,7819264,0,0,0,3222016,5300480,5221120,6857216,7820288,0,0,0,
333312,293036,1536940,297548,1516620,0,0,0,3223040,5437696,5222144,6858240,7821312,0,0,0,3224064,5438720,5359360,6859264,7822336,0,0,0,3225088,5439744,5360384,6860288,7823360,0,0,0,3226112,5440768,5361408,6861312,8095744,0,0,0,
333440,293164,1537068,297676,1516748,0,0,0,3324416,5441792,5362432,7133696,8096768,0,0,0,3325440,5442816,5363456,7134720,8097792,0,0,0,3326464,5581056,5364480,7135744,8098816,0,0,0,3327488,5582080,5365504,7136768,8099840,0,0,0,
333568,293292,1537196,297804,1520716,0,0,0,3328512,5583104,5502720,7137792,6160640,0,0,0,3426816,5584128,5503744,7410176,6161664,0,0,0,3427840,5585152,5504768,7411200,6162688,0,0,0,3428864,5586176,5505792,7412224,6163712,0,0,0,
333696,293420,1537324,297932,1520844,0,0,0,3429888,5724416,5506816,7413248,6164736,0,0,0,3430912,5725440,5507840,7414272,6437120,0,0,0,3529216,5726464,5646080,7686656,6438144,0,0,0,3530240,5727488,5647104,7687680,6439168,0,0,0,
333824,293548,1537452,298060,1520972,0,0,0,3531264,5728512,5648128,7688704,6440192,0,0,0,3532288,5729536,5649152,7689728,6441216,0,0,0,3533312,5867776,5650176,7690752,6713600,0,0,0,3631616,5868800,5651200,7963136,6714624,0,0,0,
333952,293676,1537580,298188,1521100,0,0,0,3632640,5869824,5789440,7964160,6715648,0,0,0,3633664,5870848,5790464,7965184,6716672,0,0,0,3634688,5871872,5791488,7966208,6717696,0,0,0,3635712,5872896,5792512,7967232,6990080,0,0,0,
343040,304172,1537708,302156,1521228,0,0,0,2915072,3717632,5793536,6028032,6991104,0,0,0,2916096,3718656,5794560,6029056,6992128,0,0,0,2917120,3719680,5932800,6030080,6993152,0,0,0,2918144,3720704,5933824,6031104,6994176,0,0,0,
343168,304300,1537836,302284,1521356,0,0,0,2919168,3721728,5934848,6032128,7266560,0,0,0,3017472,3722752,5935872,6304512,7267584,0,0,0,3018496,3723776,5936896,6305536,7268608,0,0,0,3019520,3860992,5937920,6306560,7269632,0,0,0,
343296,304428,1658924,302412,1521484,0,0,0,3020544,3862016,3788800,6307584,7270656,0,0,0,3021568,3863040,3789824,6308608,7543040,0,0,0,3119872,3864064,3790848,6580992,7544064,0,0,0,3120896,3865088,3791872,6582016,7545088,0,0,0,
343424,304556,1659052,302540,1521612,0,0,0,3121920,3866112,3792896,6583040,7546112,0,0,0,3122944,3867136,3793920,6584064,7547136,0,0,0,3123968,4004352,3794944,6585088,7819520,0,0,0,3222272,4005376,3932160,6857472,7820544,0,0,0,
343552,304684,1659180,302668,1521740,0,0,0,3223296,4006400,3933184,6858496,7821568,0,0,0,3224320,4007424,3934208,6859520,7822592,0,0,0,3225344,4008448,3935232,6860544,7823616,0,0,0,3226368,4009472,3936256,6861568,8096000,0,0,0,
343680,304812,1659308,302796,1521868,0,0,0,3324672,4010496,3937280,7133952,8097024,0,0,0,3325696,4147712,3938304,7134976,8098048,0,0,0,3326720,4148736,4075520,7136000,8099072,0,0,0,3327744,4149760,4076544,7137024,8100096,0,0,0,
343808,304940,1659436,302924,1525836,0,0,0,3328768,4150784,4077568,7138048,6160896,0,0,0,3427072,4151808,4078592,7410432,6161920,0,0,0,3428096,4152832,4079616,7411456,6162944,0,0,0,3429120,4153856,4080640,7412480,6163968,0,0,0,
343936,305068,1659564,303052,1525964,0,0,0,3430144,4291072,4081664,7413504,6164992,0,0,0,3431168,4292096,4218880,7414528,6437376,0,0,0,3529472,4293120,4219904,7686912,6438400,0,0,0,3530496,4294144,4220928,7687936,6439424,0,0,0,
344064,305196,1659692,303180,1526092,0,0,0,3531520,4295168,4221952,7688960,6440448,0,0,0,3532544,4296192,4222976,7689984,6441472,0,0,0,3533568,4297216,4224000,7691008,6713856,0,0,0,3631872,4434432,4225024,7963392,6714880,0,0,0,
344192,305324,1659820,303308,1526220,0,0,0,3632896,4435456,4362240,7964416,6715904,0,0,0,3633920,4436480,4363264,7965440,6716928,0,0,0,3634944,4437504,4364288,7966464,6717952,0,0,0,3635968,4438528,4365312,7967488,6990336,0,0,0,
358400,305452,1659948,327756,1526348,0,0,0,2919424,4439552,4366336,6032384,6991360,0,0,0,2920448,4440576,4367360,6033408,6992384,0,0,0,2921472,4577792,4368384,6034432,6993408,0,0,0,2922496,4578816,4505600,6035456,6994432,0,0,0,
358528,305580,1660076,327884,1526476,0,0,0,2923520,4579840,4506624,6036480,7266816,0,0,0,3021824,4580864,4507648,6308864,7267840,0,0,0,3022848,4581888,4508672,6309888,7268864,0,0,0,3023872,4582912,4509696,6310912,7269888,0,0,0,
358656,305708,1660204,328012,1526604,0,0,0,3024896,4583936,4510720,6311936,7270912,0,0,0,3025920,4721152,4511744,6312960,7543296,0,0,0,3124224,4722176,4648960,6585344,7544320,0,0,0,3125248,4723200,4649984,6586368,7545344,0,0,0,
358784,305836,1660332,328140,1526732,0,0,0,3126272,4724224,4651008,6587392,7546368,0,0,0,3127296,4725248,4652032,6588416,7547392,0,0,0,3128320,4726272,4653056,6589440,7819776,0,0,0,3226624,4727296,4654080,6861824,7820800,0,0,0,
358912,305964,1660460,328268,1526860,0,0,0,3227648,4864512,4655104,6862848,7821824,0,0,0,3228672,4865536,4792320,6863872,7822848,0,0,0,3229696,4866560,4793344,6864896,7823872,0,0,0,3230720,4867584,4794368,6865920,8096256,0,0,0,
359040,306092,1660588,328396,1526988,0,0,0,3329024,4868608,4795392,7138304,8097280,0,0,0,3330048,4869632,4796416,7139328,8098304,0,0,0,3331072,4870656,4797440,7140352,8099328,0,0,0,3332096,5007872,4798464,7141376,8100352,0,0,0,
359168,306220,1660716,328524,1530956,0,0,0,3333120,5008896,4935680,7142400,6161152,0,0,0,3431424,5009920,4936704,7414784,6162176,0,0,0,3432448,5010944,4937728,7415808,6163200,0,0,0,3433472,5011968,4938752,7416832,6164224,0,0,0,
359296,306348,1660844,328652,1531084,0,0,0,3434496,5012992,4939776,7417856,6165248,0,0,0,3435520,5014016,4940800,7418880,6437632,0,0,0,3533824,5151232,4941824,7691264,6438656,0,0,0,3534848,5152256,5079040,7692288,6439680,0,0,0,
359424,306476,1660972,328780,1531212,0,0,0,3535872,5153280,5080064,7693312,6440704,0,0,0,3536896,5154304,5081088,7694336,6441728,0,0,0,3537920,5155328,5082112,7695360,6714112,0,0,0,3636224,5156352,5083136,7967744,6715136,0,0,0,
359552,306604,1661100,328908,1531340,0,0,0,3637248,5157376,5084160,7968768,6716160,0,0,0,3638272,5294592,5085184,7969792,6717184,0,0,0,3639296,5295616,5222400,7970816,6718208,0,0,0,3640320,5296640,5223424,7971840,6990592,0,0,0,
368640,306732,1661228,332876,1531468,0,0,0,2919680,5297664,5224448,6032640,6991616,0,0,0,2920704,5298688,5225472,6033664,6992640,0,0,0,2921728,5299712,5226496,6034688,6993664,0,0,0,2922752,5300736,5227520,6035712,6994688,0,0,0,
368768,306860,1661356,333004,1531596,0,0,0,2923776,5437952,5228544,6036736,7267072,0,0,0,3022080,5438976,5365760,6309120,7268096,0,0,0,3023104,5440000,5366784,6310144,7269120,0,0,0,3024128,5441024,5367808,6311168,7270144,0,0,0,
368896,306988,1661484,333132,1531724,0,0,0,3025152,5442048,5368832,6312192,7271168,0,0,0,3026176,5443072,5369856,6313216,7543552,0,0,0,3124480,5581312,5370880,6585600,7544576,0,0,0,3125504,5582336,5371904,6586624,7545600,0,0,0,
369024,307116,1661612,333260,1531852,0,0,0,3126528,5583360,5509120,6587648,7546624,0,0,0,3127552,5584384,5510144,6588672,7547648,0,0,0,3128576,5585408,5511168,6589696,7820032,0,0,0,3226880,5586432,5512192,6862080,7821056,0,0,0,
369152,307244,1661740,333388,1531980,0,0,0,3227904,5724672,5513216,6863104,7822080,0,0,0,3228928,5725696,5514240,6864128,7823104,0,0,0,3229952,5726720,5652480,6865152,7824128,0,0,0,3230976,5727744,5653504,6866176,8096512,0,0,0,
369280,307372,1661868,333516,1532108,0,0,0,3329280,5728768,5654528,7138560,8097536,0,0,0,3330304,5729792,5655552,7139584,8098560,0,0,0,3331328,5868032,5656576,7140608,8099584,0,0,0,3332352,5869056,5657600,7141632,8100608,0,0,0,
369408,307500,1661996,333644,1577036,0,0,0,3333376,5870080,5795840,7142656,6165504,0,0,0,3431680,5871104,5796864,7415040,6166528,0,0,0,3432704,5872128,5797888,7416064,6167552,0,0,0,3433728,5873152,5798912,7417088,6168576,0,0,0,
369536,317996,1662124,333772,1577164,0,0,0,3434752,3717888,5799936,7418112,6169600,0,0,0,3435776,3718912,5800960,7419136,6441984,0,0,0,3534080,3719936,5939200,7691520,6443008,0,0,0,3535104,3720960,5940224,7692544,6444032,0,0,0,
369664,318124,1662252,333900,1577292,0,0,0,3536128,3721984,5941248,7693568,6445056,0,0,0,3537152,3723008,5942272,7694592,6446080,0,0,0,3538176,3724032,5943296,7695616,6718464,0,0,0,3636480,3861248,5944320,7968000,6719488,0,0,0,
369792,318252,1672748,334028,1577420,0,0,0,3637504,3862272,3789056,7969024,6720512,0,0,0,3638528,3863296,3790080,7970048,6721536,0,0,0,3639552,3864320,3791104,7971072,6722560,0,0,0,3640576,3865344,3792128,7972096,6994944,0,0,0,
384000,318380,1672876,337996,1577548,0,0,0,2919936,3866368,3793152,6032896,6995968,0,0,0,2920960,3867392,3794176,6033920,6996992,0,0,0,2921984,4004608,3795200,6034944,6998016,0,0,0,2923008,4005632,3932416,6035968,6999040,0,0,0,
384128,318508,1673004,338124,1577676,0,0,0,2924032,4006656,3933440,6036992,7271424,0,0,0,3022336,4007680,3934464,6309376,7272448,0,0,0,3023360,4008704,3935488,6310400,7273472,0,0,0,3024384,4009728,3936512,6311424,7274496,0,0,0,
384256,318636,1673132,338252,1577804,0,0,0,3025408,4010752,3937536,6312448,7275520,0,0,0,3026432,4147968,3938560,6313472,7547904,0,0,0,3124736,4148992,4075776,6585856,7548928,0,0,0,3125760,4150016,4076800,6586880,7549952,0,0,0,
384384,318764,1673260,338380,1577932,0,0,0,3126784,4151040,4077824,6587904,7550976,0,0,0,3127808,4152064,4078848,6588928,7552000,0,0,0,3128832,4153088,4079872,6589952,7824384,0,0,0,3227136,4154112,4080896,6862336,7825408,0,0,0,
384512,318892,1673388,338508,1578060,0,0,0,3228160,4291328,4081920,6863360,7826432,0,0,0,3229184,4292352,4219136,6864384,7827456,0,0,0,3230208,4293376,4220160,6865408,7828480,0,0,0,3231232,4294400,4221184,6866432,8100864,0,0,0,
384640,319020,1673516,338636,1578188,0,0,0,3329536,4295424,4222208,7138816,8101888,0,0,0,3330560,4296448,4223232,7139840,8102912,0,0,0,3331584,4297472,4224256,7140864,8103936,0,0,0,3332608,4434688,4225280,7141888,8104960,0,0,0,
384768,319148,1673644,338764,1582156,0,0,0,3333632,4435712,4362496,7142912,6165760,0,0,0,3431936,4436736,4363520,7415296,6166784,0,0,0,3432960,4437760,4364544,7416320,6167808,0,0,0,3433984,4438784,4365568,7417344,6168832,0,0,0,
384896,319276,1673772,338892,1582284,0,0,0,3435008,4439808,4366592,7418368,6169856,0,0,0,3436032,4440832,4367616,7419392,6442240,0,0,0,3534336,4578048,4368640,7691776,6443264,0,0,0,3535360,4579072,4505856,7692800,6444288,0,0,0,
385024,319404,1673900,339020,1582412,0,0,0,3536384,4580096,4506880,7693824,6445312,0,0,0,3537408,4581120,4507904,7694848,6446336,0,0,0,3538432,4582144,4508928,7695872,6718720,0,0,0,3636736,4583168,4509952,7968256,6719744,0,0,0,
385152,319532,1674028,339148,1582540,0,0,0,3637760,4584192,4510976,7969280,6720768,0,0,0,3638784,4721408,4512000,7970304,6721792,0,0,0,3639808,4722432,4649216,7971328,6722816,0,0,0,3640832,4723456,4650240,7972352,6995200,0,0,0,
394240,319660,1674156,343116,1582668,0,0,0,2920192,4724480,4651264,6033152,6996224,0,0,0,2921216,4725504,4652288,6034176,6997248,0,0,0,2922240,4726528,4653312,6035200,6998272,0,0,0,2923264,4727552,4654336,6036224,6999296,0,0,0,
394368,319788,1674284,343244,1582796,0,0,0,2924288,4864768,4655360,6037248,7271680,0,0,0,3022592,4865792,4792576,6309632,7272704,0,0,0,3023616,4866816,4793600,6310656,7273728,0,0,0,3024640,4867840,4794624,6311680,7274752,0,0,0,
394496,319916,1674412,343372,1582924,0,0,0,3025664,4868864,4795648,6312704,7275776,0,0,0,3026688,4869888,4796672,6313728,7548160,0,0,0,3124992,4870912,4797696,6586112,7549184,0,0,0,3126016,5008128,4798720,6587136,7550208,0,0,0,
394624,320044,1674540,343500,1583052,0,0,0,3127040,5009152,4935936,6588160,7551232,0,0,0,3128064,5010176,4936960,6589184,7552256,0,0,0,3129088,5011200,4937984,6590208,7824640,0,0,0,3227392,5012224,4939008,6862592,7825664,0,0,0,
394752,320172,1674668,343628,1583180,0,0,0,3228416,5013248,4940032,6863616,7826688,0,0,0,3229440,5014272,4941056,6864640,7827712,0,0,0,3230464,5151488,4942080,6865664,7828736,0,0,0,3231488,5152512,5079296,6866688,8101120,0,0,0,
394880,320300,1674796,343756,1583308,0,0,0,3329792,5153536,5080320,7139072,8102144,0,0,0,3330816,5154560,5081344,7140096,8103168,0,0,0,3331840,5155584,5082368,7141120,8104192,0,0,0,3332864,5156608,5083392,7142144,8105216,0,0,0,
395008,320428,1674924,343884,1587276,0,0,0,3333888,5157632,5084416,7143168,6166016,0,0,0,3432192,5294848,5085440,7415552,6167040,0,0,0,3433216,5295872,5222656,7416576,6168064,0,0,0,3434240,5296896,5223680,7417600,6169088,0,0,0,
395136,320556,1675052,344012,1587404,0,0,0,3435264,5297920,5224704,7418624,6170112,0,0,0,3436288,5298944,5225728,7419648,6442496,0,0,0,3534592,5299968,5226752,7692032,6443520,0,0,0,3535616,5300992,5227776,7693056,6444544,0,0,0,
395264,320684,1675180,344140,1587532,0,0,0,3536640,5438208,5228800,7694080,6445568,0,0,0,3537664,5439232,5366016,7695104,6446592,0,0,0,3538688,5440256,5367040,7696128,6718976,0,0,0,3636992,5441280,5368064,7968512,6720000,0,0,0,
395392,320812,1675308,344268,1587660,0,0,0,3638016,5442304,5369088,7969536,6721024,0,0,0,3639040,5443328,5370112,7970560,6722048,0,0,0,3640064,5581568,5371136,7971584,6723072,0,0,0,3641088,5582592,5372160,7972608,6995456,0,0,0,
409600,320940,1675436,368716,1587788,0,0,0,2924544,5583616,5509376,6037504,6996480,0,0,0,2925568,5584640,5510400,6038528,6997504,0,0,0,2926592,5585664,5511424,6039552,6998528,0,0,0,2927616,5586688,5512448,6040576,6999552,0,0,0,
409728,321068,1675564,368844,1587916,0,0,0,2928640,5724928,5513472,6041600,7271936,0,0,0,3026944,5725952,5514496,6313984,7272960,0,0,0,3027968,5726976,5652736,6315008,7273984,0,0,0,3028992,5728000,5653760,6316032,7275008,0,0,0,
409856,321196,1675692,368972,1588044,0,0,0,3030016,5729024,5654784,6317056,7276032,0,0,0,3031040,5730048,5655808,6318080,7548416,0,0,0,3129344,5868288,5656832,6590464,7549440,0,0,0,3130368,5869312,5657856,6591488,7550464,0,0,0,
409984,321324,1675820,369100,1588172,0,0,0,3131392,5870336,5796096,6592512,7551488,0,0,0,3132416,5871360,5797120,6593536,7552512,0,0,0,3133440,5872384,5798144,6594560,7824896,0,0,0,3231744,5873408,5799168,6866944,7825920,0,0,0,
410112,387116,1675948,369228,1588300,0,0,0,3232768,3724288,5800192,6867968,7826944,0,0,0,3233792,3725312,5801216,6868992,7827968,0,0,0,3234816,3726336,5939456,6870016,7828992,0,0,0,3235840,3727360,5940480,6871040,8101376,0,0,0,
410240,387244,1676076,369356,1588428,0,0,0,3334144,3728384,5941504,7143424,8102400,0,0,0,3335168,3729408,5942528,7144448,8103424,0,0,0,3336192,3730432,5943552,7145472,8104448,0,0,0,3337216,3867648,5944576,7146496,8105472,0,0,0,
410368,387372,1686572,369484,1592396,0,0,0,3338240,3868672,3789312,7147520,6166272,0,0,0,3436544,3869696,3790336,7419904,6167296,0,0,0,3437568,3870720,3791360,7420928,6168320,0,0,0,3438592,3871744,3792384,7421952,6169344,0,0,0,
410496,387500,1686700,369612,1592524,0,0,0,3439616,3872768,3793408,7422976,6170368,0,0,0,3440640,3873792,3794432,7424000,6442752,0,0,0,3538944,4011008,3795456,7696384,6443776,0,0,0,3539968,4012032,3932672,7697408,6444800,0,0,0,
410624,387628,1686828,369740,1592652,0,0,0,3540992,4013056,3933696,7698432,6445824,0,0,0,3542016,4014080,3934720,7699456,6446848,0,0,0,3543040,4015104,3935744,7700480,6719232,0,0,0,3641344,4016128,3936768,7972864,6720256,0,0,0,
410752,387756,1686956,369868,1592780,0,0,0,3642368,4017152,3937792,7973888,6721280,0,0,0,3643392,4154368,3938816,7974912,6722304,0,0,0,3644416,4155392,4076032,7975936,6723328,0,0,0,3645440,4156416,4077056,7976960,6995712,0,0,0,
419840,387884,1687084,373836,1592908,0,0,0,2924800,4157440,4078080,6037760,6996736,0,0,0,2925824,4158464,4079104,6038784,6997760,0,0,0,2926848,4159488,4080128,6039808,6998784,0,0,0,2927872,4160512,4081152,6040832,6999808,0,0,0,
419968,388012,1687212,373964,1593036,0,0,0,2928896,4297728,4082176,6041856,7272192,0,0,0,3027200,4298752,4219392,6314240,7273216,0,0,0,3028224,4299776,4220416,6315264,7274240,0,0,0,3029248,4300800,4221440,6316288,7275264,0,0,0,
420096,388140,1687340,374092,1593164,0,0,0,3030272,4301824,4222464,6317312,7276288,0,0,0,3031296,4302848,4223488,6318336,7548672,0,0,0,3129600,4303872,4224512,6590720,7549696,0,0,0,3130624,4441088,4225536,6591744,7550720,0,0,0,
420224,388268,1687468,374220,1593292,0,0,0,3131648,4442112,4362752,6592768,7551744,0,0,0,3132672,4443136,4363776,6593792,7552768,0,0,0,3133696,4444160,4364800,6594816,7825152,0,0,0,3232000,4445184,4365824,6867200,7826176,0,0,0,
420352,388396,1687596,374348,1593420,0,0,0,3233024,4446208,4366848,6868224,7827200,0,0,0,3234048,4447232,4367872,6869248,7828224,0,0,0,3235072,4584448,4368896,6870272,7829248,0,0,0,3236096,4585472,4506112,6871296,8101632,0,0,0,
420480,388524,1687724,374476,1593548,0,0,0,3334400,4586496,4507136,7143680,8102656,0,0,0,3335424,4587520,4508160,7144704,8103680,0,0,0,3336448,4588544,4509184,7145728,8104704,0,0,0,3337472,4589568,4510208,7146752,8105728,0,0,0,
420608,388652,1687852,374604,1617996,0,0,0,3338496,4590592,4511232,7147776,6170624,0,0,0,3436800,4727808,4512256,7420160,6171648,0,0,0,3437824,4728832,4649472,7421184,6172672,0,0,0,3438848,4729856,4650496,7422208,6173696,0,0,0,
420736,388780,1687980,374732,1618124,0,0,0,3439872,4730880,4651520,7423232,6174720,0,0,0,3440896,4731904,4652544,7424256,6447104,0,0,0,3539200,4732928,4653568,7696640,6448128,0,0,0,3540224,4733952,4654592,7697664,6449152,0,0,0,
420864,388908,1688108,374860,1618252,0,0,0,3541248,4871168,4655616,7698688,6450176,0,0,0,3542272,4872192,4792832,7699712,6451200,0,0,0,3543296,4873216,4793856,7700736,6723584,0,0,0,3641600,4874240,4794880,7973120,6724608,0,0,0,
420992,389036,1688236,374988,1618380,0,0,0,3642624,4875264,4795904,7974144,6725632,0,0,0,3643648,4876288,4796928,7975168,6726656,0,0,0,3644672,4877312,4797952,7976192,6727680,0,0,0,3645696,5014528,4798976,7977216,7000064,0,0,0,
435200,389164,1688364,378956,1618508,0,0,0,2925056,5015552,4936192,6038016,7001088,0,0,0,2926080,5016576,4937216,6039040,7002112,0,0,0,2927104,5017600,4938240,6040064,7003136,0,0,0,2928128,5018624,4939264,6041088,7004160,0,0,0,
435328,389292,1688492,379084,1618636,0,0,0,2929152,5019648,4940288,6042112,7276544,0,0,0,3027456,5020672,4941312,6314496,7277568,0,0,0,3028480,5157888,4942336,6315520,7278592,0,0,0,3029504,5158912,5079552,6316544,7279616,0,0,0,
435456,389420,1688620,379212,1618764,0,0,0,3030528,5159936,5080576,6317568,7280640,0,0,0,3031552,5160960,5081600,6318592,7553024,0,0,0,3129856,5161984,5082624,6590976,7554048,0,0,0,3130880,5163008,5083648,6592000,7555072,0,0,0,
435584,389548,1688748,379340,1618892,0,0,0,3131904,5164032,5084672,6593024,7556096,0,0,0,3132928,5301248,5085696,6594048,7557120,0,0,0,3133952,5302272,5222912,6595072,7829504,0,0,0,3232256,5303296,5223936,6867456,7830528,0,0,0,
435712,389676,1688876,379468,1619020,0,0,0,3233280,5304320,5224960,6868480,7831552,0,0,0,3234304,5305344,5225984,6869504,7832576,0,0,0,3235328,5306368,5227008,6870528,7833600,0,0,0,3236352,5307392,5228032,6871552,8105984,0,0,0,
435840,389804,1689004,379596,1619148,0,0,0,3334656,5444608,5229056,7143936,8107008,0,0,0,3335680,5445632,5366272,7144960,8108032,0,0,0,3336704,5446656,5367296,7145984,8109056,0,0,0,3337728,5447680,5368320,7147008,8110080,0,0,0,
435968,389932,1689132,379724,1623116,0,0,0,3338752,5448704,5369344,7148032,6170880,0,0,0,3437056,5449728,5370368,7420416,6171904,0,0,0,3438080,5587968,5371392,7421440,6172928,0,0,0,3439104,5588992,5372416,7422464,6173952,0,0,0,
436096,390060,1689260,379852,1623244,0,0,0,3440128,5590016,5509632,7423488,6174976,0,0,0,3441152,5591040,5510656,7424512,6447360,0,0,0,3539456,5592064,5511680,7696896,6448384,0,0,0,3540480,5593088,5512704,7697920,6449408,0,0,0,
436224,390188,1689388,379980,1623372,0,0,0,3541504,5731328,5513728,7698944,6450432,0,0,0,3542528,5732352,5514752,7699968,6451456,0,0,0,3543552,5733376,5652992,7700992,6723840,0,0,0,3641856,5734400,5654016,7973376,6724864,0,0,0,
436352,390316,1689516,380108,1623500,0,0,0,3642880,5735424,5655040,7974400,6725888,0,0,0,3643904,5736448,5656064,7975424,6726912,0,0,0,3644928,5874688,5657088,7976448,6727936,0,0,0,3645952,5875712,5658112,7977472,7000320,0,0,0,
445440,390444,1689644,384076,1623628,0,0,0,2925312,5876736,5796352,6038272,7001344,0,0,0,2926336,5877760,5797376,6039296,7002368,0,0,0,2927360,5878784,5798400,6040320,7003392,0,0,0,2928384,5879808,5799424,6041344,7004416,0,0,0,
445568,400940,1689772,384204,1623756,0,0,0,2929408,3724544,5800448,6042368,7276800,0,0,0,3027712,3725568,5801472,6314752,7277824,0,0,0,3028736,3726592,5939712,6315776,7278848,0,0,0,3029760,3727616,5940736,6316800,7279872,0,0,0,
445696,401068,1689900,384332,1623884,0,0,0,3030784,3728640,5941760,6317824,7280896,0,0,0,3031808,3729664,5942784,6318848,7553280,0,0,0,3130112,3730688,5943808,6591232,7554304,0,0,0,3131136,3867904,5944832,6592256,7555328,0,0,0,
445824,401196,1700396,384460,1624012,0,0,0,3132160,3868928,3789568,6593280,7556352,0,0,0,3133184,3869952,3790592,6594304,7557376,0,0,0,3134208,3870976,3791616,6595328,7829760,0,0,0,3232512,3872000,3792640,6867712,7830784,0,0,0,
445952,401324,1700524,384588,1624140,0,0,0,3233536,3873024,3793664,6868736,7831808,0,0,0,3234560,3874048,3794688,6869760,7832832,0,0,0,3235584,4011264,3795712,6870784,7833856,0,0,0,3236608,4012288,3932928,6871808,8106240,0,0,0,
446080,401452,1700652,384716,1624268,0,0,0,3334912,4013312,3933952,7144192,8107264,0,0,0,3335936,4014336,3934976,7145216,8108288,0,0,0,3336960,4015360,3936000,7146240,8109312,0,0,0,3337984,4016384,3937024,7147264,8110336,0,0,0,
446208,401580,1700780,384844,1628236,0,0,0,3339008,4017408,3938048,7148288,6171136,0,0,0,3437312,4154624,3939072,7420672,6172160,0,0,0,3438336,4155648,4076288,7421696,6173184,0,0,0,3439360,4156672,4077312,7422720,6174208,0,0,0,
446336,401708,1700908,384972,1628364,0,0,0,3440384,4157696,4078336,7423744,6175232,0,0,0,3441408,4158720,4079360,7424768,6447616,0,0,0,3539712,4159744,4080384,7697152,6448640,0,0,0,3540736,4160768,4081408,7698176,6449664,0,0,0,
446464,401836,1701036,385100,1628492,0,0,0,3541760,4297984,4082432,7699200,6450688,0,0,0,3542784,4299008,4219648,7700224,6451712,0,0,0,3543808,4300032,4220672,7701248,6724096,0,0,0,3642112,4301056,4221696,7973632,6725120,0,0,0,
446592,401964,1701164,385228,1628620,0,0,0,3643136,4302080,4222720,7974656,6726144,0,0,0,3644160,4303104,4223744,7975680,6727168,0,0,0,3645184,4304128,4224768,7976704,6728192,0,0,0,3646208,4441344,4225792,7977728,7000576,0,0,0,
460800,402092,1701292,430156,1628748,0,0,0,2929664,4442368,4363008,6042624,7001600,0,0,0,2930688,4443392,4364032,6043648,7002624,0,0,0,2931712,4444416,4365056,6044672,7003648,0,0,0,2932736,4445440,4366080,6045696,7004672,0,0,0,
460928,402220,1701420,430284,1628876,0,0,0,2933760,4446464,4367104,6046720,7277056,0,0,0,3032064,4447488,4368128,6319104,7278080,0,0,0,3033088,4584704,4369152,6320128,7279104,0,0,0,3034112,4585728,4506368,6321152,7280128,0,0,0,
461056,402348,1701548,430412,1629004,0,0,0,3035136,4586752,4507392,6322176,7281152,0,0,0,3036160,4587776,4508416,6323200,7553536,0,0,0,3134464,4588800,4509440,6595584,7554560,0,0,0,3135488,4589824,4510464,6596608,7555584,0,0,0,
461184,402476,1701676,430540,1629132,0,0,0,3136512,4590848,4511488,6597632,7556608,0,0,0,3137536,4728064,4512512,6598656,7557632,0,0,0,3138560,4729088,4649728,6599680,7830016,0,0,0,3236864,4730112,4650752,6872064,7831040,0,0,0,
461312,402604,1701804,430668,1629260,0,0,0,3237888,4731136,4651776,6873088,7832064,0,0,0,3238912,4732160,4652800,6874112,7833088,0,0,0,3239936,4733184,4653824,6875136,7834112,0,0,0,3240960,4734208,4654848,6876160,8106496,0,0,0,
461440,402732,1701932,430796,1629388,0,0,0,3339264,4871424,4655872,7148544,8107520,0,0,0,3340288,4872448,4793088,7149568,8108544,0,0,0,3341312,4873472,4794112,7150592,8109568,0,0,0,3342336,4874496,4795136,7151616,8110592,0,0,0,
461568,402860,1702060,430924,1633356,0,0,0,3343360,4875520,4796160,7152640,6171392,0,0,0,3441664,4876544,4797184,7425024,6172416,0,0,0,3442688,4877568,4798208,7426048,6173440,0,0,0,3443712,5014784,4799232,7427072,6174464,0,0,0,
461696,402988,1702188,431052,1633484,0,0,0,3444736,5015808,4936448,7428096,6175488,0,0,0,3445760,5016832,4937472,7429120,6447872,0,0,0,3544064,5017856,4938496,7701504,6448896,0,0,0,3545088,5018880,4939520,7702528,6449920,0,0,0,
461824,403116,1702316,431180,1633612,0,0,0,3546112,5019904,4940544,7703552,6450944,0,0,0,3547136,5020928,4941568,7704576,6451968,0,0,0,3548160,5158144,4942592,7705600,6724352,0,0,0,3646464,5159168,5079808,7977984,6725376,0,0,0,
461952,403244,1702444,431308,1633740,0,0,0,3647488,5160192,5080832,7979008,6726400,0,0,0,3648512,5161216,5081856,7980032,6727424,0,0,0,3649536,5162240,5082880,7981056,6728448,0,0,0,3650560,5163264,5083904,7982080,7000832,0,0,0,
471040,403372,1702572,435276,1633868,0,0,0,2929920,5164288,5084928,6042880,7001856,0,0,0,2930944,5301504,5085952,6043904,7002880,0,0,0,2931968,5302528,5223168,6044928,7003904,0,0,0,2932992,5303552,5224192,6045952,7004928,0,0,0,
471168,403500,1702700,435404,1633996,0,0,0,2934016,5304576,5225216,6046976,7277312,0,0,0,3032320,5305600,5226240,6319360,7278336,0,0,0,3033344,5306624,5227264,6320384,7279360,0,0,0,3034368,5307648,5228288,6321408,7280384,0,0,0,
471296,403628,1702828,435532,1634124,0,0,0,3035392,5444864,5229312,6322432,7281408,0,0,0,3036416,5445888,5366528,6323456,7553792,0,0,0,3134720,5446912,5367552,6595840,7554816,0,0,0,3135744,5447936,5368576,6596864,7555840,0,0,0,
471424,403756,1702956,435660,1634252,0,0,0,3136768,5448960,5369600,6597888,7556864,0,0,0,3137792,5449984,5370624,6598912,7557888,0,0,0,3138816,5588224,5371648,6599936,7830272,0,0,0,3237120,5589248,5372672,6872320,7831296,0,0,0,
471552,403884,1703084,435788,1634380,0,0,0,3238144,5590272,5509888,6873344,7832320,0,0,0,3239168,5591296,5510912,6874368,7833344,0,0,0,3240192,5592320,5511936,6875392,7834368,0,0,0,3241216,5593344,5512960,6876416,8106752,0,0,0,
471680,404012,1703212,435916,1634508,0,0,0,3339520,5731584,5513984,7148800,8107776,0,0,0,3340544,5732608,5515008,7149824,8108800,0,0,0,3341568,5733632,5653248,7150848,8109824,0,0,0,3342592,5734656,5654272,7151872,8110848,0,0,0,
471808,404140,1703340,436044,1658956,0,0,0,3343616,5735680,5655296,7152896,6175744,0,0,0,3441920,5736704,5656320,7425280,6176768,0,0,0,3442944,5874944,5657344,7426304,6177792,0,0,0,3443968,5875968,5658368,7427328,6178816,0,0,0,
471936,404268,1703468,436172,1659084,0,0,0,3444992,5876992,5796608,7428352,6179840,0,0,0,3446016,5878016,5797632,7429376,6452224,0,0,0,3544320,5879040,5798656,7701760,6453248,0,0,0,3545344,5880064,5799680,7702784,6454272,0,0,0,
472064,414764,1703596,436300,1659212,0,0,0,3546368,3724800,5800704,7703808,6455296,0,0,0,3547392,3725824,5801728,7704832,6456320,0,0,0,3548416,3726848,5939968,7705856,6728704,0,0,0,3646720,3727872,5940992,7978240,6729728,0,0,0,
472192,414892,1703724,436428,1659340,0,0,0,3647744,3728896,5942016,7979264,6730752,0,0,0,3648768,3729920,5943040,7980288,6731776,0,0,0,3649792,3730944,5944064,7981312,6732800,0,0,0,3650816,3868160,5945088,7982336,7005184,0,0,0,
486400,415020,1769516,440396,1659468,0,0,0,2930176,3869184,3795968,6043136,7006208,0,0,0,2931200,3870208,3796992,6044160,7007232,0,0,0,2932224,3871232,3798016,6045184,7008256,0,0,0,2933248,3872256,3799040,6046208,7009280,0,0,0,
486528,415148,1769644,440524,1659596,0,0,0,2934272,3873280,3800064,6047232,7281664,0,0,0,3032576,3874304,3801088,6319616,7282688,0,0,0,3033600,4011520,3802112,6320640,7283712,0,0,0,3034624,4012544,3939328,6321664,7284736,0,0,0,
486656,415276,1769772,440652,1659724,0,0,0,3035648,4013568,3940352,6322688,7285760,0,0,0,3036672,4014592,3941376,6323712,7558144,0,0,0,3134976,4015616,3942400,6596096,7559168,0,0,0,3136000,4016640,3943424,6597120,7560192,0,0,0,
486784,415404,1769900,440780,1659852,0,0,0,3137024,4017664,3944448,6598144,7561216,0,0,0,3138048,4154880,3945472,6599168,7562240,0,0,0,3139072,4155904,4082688,6600192,7834624,0,0,0,3237376,4156928,4083712,6872576,7835648,0,0,0,
486912,415532,1770028,440908,1659980,0,0,0,3238400,4157952,4084736,6873600,7836672,0,0,0,3239424,4158976,4085760,6874624,7837696,0,0,0,3240448,4160000,4086784,6875648,7838720,0,0,0,3241472,4161024,4087808,6876672,8111104,0,0,0,
487040,415660,1770156,441036,1660108,0,0,0,3339776,4298240,4088832,7149056,8112128,0,0,0,3340800,4299264,4226048,7150080,8113152,0,0,0,3341824,4300288,4227072,7151104,8114176,0,0,0,3342848,4301312,4228096,7152128,8115200,0,0,0,
487168,415788,1770284,441164,1664076,0,0,0,3343872,4302336,4229120,7153152,6176000,0,0,0,3442176,4303360,4230144,7425536,6177024,0,0,0,3443200,4304384,4231168,7426560,6178048,0,0,0,3444224,4441600,4232192,7427584,6179072,0,0,0,
487296,415916,1770412,441292,1664204,0,0,0,3445248,4442624,4369408,7428608,6180096,0,0,0,3446272,4443648,4370432,7429632,6452480,0,0,0,3544576,4444672,4371456,7702016,6453504,0,0,0,3545600,4445696,4372480,7703040,6454528,0,0,0,
487424,416044,1770540,441420,1664332,0,0,0,3546624,4446720,4373504,7704064,6455552,0,0,0,3547648,4447744,4374528,7705088,6456576,0,0,0,3548672,4584960,4375552,7706112,6728960,0,0,0,3646976,4585984,4512768,7978496,6729984,0,0,0,
487552,416172,1770668,441548,1664460,0,0,0,3648000,4587008,4513792,7979520,6731008,0,0,0,3649024,4588032,4514816,7980544,6732032,0,0,0,3650048,4589056,4515840,7981568,6733056,0,0,0,3651072,4590080,4516864,7982592,7005440,0,0,0,
496640,416300,1770796,445516,1664588,0,0,0,2930432,4591104,4517888,6043392,7006464,0,0,0,2931456,4728320,4518912,6044416,7007488,0,0,0,2932480,4729344,4656128,6045440,7008512,0,0,0,2933504,4730368,4657152,6046464,7009536,0,0,0,
496768,416428,1770924,445644,1664716,0,0,0,2934528,4731392,4658176,6047488,7281920,0,0,0,3032832,4732416,4659200,6319872,7282944,0,0,0,3033856,4733440,4660224,6320896,7283968,0,0,0,3034880,4734464,4661248,6321920,7284992,0,0,0,
496896,416556,1771052,445772,1664844,0,0,0,3035904,4871680,4662272,6322944,7286016,0,0,0,3036928,4872704,4799488,6323968,7558400,0,0,0,3135232,4873728,4800512,6596352,7559424,0,0,0,3136256,4874752,4801536,6597376,7560448,0,0,0,
497024,416684,1771180,445900,1664972,0,0,0,3137280,4875776,4802560,6598400,7561472,0,0,0,3138304,4876800,4803584,6599424,7562496,0,0,0,3139328,4877824,4804608,6600448,7834880,0,0,0,3237632,5015040,4805632,6872832,7835904,0,0,0,
497152,416812,1771308,446028,1665100,0,0,0,3238656,5016064,4942848,6873856,7836928,0,0,0,3239680,5017088,4943872,6874880,7837952,0,0,0,3240704,5018112,4944896,6875904,7838976,0,0,0,3241728,5019136,4945920,6876928,8111360,0,0,0,
497280,416940,1771436,446156,1665228,0,0,0,3340032,5020160,4946944,7149312,8112384,0,0,0,3341056,5021184,4947968,7150336,8113408,0,0,0,3342080,5158400,4948992,7151360,8114432,0,0,0,3343104,5159424,5086208,7152384,8115456,0,0,0,
497408,417068,1771564,446284,1669196,0,0,0,3344128,5160448,5087232,7153408,6176256,0,0,0,3442432,5161472,5088256,7425792,6177280,0,0,0,3443456,5162496,5089280,7426816,6178304,0,0,0,3444480,5163520,5090304,7427840,6179328,0,0,0,
497536,417196,1771692,446412,1669324,0,0,0,3445504,5164544,5091328,7428864,6180352,0,0,0,3446528,5301760,5092352,7429888,6452736,0,0,0,3544832,5302784,5229568,7702272,6453760,0,0,0,3545856,5303808,5230592,7703296,6454784,0,0,0,
497664,417324,1771820,446540,1669452,0,0,0,3546880,5304832,5231616,7704320,6455808,0,0,0,3547904,5305856,5232640,7705344,6456832,0,0,0,3548928,5306880,5233664,7706368,6729216,0,0,0,3647232,5307904,5234688,7978752,6730240,0,0,0,
497792,417452,1771948,446668,1669580,0,0,0,3648256,5445120,5235712,7979776,6731264,0,0,0,3649280,5446144,5372928,7980800,6732288,0,0,0,3650304,5447168,5373952,7981824,6733312,0,0,0,3651328,5448192,5374976,7982848,7005696,0,0,0,
512000,417580,1772076,471116,1669708,0,0,0,2934784,5449216,5376000,6047744,7006720,0,0,0,2935808,5450240,5377024,6048768,7007744,0,0,0,2936832,5588480,5378048,6049792,7008768,0,0,0,2937856,5589504,5379072,6050816,7009792,0,0,0,
512128,417708,1772204,471244,1669836,0,0,0,2938880,5590528,5516288,6051840,7282176,0,0,0,3037184,5591552,5517312,6324224,7283200,0,0,0,3038208,5592576,5518336,6325248,7284224,0,0,0,3039232,5593600,5519360,6326272,7285248,0,0,0,
512256,417836,1772332,471372,1669964,0,0,0,3040256,5731840,5520384,6327296,7286272,0,0,0,3041280,5732864,5521408,6328320,7558656,0,0,0,3139584,5733888,5659648,6600704,7559680,0,0,0,3140608,5734912,5660672,6601728,7560704,0,0,0,
512384,417964,1772460,471500,1670092,0,0,0,3141632,5735936,5661696,6602752,7561728,0,0,0,3142656,5736960,5662720,6603776,7562752,0,0,0,3143680,5875200,5663744,6604800,7835136,0,0,0,3241984,5876224,5664768,6877184,7836160,0,0,0,
512512,418092,1772588,471628,1670220,0,0,0,3243008,5877248,5803008,6878208,7837184,0,0,0,3244032,5878272,5804032,6879232,7838208,0,0,0,3245056,5879296,5805056,6880256,7839232,0,0,0,3246080,5880320,5806080,6881280,8111616,0,0,0,
512640,428588,1772716,471756,1670348,0,0,0,3344384,3725056,5807104,7153664,8112640,0,0,0,3345408,3726080,5808128,7154688,8113664,0,0,0,3346432,3727104,5946368,7155712,8114688,0,0,0,3347456,3728128,5947392,7156736,8115712,0,0,0,
512768,428716,1772844,471884,1674316,0,0,0,3348480,3729152,5948416,7157760,6176512,0,0,0,3446784,3730176,5949440,7430144,6177536,0,0,0,3447808,3731200,5950464,7431168,6178560,0,0,0,3448832,3868416,5951488,7432192,6179584,0,0,0,
512896,428844,1783340,472012,1674444,0,0,0,3449856,3869440,3796224,7433216,6180608,0,0,0,3450880,3870464,3797248,7434240,6452992,0,0,0,3549184,3871488,3798272,7706624,6454016,0,0,0,3550208,3872512,3799296,7707648,6455040,0,0,0,
513024,428972,1783468,472140,1674572,0,0,0,3551232,3873536,3800320,7708672,6456064,0,0,0,3552256,3874560,3801344,7709696,6457088,0,0,0,3553280,4011776,3802368,7710720,6729472,0,0,0,3651584,4012800,3939584,7983104,6730496,0,0,0,
513152,429100,1783596,472268,1674700,0,0,0,3652608,4013824,3940608,7984128,6731520,0,0,0,3653632,4014848,3941632,7985152,6732544,0,0,0,3654656,4015872,3942656,7986176,6733568,0,0,0,3655680,4016896,3943680,7987200,7005952,0,0,0,
522240,429228,1783724,476236,1674828,0,0,0,2935040,4017920,3944704,6048000,7006976,0,0,0,2936064,4155136,3945728,6049024,7008000,0,0,0,2937088,4156160,4082944,6050048,7009024,0,0,0,2938112,4157184,4083968,6051072,7010048,0,0,0,
522368,429356,1783852,476364,1674956,0,0,0,2939136,4158208,4084992,6052096,7282432,0,0,0,3037440,4159232,4086016,6324480,7283456,0,0,0,3038464,4160256,4087040,6325504,7284480,0,0,0,3039488,4161280,4088064,6326528,7285504,0,0,0,
522496,429484,1783980,476492,1675084,0,0,0,3040512,4298496,4089088,6327552,7286528,0,0,0,3041536,4299520,4226304,6328576,7558912,0,0,0,3139840,4300544,4227328,6600960,7559936,0,0,0,3140864,4301568,4228352,6601984,7560960,0,0,0,
522624,429612,1784108,476620,1675212,0,0,0,3141888,4302592,4229376,6603008,7561984,0,0,0,3142912,4303616,4230400,6604032,7563008,0,0,0,3143936,4304640,4231424,6605056,7835392,0,0,0,3242240,4441856,4232448,6877440,7836416,0,0,0,
522752,429740,1784236,476748,1675340,0,0,0,3243264,4442880,4369664,6878464,7837440,0,0,0,3244288,4443904,4370688,6879488,7838464,0,0,0,3245312,4444928,4371712,6880512,7839488,0,0,0,3246336,4445952,4372736,6881536,8111872,0,0,0,
522880,429868,1784364,476876,1675468,0,0,0,3344640,4446976,4373760,7153920,8112896,0,0,0,3345664,4448000,4374784,7154944,8113920,0,0,0,3346688,4585216,4375808,7155968,8114944,0,0,0,3347712,4586240,4513024,7156992,8115968,0,0,0,
523008,429996,1784492,477004,1720396,0,0,0,3348736,4587264,4514048,7158016,6180864,0,0,0,3447040,4588288,4515072,7430400,6181888,0,0,0,3448064,4589312,4516096,7431424,6182912,0,0,0,3449088,4590336,4517120,7432448,6183936,0,0,0,
523136,430124,1784620,477132,1720524,0,0,0,3450112,4591360,4518144,7433472,6184960,0,0,0,3451136,4728576,4519168,7434496,6457344,0,0,0,3549440,4729600,4656384,7706880,6458368,0,0,0,3550464,4730624,4657408,7707904,6459392,0,0,0,
523264,430252,1784748,477260,1720652,0,0,0,3551488,4731648,4658432,7708928,6460416,0,0,0,3552512,4732672,4659456,7709952,6461440,0,0,0,3553536,4733696,4660480,7710976,6733824,0,0,0,3651840,4734720,4661504,7983360,6734848,0,0,0,
523392,430380,1784876,477388,1720780,0,0,0,3652864,4871936,4662528,7984384,6735872,0,0,0,3653888,4872960,4799744,7985408,6736896,0,0,0,3654912,4873984,4800768,7986432,6737920,0,0,0,3655936,4875008,4801792,7987456,7010304,0,0,0,
537600,430508,1785004,481356,1720908,0,0,0,2935296,4876032,4802816,6048256,7011328,0,0,0,2936320,4877056,4803840,6049280,7012352,0,0,0,2937344,4878080,4804864,6050304,7013376,0,0,0,2938368,5015296,4805888,6051328,7014400,0,0,0,
537728,430636,1785132,481484,1721036,0,0,0,2939392,5016320,4943104,6052352,7286784,0,0,0,3037696,5017344,4944128,6324736,7287808,0,0,0,3038720,5018368,4945152,6325760,7288832,0,0,0,3039744,5019392,4946176,6326784,7289856,0,0,0,
537856,430764,1785260,481612,1721164,0,0,0,3040768,5020416,4947200,6327808,7290880,0,0,0,3041792,5021440,4948224,6328832,7563264,0,0,0,3140096,5158656,4949248,6601216,7564288,0,0,0,3141120,5159680,5086464,6602240,7565312,0,0,0,
537984,430892,1785388,481740,1721292,0,0,0,3142144,5160704,5087488,6603264,7566336,0,0,0,3143168,5161728,5088512,6604288,7567360,0,0,0,3144192,5162752,5089536,6605312,7839744,0,0,0,3242496,5163776,5090560,6877696,7840768,0,0,0,
538112,431020,1785516,481868,1721420,0,0,0,3243520,5164800,5091584,6878720,7841792,0,0,0,3244544,5302016,5092608,6879744,7842816,0,0,0,3245568,5303040,5229824,6880768,7843840,0,0,0,3246592,5304064,5230848,6881792,8116224,0,0,0,
538240,431148,1785644,481996,1721548,0,0,0,3344896,5305088,5231872,7154176,8117248,0,0,0,3345920,5306112,5232896,7155200,8118272,0,0,0,3346944,5307136,5233920,7156224,8119296,0,0,0,3347968,5308160,5234944,7157248,8120320,0,0,0,
538368,431276,1785772,482124,1725516,0,0,0,3348992,5445376,5235968,7158272,6181120,0,0,0,3447296,5446400,5373184,7430656,6182144,0,0,0,3448320,5447424,5374208,7431680,6183168,0,0,0,3449344,5448448,5375232,7432704,6184192,0,0,0,
538496,431404,1785900,482252,1725644,0,0,0,3450368,5449472,5376256,7433728,6185216,0,0,0,3451392,5450496,5377280,7434752,6457600,0,0,0,3549696,5588736,5378304,7707136,6458624,0,0,0,3550720,5589760,5379328,7708160,6459648,0,0,0,
538624,431532,1786028,482380,1725772,0,0,0,3551744,5590784,5516544,7709184,6460672,0,0,0,3552768,5591808,5517568,7710208,6461696,0,0,0,3553792,5592832,5518592,7711232,6734080,0,0,0,3652096,5593856,5519616,7983616,6735104,0,0,0,
538752,431660,1786156,482508,1725900,0,0,0,3653120,5732096,5520640,7984640,6736128,0,0,0,3654144,5733120,5521664,7985664,6737152,0,0,0,3655168,5734144,5659904,7986688,6738176,0,0,0,3656192,5735168,5660928,7987712,7010560,0,0,0,
547840,431788,1786284,486476,1726028,0,0,0,2935552,5736192,5661952,6048512,7011584,0,0,0,2936576,5737216,5662976,6049536,7012608,0,0,0,2937600,5875456,5664000,6050560,7013632,0,0,0,2938624,5876480,5665024,6051584,7014656,0,0,0,
547968,431916,1786412,486604,1726156,0,0,0,2939648,5877504,5803264,6052608,7287040,0,0,0,3037952,5878528,5804288,6324992,7288064,0,0,0,3038976,5879552,5805312,6326016,7289088,0,0,0,3040000,5880576,5806336,6327040,7290112,0,0,0,
548096,553004,1786540,486732,1726284,0,0,0,3041024,3731456,5807360,6328064,7291136,0,0,0,3042048,3732480,5808384,6329088,7563520,0,0,0,3140352,3733504,5946624,6601472,7564544,0,0,0,3141376,3734528,5947648,6602496,7565568,0,0,0,
548224,553132,1786668,486860,1726412,0,0,0,3142400,3735552,5948672,6603520,7566592,0,0,0,3143424,3736576,5949696,6604544,7567616,0,0,0,3144448,3737600,5950720,6605568,7840000,0,0,0,3242752,3874816,5951744,6877952,7841024,0,0,0,
548352,553260,1797164,486988,1726540,0,0,0,3243776,3875840,3796480,6878976,7842048,0,0,0,3244800,3876864,3797504,6880000,7843072,0,0,0,3245824,3877888,3798528,6881024,7844096,0,0,0,3246848,3878912,3799552,6882048,8116480,0,0,0,
548480,553388,1797292,487116,1726668,0,0,0,3345152,3879936,3800576,7154432,8117504,0,0,0,3346176,3880960,3801600,7155456,8118528,0,0,0,3347200,4018176,3802624,7156480,8119552,0,0,0,3348224,4019200,3939840,7157504,8120576,0,0,0,
548608,553516,1797420,487244,1730636,0,0,0,3349248,4020224,3940864,7158528,6181376,0,0,0,3447552,4021248,3941888,7430912,6182400,0,0,0,3448576,4022272,3942912,7431936,6183424,0,0,0,3449600,4023296,3943936,7432960,6184448,0,0,0,
548736,553644,1797548,487372,1730764,0,0,0,3450624,4024320,3944960,7433984,6185472,0,0,0,3451648,4161536,3945984,7435008,6457856,0,0,0,3549952,4162560,4083200,7707392,6458880,0,0,0,3550976,4163584,4084224,7708416,6459904,0,0,0,
548864,553772,1797676,487500,1730892,0,0,0,3552000,4164608,4085248,7709440,6460928,0,0,0,3553024,4165632,4086272,7710464,6461952,0,0,0,3554048,4166656,4087296,7711488,6734336,0,0,0,3652352,4167680,4088320,7983872,6735360,0,0,0,
548992,553900,1797804,487628,1731020,0,0,0,3653376,4304896,4089344,7984896,6736384,0,0,0,3654400,4305920,4226560,7985920,6737408,0,0,0,3655424,4306944,4227584,7986944,6738432,0,0,0,3656448,4307968,4228608,7987968,7010816,0,0,0,
563200,554028,1797932,512076,1731148,0,0,0,2939904,4308992,4229632,6052864,7011840,0,0,0,2940928,4310016,4230656,6053888,7012864,0,0,0,2941952,4311040,4231680,6054912,7013888,0,0,0,2942976,4448256,4232704,6055936,7014912,0,0,0,
563328,554156,1798060,512204,1731276,0,0,0,2944000,4449280,4369920,6056960,7287296,0,0,0,3042304,4450304,4370944,6329344,7288320,0,0,0,3043328,4451328,4371968,6330368,7289344,0,0,0,3044352,4452352,4372992,6331392,7290368,0,0,0,
563456,554284,1798188,512332,1731404,0,0,0,3045376,4453376,4374016,6332416,7291392,0,0,0,3046400,4454400,4375040,6333440,7563776,0,0,0,3144704,4591616,4376064,6605824,7564800,0,0,0,3145728,4592640,4513280,6606848,7565824,0,0,0,
563584,554412,1798316,512460,1731532,0,0,0,3146752,4593664,4514304,6607872,7566848,0,0,0,3147776,4594688,4515328,6608896,7567872,0,0,0,3148800,4595712,4516352,6609920,7840256,0,0,0,3247104,4596736,4517376,6882304,7841280,0,0,0,
563712,554540,1798444,512588,1731660,0,0,0,3248128,4597760,4518400,6883328,7842304,0,0,0,3249152,4734976,4519424,6884352,7843328,0,0,0,3250176,4736000,4656640,6885376,7844352,0,0,0,3251200,4737024,4657664,6886400,8116736,0,0,0,
563840,554668,1798572,512716,1731788,0,0,0,3349504,4738048,4658688,7158784,8117760,0,0,0,3350528,4739072,4659712,7159808,8118784,0,0,0,3351552,4740096,4660736,7160832,8119808,0,0,0,3352576,4741120,4661760,7161856,8120832,0,0,0,
563968,554796,1798700,512844,1735756,0,0,0,3353600,4878336,4662784,7162880,6181632,0,0,0,3451904,4879360,4800000,7435264,6182656,0,0,0,3452928,4880384,4801024,7436288,6183680,0,0,0,3453952,4881408,4802048,7437312,6184704,0,0,0,
564096,554924,1798828,512972,1735884,0,0,0,3454976,4882432,4803072,7438336,6185728,0,0,0,3456000,4883456,4804096,7439360,6458112,0,0,0,3554304,4884480,4805120,7711744,6459136,0,0,0,3555328,5021696,4806144,7712768,6460160,0,0,0,
564224,555052,1798956,513100,1736012,0,0,0,3556352,5022720,4943360,7713792,6461184,0,0,0,3557376,5023744,4944384,7714816,6462208,0,0,0,3558400,5024768,4945408,7715840,6734592,0,0,0,3656704,5025792,4946432,7988224,6735616,0,0,0,
564352,555180,1799084,513228,1736140,0,0,0,3657728,5026816,4947456,7989248,6736640,0,0,0,3658752,5027840,4948480,7990272,6737664,0,0,0,3659776,5165056,4949504,7991296,6738688,0,0,0,3660800,5166080,5086720,7992320,7011072,0,0,0,
573440,555308,1799212,517196,1736268,0,0,0,2940160,5167104,5087744,6053120,7012096,0,0,0,2941184,5168128,5088768,6054144,7013120,0,0,0,2942208,5169152,5089792,6055168,7014144,0,0,0,2943232,5170176,5090816,6056192,7015168,0,0,0,
573568,555436,1799340,517324,1736396,0,0,0,2944256,5171200,5091840,6057216,7287552,0,0,0,3042560,5308416,5092864,6329600,7288576,0,0,0,3043584,5309440,5230080,6330624,7289600,0,0,0,3044608,5310464,5231104,6331648,7290624,0,0,0,
573696,555564,1799468,517452,1736524,0,0,0,3045632,5311488,5232128,6332672,7291648,0,0,0,3046656,5312512,5233152,6333696,7564032,0,0,0,3144960,5313536,5234176,6606080,7565056,0,0,0,3145984,5314560,5235200,6607104,7566080,0,0,0,
573824,555692,1799596,517580,1736652,0,0,0,3147008,5451776,5236224,6608128,7567104,0,0,0,3148032,5452800,5373440,6609152,7568128,0,0,0,3149056,5453824,5374464,6610176,7840512,0,0,0,3247360,5454848,5375488,6882560,7841536,0,0,0,
573952,555820,1799724,517708,1736780,0,0,0,3248384,5455872,5376512,6883584,7842560,0,0,0,3249408,5456896,5377536,6884608,7843584,0,0,0,3250432,5595136,5378560,6885632,7844608,0,0,0,3251456,5596160,5379584,6886656,8116992,0,0,0,
574080,555948,1799852,517836,1736908,0,0,0,3349760,5597184,5516800,7159040,8118016,0,0,0,3350784,5598208,5517824,7160064,8119040,0,0,0,3351808,5599232,5518848,7161088,8120064,0,0,0,3352832,5600256,5519872,7162112,8121088,0,0,0,
574208,556076,1799980,517964,1761356,0,0,0,3353856,5738496,5520896,7163136,6185984,0,0,0,3452160,5739520,5521920,7435520,6187008,0,0,0,3453184,5740544,5660160,7436544,6188032,0,0,0,3454208,5741568,5661184,7437568,6189056,0,0,0,
574336,556204,1800108,518092,1761484,0,0,0,3455232,5742592,5662208,7438592,6190080,0,0,0,3456256,5743616,5663232,7439616,6462464,0,0,0,3554560,5881856,5664256,7712000,6463488,0,0,0,3555584,5882880,5665280,7713024,6464512,0,0,0,
574464,556332,1800236,518220,1761612,0,0,0,3556608,5883904,5803520,7714048,6465536,0,0,0,3557632,5884928,5804544,7715072,6466560,0,0,0,3558656,5885952,5805568,7716096,6738944,0,0,0,3656960,5886976,5806592,7988480,6739968,0,0,0,
574592,566828,1800364,518348,1761740,0,0,0,3657984,3731712,5807616,7989504,6740992,0,0,0,3659008,3732736,5808640,7990528,6742016,0,0,0,3660032,3733760,5946880,7991552,6743040,0,0,0,3661056,3734784,5947904,7992576,7015424,0,0,0,
588800,566956,1800492,522316,1761868,0,0,0,2940416,3735808,5948928,6053376,7016448,0,0,0,2941440,3736832,5949952,6054400,7017472,0,0,0,2942464,3737856,5950976,6055424,7018496,0,0,0,2943488,3875072,5952000,6056448,7019520,0,0,0,
588928,567084,1810988,522444,1761996,0,0,0,2944512,3876096,3796736,6057472,7291904,0,0,0,3042816,3877120,3797760,6329856,7292928,0,0,0,3043840,3878144,3798784,6330880,7293952,0,0,0,3044864,3879168,3799808,6331904,7294976,0,0,0,
589056,567212,1811116,522572,1762124,0,0,0,3045888,3880192,3800832,6332928,7296000,0,0,0,3046912,3881216,3801856,6333952,7568384,0,0,0,3145216,4018432,3802880,6606336,7569408,0,0,0,3146240,4019456,3940096,6607360,7570432,0,0,0,
589184,567340,1811244,522700,1762252,0,0,0,3147264,4020480,3941120,6608384,7571456,0,0,0,3148288,4021504,3942144,6609408,7572480,0,0,0,3149312,4022528,3943168,6610432,7844864,0,0,0,3247616,4023552,3944192,6882816,7845888,0,0,0,
589312,567468,1811372,522828,1762380,0,0,0,3248640,4024576,3945216,6883840,7846912,0,0,0,3249664,4161792,3946240,6884864,7847936,0,0,0,3250688,4162816,4083456,6885888,7848960,0,0,0,3251712,4163840,4084480,6886912,8121344,0,0,0,
589440,567596,1811500,522956,1762508,0,0,0,3350016,4164864,4085504,7159296,8122368,0,0,0,3351040,4165888,4086528,7160320,8123392,0,0,0,3352064,4166912,4087552,7161344,8124416,0,0,0,3353088,4167936,4088576,7162368,8125440,0,0,0,
589568,567724,1811628,523084,1766476,0,0,0,3354112,4305152,4089600,7163392,6186240,0,0,0,3452416,4306176,4226816,7435776,6187264,0,0,0,3453440,4307200,4227840,7436800,6188288,0,0,0,3454464,4308224,4228864,7437824,6189312,0,0,0,
589696,567852,1811756,523212,1766604,0,0,0,3455488,4309248,4229888,7438848,6190336,0,0,0,3456512,4310272,4230912,7439872,6462720,0,0,0,3554816,4311296,4231936,7712256,6463744,0,0,0,3555840,4448512,4232960,7713280,6464768,0,0,0,
589824,567980,1811884,523340,1766732,0,0,0,3556864,4449536,4370176,7714304,6465792,0,0,0,3557888,4450560,4371200,7715328,6466816,0,0,0,3558912,4451584,4372224,7716352,6739200,0,0,0,3657216,4452608,4373248,7988736,6740224,0,0,0,
589952,568108,1812012,523468,1766860,0,0,0,3658240,4453632,4374272,7989760,6741248,0,0,0,3659264,4454656,4375296,7990784,6742272,0,0,0,3660288,4591872,4376320,7991808,6743296,0,0,0,3661312,4592896,4513536,7992832,7015680,0,0,0,
599040,568236,1812140,527436,1766988,0,0,0,2940672,4593920,4514560,6053632,7016704,0,0,0,2941696,4594944,4515584,6054656,7017728,0,0,0,2942720,4595968,4516608,6055680,7018752,0,0,0,2943744,4596992,4517632,6056704,7019776,0,0,0,
599168,568364,1812268,527564,1767116,0,0,0,2944768,4598016,4518656,6057728,7292160,0,0,0,3043072,4735232,4519680,6330112,7293184,0,0,0,3044096,4736256,4656896,6331136,7294208,0,0,0,3045120,4737280,4657920,6332160,7295232,0,0,0,
599296,568492,1812396,527692,1767244,0,0,0,3046144,4738304,4658944,6333184,7296256,0,0,0,3047168,4739328,4659968,6334208,7568640,0,0,0,3145472,4740352,4660992,6606592,7569664,0,0,0,3146496,4741376,4662016,6607616,7570688,0,0,0,
599424,568620,1812524,527820,1767372,0,0,0,3147520,4878592,4663040,6608640,7571712,0,0,0,3148544,4879616,4800256,6609664,7572736,0,0,0,3149568,4880640,4801280,6610688,7845120,0,0,0,3247872,4881664,4802304,6883072,7846144,0,0,0,
599552,568748,1812652,527948,1767500,0,0,0,3248896,4882688,4803328,6884096,7847168,0,0,0,3249920,4883712,4804352,6885120,7848192,0,0,0,3250944,4884736,4805376,6886144,7849216,0,0,0,3251968,5021952,4806400,6887168,8121600,0,0,0,
599680,568876,1812780,528076,1767628,0,0,0,3350272,5022976,4943616,7159552,8122624,0,0,0,3351296,5024000,4944640,7160576,8123648,0,0,0,3352320,5025024,4945664,7161600,8124672,0,0,0,3353344,5026048,4946688,7162624,8125696,0,0,0,
599808,569004,1812908,528204,1771596,0,0,0,3354368,5027072,4947712,7163648,6186496,0,0,0,3452672,5028096,4948736,7436032,6187520,0,0,0,3453696,5165312,4949760,7437056,6188544,0,0,0,3454720,5166336,5086976,7438080,6189568,0,0,0,
599936,569132,1813036,528332,1771724,0,0,0,3455744,5167360,5088000,7439104,6190592,0,0,0,3456768,5168384,5089024,7440128,6462976,0,0,0,3555072,5169408,5090048,7712512,6464000,0,0,0,3556096,5170432,5091072,7713536,6465024,0,0,0,
600064,569260,1813164,528460,1771852,0,0,0,3557120,5171456,5092096,7714560,6466048,0,0,0,3558144,5308672,5093120,7715584,6467072,0,0,0,3559168,5309696,5230336,7716608,6739456,0,0,0,3657472,5310720,5231360,7988992,6740480,0,0,0,
600192,569388,1813292,528588,1771980,0,0,0,3658496,5311744,5232384,7990016,6741504,0,0,0,3659520,5312768,5233408,7991040,6742528,0,0,0,3660544,5313792,5234432,7992064,6743552,0,0,0,3661568,5314816,5235456,7993088,7015936,0,0,0,
614400,569516,1813420,573516,1772108,0,0,0,2945024,5452032,5236480,6057984,7016960,0,0,0,2946048,5453056,5373696,6059008,7017984,0,0,0,2947072,5454080,5374720,6060032,7019008,0,0,0,2948096,5455104,5375744,6061056,7020032,0,0,0,
614528,569644,1813548,573644,1772236,0,0,0,2949120,5456128,5376768,6062080,7292416,0,0,0,3047424,5457152,5377792,6334464,7293440,0,0,0,3048448,5595392,5378816,6335488,7294464,0,0,0,3049472,5596416,5379840,6336512,7295488,0,0,0,
614656,569772,1813676,573772,1772364,0,0,0,3050496,5597440,5517056,6337536,7296512,0,0,0,3051520,5598464,5518080,6338560,7568896,0,0,0,3149824,5599488,5519104,6610944,7569920,0,0,0,3150848,5600512,5520128,6611968,7570944,0,0,0,
614784,569900,1813804,573900,1772492,0,0,0,3151872,5738752,5521152,6612992,7571968,0,0,0,3152896,5739776,5522176,6614016,7572992,0,0,0,3153920,5740800,5660416,6615040,7845376,0,0,0,3252224,5741824,5661440,6887424,7846400,0,0,0,
614912,570028,1813932,574028,1772620,0,0,0,3253248,5742848,5662464,6888448,7847424,0,0,0,3254272,5743872,5663488,6889472,7848448,0,0,0,3255296,5882112,5664512,6890496,7849472,0,0,0,3256320,5883136,5665536,6891520,8121856,0,0,0,
615040,570156,1814060,574156,1772748,0,0,0,3354624,5884160,5803776,7163904,8122880,0,0,0,3355648,5885184,5804800,7164928,8123904,0,0,0,3356672,5886208,5805824,7165952,8124928,0,0,0,3357696,5887232,5806848,7166976,8125952,0,0,0,
615168,580652,1814188,574284,1776716,0,0,0,3358720,3731968,5807872,7168000,6186752,0,0,0,3457024,3732992,5808896,7440384,6187776,0,0,0,3458048,3734016,5947136,7441408,6188800,0,0,0,3459072,3735040,5948160,7442432,6189824,0,0,0,
615296,580780,1814316,574412,1776844,0,0,0,3460096,3736064,5949184,7443456,6190848,0,0,0,3461120,3737088,5950208,7444480,6463232,0,0,0,3559424,3738112,5951232,7716864,6464256,0,0,0,3560448,3875328,5952256,7717888,6465280,0,0,0,
615424,580908,1935404,574540,1776972,0,0,0,3561472,3876352,3803136,7718912,6466304,0,0,0,3562496,3877376,3804160,7719936,6467328,0,0,0,3563520,3878400,3805184,7720960,6739712,0,0,0,3661824,3879424,3806208,7993344,6740736,0,0,0,
615552,581036,1935532,574668,1777100,0,0,0,3662848,3880448,3807232,7994368,6741760,0,0,0,3663872,3881472,3808256,7995392,6742784,0,0,0,3664896,4018688,3809280,7996416,6743808,0,0,0,3665920,4019712,3946496,7997440,7016192,0,0,0,
624640,581164,1935660,578636,1777228,0,0,0,2945280,4020736,3947520,6058240,7017216,0,0,0,2946304,4021760,3948544,6059264,7018240,0,0,0,2947328,4022784,3949568,6060288,7019264,0,0,0,2948352,4023808,3950592,6061312,7020288,0,0,0,
624768,581292,1935788,578764,1777356,0,0,0,2949376,4024832,3951616,6062336,7292672,0,0,0,3047680,4162048,3952640,6334720,7293696,0,0,0,3048704,4163072,4089856,6335744,7294720,0,0,0,3049728,4164096,4090880,6336768,7295744,0,0,0,
624896,581420,1935916,578892,1777484,0,0,0,3050752,4165120,4091904,6337792,7296768,0,0,0,3051776,4166144,4092928,6338816,7569152,0,0,0,3150080,4167168,4093952,6611200,7570176,0,0,0,3151104,4168192,4094976,6612224,7571200,0,0,0,
625024,581548,1936044,579020,1777612,0,0,0,3152128,4305408,4096000,6613248,7572224,0,0,0,3153152,4306432,4233216,6614272,7573248,0,0,0,3154176,4307456,4234240,6615296,7845632,0,0,0,3252480,4308480,4235264,6887680,7846656,0,0,0,
625152,581676,1936172,579148,1777740,0,0,0,3253504,4309504,4236288,6888704,7847680,0,0,0,3254528,4310528,4237312,6889728,7848704,0,0,0,3255552,4311552,4238336,6890752,7849728,0,0,0,3256576,4448768,4239360,6891776,8122112,0,0,0,
625280,581804,1936300,579276,1777868,0,0,0,3354880,4449792,4376576,7164160,8123136,0,0,0,3355904,4450816,4377600,7165184,8124160,0,0,0,3356928,4451840,4378624,7166208,8125184,0,0,0,3357952,4452864,4379648,7167232,8126208,0,0,0,
625408,581932,1936428,579404,1802316,0,0,0,3358976,4453888,4380672,7168256,6191104,0,0,0,3457280,4454912,4381696,7440640,6192128,0,0,0,3458304,4592128,4382720,7441664,6193152,0,0,0,3459328,4593152,4519936,7442688,6194176,0,0,0,
625536,582060,1936556,579532,1802444,0,0,0,3460352,4594176,4520960,7443712,6195200,0,0,0,3461376,4595200,4521984,7444736,6467584,0,0,0,3559680,4596224,4523008,7717120,6468608,0,0,0,3560704,4597248,4524032,7718144,6469632,0,0,0,
625664,582188,1936684,579660,1802572,0,0,0,3561728,4598272,4525056,7719168,6470656,0,0,0,3562752,4735488,4526080,7720192,6471680,0,0,0,3563776,4736512,4663296,7721216,6744064,0,0,0,3662080,4737536,4664320,7993600,6745088,0,0,0,
625792,582316,1936812,579788,1802700,0,0,0,3663104,4738560,4665344,7994624,6746112,0,0,0,3664128,4739584,4666368,7995648,6747136,0,0,0,3665152,4740608,4667392,7996672,6748160,0,0,0,3666176,4741632,4668416,7997696,7020544,0,0,0,
640000,582444,1936940,583756,1802828,0,0,0,2945536,4878848,4669440,6058496,7021568,0,0,0,2946560,4879872,4806656,6059520,7022592,0,0,0,2947584,4880896,4807680,6060544,7023616,0,0,0,2948608,4881920,4808704,6061568,7024640,0,0,0,
640128,582572,1937068,583884,1802956,0,0,0,2949632,4882944,4809728,6062592,7297024,0,0,0,3047936,4883968,4810752,6334976,7298048,0,0,0,3048960,4884992,4811776,6336000,7299072,0,0,0,3049984,5022208,4812800,6337024,7300096,0,0,0,
640256,582700,1937196,584012,1803084,0,0,0,3051008,5023232,4950016,6338048,7301120,0,0,0,3052032,5024256,4951040,6339072,7573504,0,0,0,3150336,5025280,4952064,6611456,7574528,0,0,0,3151360,5026304,4953088,6612480,7575552,0,0,0,
640384,582828,1937324,584140,1803212,0,0,0,3152384,5027328,4954112,6613504,7576576,0,0,0,3153408,5028352,4955136,6614528,7577600,0,0,0,3154432,5165568,4956160,6615552,7849984,0,0,0,3252736,5166592,5093376,6887936,7851008,0,0,0,
640512,582956,1937452,584268,1803340,0,0,0,3253760,5167616,5094400,6888960,7852032,0,0,0,3254784,5168640,5095424,6889984,7853056,0,0,0,3255808,5169664,5096448,6891008,7854080,0,0,0,3256832,5170688,5097472,6892032,8126464,0,0,0,
640640,583084,1937580,584396,1803468,0,0,0,3355136,5171712,5098496,7164416,8127488,0,0,0,3356160,5308928,5099520,7165440,8128512,0,0,0,3357184,5309952,5236736,7166464,8129536,0,0,0,3358208,5310976,5237760,7167488,8130560,0,0,0,
640768,583212,1937708,584524,1807436,0,0,0,3359232,5312000,5238784,7168512,6191360,0,0,0,3457536,5313024,5239808,7440896,6192384,0,0,0,3458560,5314048,5240832,7441920,6193408,0,0,0,3459584,5315072,5241856,7442944,6194432,0,0,0,
640896,583340,1937836,584652,1807564,0,0,0,3460608,5452288,5242880,7443968,6195456,0,0,0,3461632,5453312,5380096,7444992,6467840,0,0,0,3559936,5454336,5381120,7717376,6468864,0,0,0,3560960,5455360,5382144,7718400,6469888,0,0,0,
641024,583468,1937964,584780,1807692,0,0,0,3561984,5456384,5383168,7719424,6470912,0,0,0,3563008,5457408,5384192,7720448,6471936,0,0,0,3564032,5595648,5385216,7721472,6744320,0,0,0,3662336,5596672,5386240,7993856,6745344,0,0,0,
641152,583596,1938092,584908,1807820,0,0,0,3663360,5597696,5523456,7994880,6746368,0,0,0,3664384,5598720,5524480,7995904,6747392,0,0,0,3665408,5599744,5525504,7996928,6748416,0,0,0,3666432,5600768,5526528,7997952,7020800,0,0,0,
650240,583724,1938220,588876,1807948,0,0,0,2945792,5739008,5527552,6058752,7021824,0,0,0,2946816,5740032,5528576,6059776,7022848,0,0,0,2947840,5741056,5666816,6060800,7023872,0,0,0,2948864,5742080,5667840,6061824,7024896,0,0,0,
650368,583852,1938348,589004,1808076,0,0,0,2949888,5743104,5668864,6062848,7297280,0,0,0,3048192,5744128,5669888,6335232,7298304,0,0,0,3049216,5882368,5670912,6336256,7299328,0,0,0,3050240,5883392,5671936,6337280,7300352,0,0,0,
650496,583980,1938476,589132,1808204,0,0,0,3051264,5884416,5810176,6338304,7301376,0,0,0,3052288,5885440,5811200,6339328,7573760,0,0,0,3150592,5886464,5812224,6611712,7574784,0,0,0,3151616,5887488,5813248,6612736,7575808,0,0,0,
650624,594476,1938604,589260,1808332,0,0,0,3152640,3732224,5814272,6613760,7576832,0,0,0,3153664,3733248,5815296,6614784,7577856,0,0,0,3154688,3734272,5953536,6615808,7850240,0,0,0,3252992,3735296,5954560,6888192,7851264,0,0,0,
650752,594604,1938732,589388,1808460,0,0,0,3254016,3736320,5955584,6889216,7852288,0,0,0,3255040,3737344,5956608,6890240,7853312,0,0,0,3256064,3738368,5957632,6891264,7854336,0,0,0,3257088,3875584,5958656,6892288,8126720,0,0,0,
650880,594732,1949228,589516,1808588,0,0,0,3355392,3876608,3803392,7164672,8127744,0,0,0,3356416,3877632,3804416,7165696,8128768,0,0,0,3357440,3878656,3805440,7166720,8129792,0,0,0,3358464,3879680,3806464,7167744,8130816,0,0,0,
651008,594860,1949356,589644,1812556,0,0,0,3359488,3880704,3807488,7168768,6191616,0,0,0,3457792,3881728,3808512,7441152,6192640,0,0,0,3458816,4018944,3809536,7442176,6193664,0,0,0,3459840,4019968,3946752,7443200,6194688,0,0,0,
651136,594988,1949484,589772,1812684,0,0,0,3460864,4020992,3947776,7444224,6195712,0,0,0,3461888,4022016,3948800,7445248,6468096,0,0,0,3560192,4023040,3949824,7717632,6469120,0,0,0,3561216,4024064,3950848,7718656,6470144,0,0,0,
651264,595116,1949612,589900,1812812,0,0,0,3562240,4025088,3951872,7719680,6471168,0,0,0,3563264,4162304,3952896,7720704,6472192,0,0,0,3564288,4163328,4090112,7721728,6744576,0,0,0,3662592,4164352,4091136,7994112,6745600,0,0,0,
651392,595244,1949740,590028,1812940,0,0,0,3663616,4165376,4092160,7995136,6746624,0,0,0,3664640,4166400,4093184,7996160,6747648,0,0,0,3665664,4167424,4094208,7997184,6748672,0,0,0,3666688,4168448,4095232,7998208,7021056,0,0,0,
665600,595372,1949868,614476,1813068,0,0,0,2950144,4305664,4096256,6063104,7022080,0,0,0,2951168,4306688,4233472,6064128,7023104,0,0,0,2952192,4307712,4234496,6065152,7024128,0,0,0,2953216,4308736,4235520,6066176,7025152,0,0,0,
665728,595500,1949996,614604,1813196,0,0,0,2954240,4309760,4236544,6067200,7297536,0,0,0,3052544,4310784,4237568,6339584,7298560,0,0,0,3053568,4311808,4238592,6340608,7299584,0,0,0,3054592,4449024,4239616,6341632,7300608,0,0,0,
665856,595628,1950124,614732,1813324,0,0,0,3055616,4450048,4376832,6342656,7301632,0,0,0,3056640,4451072,4377856,6343680,7574016,0,0,0,3154944,4452096,4378880,6616064,7575040,0,0,0,3155968,4453120,4379904,6617088,7576064,0,0,0,
665984,595756,1950252,614860,1813452,0,0,0,3156992,4454144,4380928,6618112,7577088,0,0,0,3158016,4455168,4381952,6619136,7578112,0,0,0,3159040,4592384,4382976,6620160,7850496,0,0,0,3257344,4593408,4520192,6892544,7851520,0,0,0,
666112,595884,1950380,614988,1813580,0,0,0,3258368,4594432,4521216,6893568,7852544,0,0,0,3259392,4595456,4522240,6894592,7853568,0,0,0,3260416,4596480,4523264,6895616,7854592,0,0,0,3261440,4597504,4524288,6896640,8126976,0,0,0,
666240,596012,1950508,615116,1813708,0,0,0,3359744,4598528,4525312,7169024,8128000,0,0,0,3360768,4735744,4526336,7170048,8129024,0,0,0,3361792,4736768,4663552,7171072,8130048,0,0,0,3362816,4737792,4664576,7172096,8131072,0,0,0,
666368,596140,1950636,615244,1817676,0,0,0,3363840,4738816,4665600,7173120,6191872,0,0,0,3462144,4739840,4666624,7445504,6192896,0,0,0,3463168,4740864,4667648,7446528,6193920,0,0,0,3464192,4741888,4668672,7447552,6194944,0,0,0,
666496,596268,1950764,615372,1817804,0,0,0,3465216,4879104,4669696,7448576,6195968,0,0,0,3466240,4880128,4806912,7449600,6468352,0,0,0,3564544,4881152,4807936,7721984,6469376,0,0,0,3565568,4882176,4808960,7723008,6470400,0,0,0,
666624,596396,1950892,615500,1817932,0,0,0,3566592,4883200,4809984,7724032,6471424,0,0,0,3567616,4884224,4811008,7725056,6472448,0,0,0,3568640,4885248,4812032,7726080,6744832,0,0,0,3666944,5022464,4813056,7998464,6745856,0,0,0,
666752,596524,1951020,615628,1818060,0,0,0,3667968,5023488,4950272,7999488,6746880,0,0,0,3668992,5024512,4951296,8000512,6747904,0,0,0,3670016,5025536,4952320,8001536,6748928,0,0,0,3671040,5026560,4953344,8002560,7021312,0,0,0,
675840,596652,1951148,619596,1818188,0,0,0,2950400,5027584,4954368,6063360,7022336,0,0,0,2951424,5028608,4955392,6064384,7023360,0,0,0,2952448,5165824,4956416,6065408,7024384,0,0,0,2953472,5166848,5093632,6066432,7025408,0,0,0,
675968,596780,1951276,619724,1818316,0,0,0,2954496,5167872,5094656,6067456,7297792,0,0,0,3052800,5168896,5095680,6339840,7298816,0,0,0,3053824,5169920,5096704,6340864,7299840,0,0,0,3054848,5170944,5097728,6341888,7300864,0,0,0,
676096,596908,1951404,619852,1818444,0,0,0,3055872,5171968,5098752,6342912,7301888,0,0,0,3056896,5309184,5099776,6343936,7574272,0,0,0,3155200,5310208,5236992,6616320,7575296,0,0,0,3156224,5311232,5238016,6617344,7576320,0,0,0,
676224,597036,1951532,619980,1818572,0,0,0,3157248,5312256,5239040,6618368,7577344,0,0,0,3158272,5313280,5240064,6619392,7578368,0,0,0,3159296,5314304,5241088,6620416,7850752,0,0,0,3257600,5315328,5242112,6892800,7851776,0,0,0,
676352,597164,1951660,620108,1818700,0,0,0,3258624,5452544,5243136,6893824,7852800,0,0,0,3259648,5453568,5380352,6894848,7853824,0,0,0,3260672,5454592,5381376,6895872,7854848,0,0,0,3261696,5455616,5382400,6896896,8127232,0,0,0,
676480,597292,1951788,620236,1818828,0,0,0,3360000,5456640,5383424,7169280,8128256,0,0,0,3361024,5457664,5384448,7170304,8129280,0,0,0,3362048,5595904,5385472,7171328,8130304,0,0,0,3363072,5596928,5386496,7172352,8131328,0,0,0,
676608,597420,1951916,620364,1843276,0,0,0,3364096,5597952,5523712,7173376,6196224,0,0,0,3462400,5598976,5524736,7445760,6197248,0,0,0,3463424,5600000,5525760,7446784,6198272,0,0,0,3464448,5601024,5526784,7447808,6199296,0,0,0,
676736,597548,1952044,620492,1843404,0,0,0,3465472,5739264,5527808,7448832,6200320,0,0,0,3466496,5740288,5528832,7449856,6472704,0,0,0,3564800,5741312,5667072,7722240,6473728,0,0,0,3565824,5742336,5668096,7723264,6474752,0,0,0,
676864,597676,1952172,620620,1843532,0,0,0,3566848,5743360,5669120,7724288,6475776,0,0,0,3567872,5744384,5670144,7725312,6476800,0,0,0,3568896,5882624,5671168,7726336,6749184,0,0,0,3667200,5883648,5672192,7998720,6750208,0,0,0,
676992,597804,1952300,620748,1843660,0,0,0,3668224,5884672,5810432,7999744,6751232,0,0,0,3669248,5885696,5811456,8000768,6752256,0,0,0,3670272,5886720,5812480,8001792,6753280,0,0,0,3671296,5887744,5813504,8002816,7025664,0,0,0,
691200,663596,1952428,624716,1843788,0,0,0,2950656,3738624,5814528,6063616,7026688,0,0,0,2951680,3739648,5815552,6064640,7027712,0,0,0,2952704,3740672,5953792,6065664,7028736,0,0,0,2953728,3741696,5954816,6066688,7029760,0,0,0,
691328,663724,1952556,624844,1843916,0,0,0,2954752,3742720,5955840,6067712,7302144,0,0,0,3053056,3743744,5956864,6340096,7303168,0,0,0,3054080,3744768,5957888,6341120,7304192,0,0,0,3055104,3881984,5958912,6342144,7305216,0,0,0,
691456,663852,1963052,624972,1844044,0,0,0,3056128,3883008,3803648,6343168,7306240,0,0,0,3057152,3884032,3804672,6344192,7578624,0,0,0,3155456,3885056,3805696,6616576,7579648,0,0,0,3156480,3886080,3806720,6617600,7580672,0,0,0,
691584,663980,1963180,625100,1844172,0,0,0,3157504,3887104,3807744,6618624,7581696,0,0,0,3158528,3888128,3808768,6619648,7582720,0,0,0,3159552,4025344,3809792,6620672,7855104,0,0,0,3257856,4026368,3947008,6893056,7856128,0,0,0,
691712,664108,1963308,625228,1844300,0,0,0,3258880,4027392,3948032,6894080,7857152,0,0,0,3259904,4028416,3949056,6895104,7858176,0,0,0,3260928,4029440,3950080,6896128,7859200,0,0,0,3261952,4030464,3951104,6897152,8131584,0,0,0,
691840,664236,1963436,625356,1844428,0,0,0,3360256,4031488,3952128,7169536,8132608,0,0,0,3361280,4168704,3953152,7170560,8133632,0,0,0,3362304,4169728,4090368,7171584,8134656,0,0,0,3363328,4170752,4091392,7172608,8135680,0,0,0,
691968,664364,1963564,625484,1848396,0,0,0,3364352,4171776,4092416,7173632,6196480,0,0,0,3462656,4172800,4093440,7446016,6197504,0,0,0,3463680,4173824,4094464,7447040,6198528,0,0,0,3464704,4174848,4095488,7448064,6199552,0,0,0,
692096,664492,1963692,625612,1848524,0,0,0,3465728,4312064,4096512,7449088,6200576,0,0,0,3466752,4313088,4233728,7450112,6472960,0,0,0,3565056,4314112,4234752,7722496,6473984,0,0,0,3566080,4315136,4235776,7723520,6475008,0,0,0,
692224,664620,1963820,625740,1848652,0,0,0,3567104,4316160,4236800,7724544,6476032,0,0,0,3568128,4317184,4237824,7725568,6477056,0,0,0,3569152,4318208,4238848,7726592,6749440,0,0,0,3667456,4455424,4239872,7998976,6750464,0,0,0,
692352,664748,1963948,625868,1848780,0,0,0,3668480,4456448,4377088,8000000,6751488,0,0,0,3669504,4457472,4378112,8001024,6752512,0,0,0,3670528,4458496,4379136,8002048,6753536,0,0,0,3671552,4459520,4380160,8003072,7025920,0,0,0,
701440,664876,1964076,629836,1848908,0,0,0,2950912,4460544,4381184,6063872,7026944,0,0,0,2951936,4461568,4382208,6064896,7027968,0,0,0,2952960,4598784,4383232,6065920,7028992,0,0,0,2953984,4599808,4520448,6066944,7030016,0,0,0,
701568,665004,1964204,629964,1849036,0,0,0,2955008,4600832,4521472,6067968,7302400,0,0,0,3053312,4601856,4522496,6340352,7303424,0,0,0,3054336,4602880,4523520,6341376,7304448,0,0,0,3055360,4603904,4524544,6342400,7305472,0,0,0,
701696,665132,1964332,630092,1849164,0,0,0,3056384,4604928,4525568,6343424,7306496,0,0,0,3057408,4742144,4526592,6344448,7578880,0,0,0,3155712,4743168,4663808,6616832,7579904,0,0,0,3156736,4744192,4664832,6617856,7580928,0,0,0,
701824,665260,1964460,630220,1849292,0,0,0,3157760,4745216,4665856,6618880,7581952,0,0,0,3158784,4746240,4666880,6619904,7582976,0,0,0,3159808,4747264,4667904,6620928,7855360,0,0,0,3258112,4748288,4668928,6893312,7856384,0,0,0,
701952,665388,1964588,630348,1849420,0,0,0,3259136,4885504,4669952,6894336,7857408,0,0,0,3260160,4886528,4807168,6895360,7858432,0,0,0,3261184,4887552,4808192,6896384,7859456,0,0,0,3262208,4888576,4809216,6897408,8131840,0,0,0,
702080,665516,1964716,630476,1849548,0,0,0,3360512,4889600,4810240,7169792,8132864,0,0,0,3361536,4890624,4811264,7170816,8133888,0,0,0,3362560,4891648,4812288,7171840,8134912,0,0,0,3363584,5028864,4813312,7172864,8135936,0,0,0,
702208,665644,1964844,630604,1853516,0,0,0,3364608,5029888,4950528,7173888,6196736,0,0,0,3462912,5030912,4951552,7446272,6197760,0,0,0,3463936,5031936,4952576,7447296,6198784,0,0,0,3464960,5032960,4953600,7448320,6199808,0,0,0,
702336,665772,1964972,630732,1853644,0,0,0,3465984,5033984,4954624,7449344,6200832,0,0,0,3467008,5035008,4955648,7450368,6473216,0,0,0,3565312,5172224,4956672,7722752,6474240,0,0,0,3566336,5173248,5093888,7723776,6475264,0,0,0,
702464,665900,1965100,630860,1853772,0,0,0,3567360,5174272,5094912,7724800,6476288,0,0,0,3568384,5175296,5095936,7725824,6477312,0,0,0,3569408,5176320,5096960,7726848,6749696,0,0,0,3667712,5177344,5097984,7999232,6750720,0,0,0,
702592,666028,1965228,630988,1853900,0,0,0,3668736,5178368,5099008,8000256,6751744,0,0,0,3669760,5315584,5100032,8001280,6752768,0,0,0,3670784,5316608,5237248,8002304,6753792,0,0,0,3671808,5317632,5238272,8003328,7026176,0,0,0,
716800,666156,1965356,655436,1854028,0,0,0,2955264,5318656,5239296,6068224,7027200,0,0,0,2956288,5319680,5240320,6069248,7028224,0,0,0,2957312,5320704,5241344,6070272,7029248,0,0,0,2958336,5321728,5242368,6071296,7030272,0,0,0,
716928,666284,1965484,655564,1854156,0,0,0,2959360,5458944,5243392,6072320,7302656,0,0,0,3057664,5459968,5380608,6344704,7303680,0,0,0,3058688,5460992,5381632,6345728,7304704,0,0,0,3059712,5462016,5382656,6346752,7305728,0,0,0,
717056,666412,1965612,655692,1854284,0,0,0,3060736,5463040,5383680,6347776,7306752,0,0,0,3061760,5464064,5384704,6348800,7579136,0,0,0,3160064,5602304,5385728,6621184,7580160,0,0,0,3161088,5603328,5386752,6622208,7581184,0,0,0,
717184,666540,1965740,655820,1854412,0,0,0,3162112,5604352,5523968,6623232,7582208,0,0,0,3163136,5605376,5524992,6624256,7583232,0,0,0,3164160,5606400,5526016,6625280,7855616,0,0,0,3262464,5607424,5527040,6897664,7856640,0,0,0,
717312,666668,1965868,655948,1854540,0,0,0,3263488,5745664,5528064,6898688,7857664,0,0,0,3264512,5746688,5529088,6899712,7858688,0,0,0,3265536,5747712,5667328,6900736,7859712,0,0,0,3266560,5748736,5668352,6901760,8132096,0,0,0,
717440,666796,1965996,656076,1854668,0,0,0,3364864,5749760,5669376,7174144,8133120,0,0,0,3365888,5750784,5670400,7175168,8134144,0,0,0,3366912,5889024,5671424,7176192,8135168,0,0,0,3367936,5890048,5672448,7177216,8136192,0,0,0,
717568,666924,1966124,656204,1858636,0,0,0,3368960,5891072,5810688,7178240,6196992,0,0,0,3467264,5892096,5811712,7450624,6198016,0,0,0,3468288,5893120,5812736,7451648,6199040,0,0,0,3469312,5894144,5813760,7452672,6200064,0,0,0,
717696,677420,1966252,656332,1858764,0,0,0,3470336,3738880,5814784,7453696,6201088,0,0,0,3471360,3739904,5815808,7454720,6473472,0,0,0,3569664,3740928,5954048,7727104,6474496,0,0,0,3570688,3741952,5955072,7728128,6475520,0,0,0,
717824,677548,1966380,656460,1858892,0,0,0,3571712,3742976,5956096,7729152,6476544,0,0,0,3572736,3744000,5957120,7730176,6477568,0,0,0,3573760,3745024,5958144,7731200,6749952,0,0,0,3672064,3882240,5959168,8003584,6750976,0,0,0,
717952,677676,1976876,656588,1859020,0,0,0,3673088,3883264,3803904,8004608,6752000,0,0,0,3674112,3884288,3804928,8005632,6753024,0,0,0,3675136,3885312,3805952,8006656,6754048,0,0,0,3676160,3886336,3806976,8007680,7026432,0,0,0,
727040,677804,1977004,660556,1859148,0,0,0,2955520,3887360,3808000,6068480,7027456,0,0,0,2956544,3888384,3809024,6069504,7028480,0,0,0,2957568,4025600,3810048,6070528,7029504,0,0,0,2958592,4026624,3947264,6071552,7030528,0,0,0,
727168,677932,1977132,660684,1859276,0,0,0,2959616,4027648,3948288,6072576,7302912,0,0,0,3057920,4028672,3949312,6344960,7303936,0,0,0,3058944,4029696,3950336,6345984,7304960,0,0,0,3059968,4030720,3951360,6347008,7305984,0,0,0,
727296,678060,1977260,660812,1859404,0,0,0,3060992,4031744,3952384,6348032,7307008,0,0,0,3062016,4168960,3953408,6349056,7579392,0,0,0,3160320,4169984,4090624,6621440,7580416,0,0,0,3161344,4171008,4091648,6622464,7581440,0,0,0,
727424,678188,1977388,660940,1859532,0,0,0,3162368,4172032,4092672,6623488,7582464,0,0,0,3163392,4173056,4093696,6624512,7583488,0,0,0,3164416,4174080,4094720,6625536,7855872,0,0,0,3262720,4175104,4095744,6897920,7856896,0,0,0,
727552,678316,1977516,661068,1859660,0,0,0,3263744,4312320,4096768,6898944,7857920,0,0,0,3264768,4313344,4233984,6899968,7858944,0,0,0,3265792,4314368,4235008,6900992,7859968,0,0,0,3266816,4315392,4236032,6902016,8132352,0,0,0,
727680,678444,1977644,661196,1859788,0,0,0,3365120,4316416,4237056,7174400,8133376,0,0,0,3366144,4317440,4238080,7175424,8134400,0,0,0,3367168,4318464,4239104,7176448,8135424,0,0,0,3368192,4455680,4240128,7177472,8136448,0,0,0,
727808,678572,1977772,661324,1884236,0,0,0,3369216,4456704,4377344,7178496,6201344,0,0,0,3467520,4457728,4378368,7450880,6202368,0,0,0,3468544,4458752,4379392,7451904,6203392,0,0,0,3469568,4459776,4380416,7452928,6204416,0,0,0,
727936,678700,1977900,661452,1884364,0,0,0,3470592,4460800,4381440,7453952,6205440,0,0,0,3471616,4461824,4382464,7454976,6477824,0,0,0,3569920,4599040,4383488,7727360,6478848,0,0,0,3570944,4600064,4520704,7728384,6479872,0,0,0,
728064,678828,1978028,661580,1884492,0,0,0,3571968,4601088,4521728,7729408,6480896,0,0,0,3572992,4602112,4522752,7730432,6481920,0,0,0,3574016,4603136,4523776,7731456,6754304,0,0,0,3672320,4604160,4524800,8003840,6755328,0,0,0,
728192,678956,1978156,661708,1884620,0,0,0,3673344,4605184,4525824,8004864,6756352,0,0,0,3674368,4742400,4526848,8005888,6757376,0,0,0,3675392,4743424,4664064,8006912,6758400,0,0,0,3676416,4744448,4665088,8007936,7030784,0,0,0,
742400,679084,1978284,665676,1884748,0,0,0,2955776,4745472,4666112,6068736,7031808,0,0,0,2956800,4746496,4667136,6069760,7032832,0,0,0,2957824,4747520,4668160,6070784,7033856,0,0,0,2958848,4748544,4669184,6071808,7034880,0,0,0,
742528,679212,1978412,665804,1884876,0,0,0,2959872,4885760,4670208,6072832,7307264,0,0,0,3058176,4886784,4807424,6345216,7308288,0,0,0,3059200,4887808,4808448,6346240,7309312,0,0,0,3060224,4888832,4809472,6347264,7310336,0,0,0,
742656,679340,1978540,665932,1885004,0,0,0,3061248,4889856,4810496,6348288,7311360,0,0,0,3062272,4890880,4811520,6349312,7583744,0,0,0,3160576,4891904,4812544,6621696,7584768,0,0,0,3161600,5029120,4813568,6622720,7585792,0,0,0,
742784,679468,1978668,666060,1885132,0,0,0,3162624,5030144,4950784,6623744,7586816,0,0,0,3163648,5031168,4951808,6624768,7587840,0,0,0,3164672,5032192,4952832,6625792,7860224,0,0,0,3262976,5033216,4953856,6898176,7861248,0,0,0,
742912,679596,1978796,666188,1885260,0,0,0,3264000,5034240,4954880,6899200,7862272,0,0,0,3265024,5035264,4955904,6900224,7863296,0,0,0,3266048,5172480,4956928,6901248,7864320,0,0,0,3267072,5173504,5094144,6902272,8136704,0,0,0,
743040,679724,1978924,666316,1885388,0,0,0,3365376,5174528,5095168,7174656,8137728,0,0,0,3366400,5175552,5096192,7175680,8138752,0,0,0,3367424,5176576,5097216,7176704,8139776,0,0,0,3368448,5177600,5098240,7177728,8140800,0,0,0,
743168,679852,1979052,666444,1889356,0,0,0,3369472,5178624,5099264,7178752,6201600,0,0,0,3467776,5315840,5100288,7451136,6202624,0,0,0,3468800,5316864,5237504,7452160,6203648,0,0,0,3469824,5317888,5238528,7453184,6204672,0,0,0,
743296,679980,1979180,666572,1889484,0,0,0,3470848,5318912,5239552,7454208,6205696,0,0,0,3471872,5319936,5240576,7455232,6478080,0,0,0,3570176,5320960,5241600,7727616,6479104,0,0,0,3571200,5321984,5242624,7728640,6480128,0,0,0,
743424,680108,1979308,666700,1889612,0,0,0,3572224,5459200,5243648,7729664,6481152,0,0,0,3573248,5460224,5380864,7730688,6482176,0,0,0,3574272,5461248,5381888,7731712,6754560,0,0,0,3672576,5462272,5382912,8004096,6755584,0,0,0,
743552,680236,1979436,666828,1889740,0,0,0,3673600,5463296,5383936,8005120,6756608,0,0,0,3674624,5464320,5384960,8006144,6757632,0,0,0,3675648,5602560,5385984,8007168,6758656,0,0,0,3676672,5603584,5387008,8008192,7031040,0,0,0,
752640,680364,1979564,670796,1889868,0,0,0,2956032,5604608,5524224,6068992,7032064,0,0,0,2957056,5605632,5525248,6070016,7033088,0,0,0,2958080,5606656,5526272,6071040,7034112,0,0,0,2959104,5607680,5527296,6072064,7035136,0,0,0,
752768,680492,1979692,670924,1889996,0,0,0,2960128,5745920,5528320,6073088,7307520,0,0,0,3058432,5746944,5529344,6345472,7308544,0,0,0,3059456,5747968,5667584,6346496,7309568,0,0,0,3060480,5748992,5668608,6347520,7310592,0,0,0,
752896,680620,1979820,671052,1890124,0,0,0,3061504,5750016,5669632,6348544,7311616,0,0,0,3062528,5751040,5670656,6349568,7584000,0,0,0,3160832,5889280,5671680,6621952,7585024,0,0,0,3161856,5890304,5672704,6622976,7586048,0,0,0,
753024,680748,1979948,671180,1890252,0,0,0,3162880,5891328,5810944,6624000,7587072,0,0,0,3163904,5892352,5811968,6625024,7588096,0,0,0,3164928,5893376,5812992,6626048,7860480,0,0,0,3263232,5894400,5814016,6898432,7861504,0,0,0,
753152,691244,1980076,671308,1890380,0,0,0,3264256,3739136,5815040,6899456,7862528,0,0,0,3265280,3740160,5816064,6900480,7863552,0,0,0,3266304,3741184,5954304,6901504,7864576,0,0,0,3267328,3742208,5955328,6902528,8136960,0,0,0,
753280,691372,1980204,671436,1890508,0,0,0,3365632,3743232,5956352,7174912,8137984,0,0,0,3366656,3744256,5957376,7175936,8139008,0,0,0,3367680,3745280,5958400,7176960,8140032,0,0,0,3368704,3882496,5959424,7177984,8141056,0,0,0,
753408,691500,2045996,671564,1894476,0,0,0,3369728,3883520,3810304,7179008,6201856,0,0,0,3468032,3884544,3811328,7451392,6202880,0,0,0,3469056,3885568,3812352,7452416,6203904,0,0,0,3470080,3886592,3813376,7453440,6204928,0,0,0,
753536,691628,2046124,671692,1894604,0,0,0,3471104,3887616,3814400,7454464,6205952,0,0,0,3472128,3888640,3815424,7455488,6478336,0,0,0,3570432,4025856,3816448,7727872,6479360,0,0,0,3571456,4026880,3953664,7728896,6480384,0,0,0,
753664,691756,2046252,671820,1894732,0,0,0,3572480,4027904,3954688,7729920,6481408,0,0,0,3573504,4028928,3955712,7730944,6482432,0,0,0,3574528,4029952,3956736,7731968,6754816,0,0,0,3672832,4030976,3957760,8004352,6755840,0,0,0,
753792,691884,2046380,671948,1894860,0,0,0,3673856,4032000,3958784,8005376,6756864,0,0,0,3674880,4169216,3959808,8006400,6757888,0,0,0,3675904,4170240,4097024,8007424,6758912,0,0,0,3676928,4171264,4098048,8008448,7031296,0,0,0,
768000,692012,2046508,716876,1894988,0,0,0,2960384,4172288,4099072,6073344,7032320,0,0,0,2961408,4173312,4100096,6074368,7033344,0,0,0,2962432,4174336,4101120,6075392,7034368,0,0,0,2963456,4175360,4102144,6076416,7035392,0,0,0,
768128,692140,2046636,717004,1895116,0,0,0,2964480,4312576,4103168,6077440,7307776,0,0,0,3062784,4313600,4240384,6349824,7308800,0,0,0,3063808,4314624,4241408,6350848,7309824,0,0,0,3064832,4315648,4242432,6351872,7310848,0,0,0,
768256,692268,2046764,717132,1895244,0,0,0,3065856,4316672,4243456,6352896,7311872,0,0,0,3066880,4317696,4244480,6353920,7584256,0,0,0,3165184,4318720,4245504,6626304,7585280,0,0,0,3166208,4455936,4246528,6627328,7586304,0,0,0,
768384,692396,2046892,717260,1895372,0,0,0,3167232,4456960,4383744,6628352,7587328,0,0,0,3168256,4457984,4384768,6629376,7588352,0,0,0,3169280,4459008,4385792,6630400,7860736,0,0,0,3267584,4460032,4386816,6902784,7861760,0,0,0,
768512,692524,2047020,717388,1895500,0,0,0,3268608,4461056,4387840,6903808,7862784,0,0,0,3269632,4462080,4388864,6904832,7863808,0,0,0,3270656,4599296,4389888,6905856,7864832,0,0,0,3271680,4600320,4527104,6906880,8137216,0,0,0,
768640,692652,2047148,717516,1895628,0,0,0,3369984,4601344,4528128,7179264,8138240,0,0,0,3371008,4602368,4529152,7180288,8139264,0,0,0,3372032,4603392,4530176,7181312,8140288,0,0,0,3373056,4604416,4531200,7182336,8141312,0,0,0,
768768,692780,2047276,717644,1899596,0,0,0,3374080,4605440,4532224,7183360,6202112,0,0,0,3472384,4742656,4533248,7455744,6203136,0,0,0,3473408,4743680,4670464,7456768,6204160,0,0,0,3474432,4744704,4671488,7457792,6205184,0,0,0,
768896,692908,2047404,717772,1899724,0,0,0,3475456,4745728,4672512,7458816,6206208,0,0,0,3476480,4746752,4673536,7459840,6478592,0,0,0,3574784,4747776,4674560,7732224,6479616,0,0,0,3575808,4748800,4675584,7733248,6480640,0,0,0,
769024,693036,2047532,717900,1899852,0,0,0,3576832,4886016,4676608,7734272,6481664,0,0,0,3577856,4887040,4813824,7735296,6482688,0,0,0,3578880,4888064,4814848,7736320,6755072,0,0,0,3677184,4889088,4815872,8008704,6756096,0,0,0,
769152,693164,2047660,718028,1899980,0,0,0,3678208,4890112,4816896,8009728,6757120,0,0,0,3679232,4891136,4817920,8010752,6758144,0,0,0,3680256,4892160,4818944,8011776,6759168,0,0,0,3681280,5029376,4819968,8012800,7031552,0,0,0,
778240,693292,2047788,721996,1900108,0,0,0,2960640,5030400,4957184,6073600,7032576,0,0,0,2961664,5031424,4958208,6074624,7033600,0,0,0,2962688,5032448,4959232,6075648,7034624,0,0,0,2963712,5033472,4960256,6076672,7035648,0,0,0,
778368,693420,2047916,722124,1900236,0,0,0,2964736,5034496,4961280,6077696,7308032,0,0,0,3063040,5035520,4962304,6350080,7309056,0,0,0,3064064,5172736,4963328,6351104,7310080,0,0,0,3065088,5173760,5100544,6352128,7311104,0,0,0,
778496,693548,2048044,722252,1900364,0,0,0,3066112,5174784,5101568,6353152,7312128,0,0,0,3067136,5175808,5102592,6354176,7584512,0,0,0,3165440,5176832,5103616,6626560,7585536,0,0,0,3166464,5177856,5104640,6627584,7586560,0,0,0,
778624,693676,2048172,722380,1900492,0,0,0,3167488,5178880,5105664,6628608,7587584,0,0,0,3168512,5316096,5106688,6629632,7588608,0,0,0,3169536,5317120,5243904,6630656,7860992,0,0,0,3267840,5318144,5244928,6903040,7862016,0,0,0,
778752,693804,2048300,722508,1900620,0,0,0,3268864,5319168,5245952,6904064,7863040,0,0,0,3269888,5320192,5246976,6905088,7864064,0,0,0,3270912,5321216,5248000,6906112,7865088,0,0,0,3271936,5322240,5249024,6907136,8137472,0,0,0,
778880,693932,2048428,722636,1900748,0,0,0,3370240,5459456,5250048,7179520,8138496,0,0,0,3371264,5460480,5387264,7180544,8139520,0,0,0,3372288,5461504,5388288,7181568,8140544,0,0,0,3373312,5462528,5389312,7182592,8141568,0,0,0,
779008,694060,2048556,722764,1925196,0,0,0,3374336,5463552,5390336,7183616,6206464,0,0,0,3472640,5464576,5391360,7456000,6207488,0,0,0,3473664,5602816,5392384,7457024,6208512,0,0,0,3474688,5603840,5393408,7458048,6209536,0,0,0,
779136,694188,2048684,722892,1925324,0,0,0,3475712,5604864,5530624,7459072,6210560,0,0,0,3476736,5605888,5531648,7460096,6482944,0,0,0,3575040,5606912,5532672,7732480,6483968,0,0,0,3576064,5607936,5533696,7733504,6484992,0,0,0,
779264,694316,2048812,723020,1925452,0,0,0,3577088,5746176,5534720,7734528,6486016,0,0,0,3578112,5747200,5535744,7735552,6487040,0,0,0,3579136,5748224,5673984,7736576,6759424,0,0,0,3677440,5749248,5675008,8008960,6760448,0,0,0,
779392,694444,2048940,723148,1925580,0,0,0,3678464,5750272,5676032,8009984,6761472,0,0,0,3679488,5751296,5677056,8011008,6762496,0,0,0,3680512,5889536,5678080,8012032,6763520,0,0,0,3681536,5890560,5679104,8013056,7035904,0,0,0,
793600,694572,2049068,727116,1925708,0,0,0,2960896,5891584,5817344,6073856,7036928,0,0,0,2961920,5892608,5818368,6074880,7037952,0,0,0,2962944,5893632,5819392,6075904,7038976,0,0,0,2963968,5894656,5820416,6076928,7040000,0,0,0,
793728,705068,2049196,727244,1925836,0,0,0,2964992,3739392,5821440,6077952,7312384,0,0,0,3063296,3740416,5822464,6350336,7313408,0,0,0,3064320,3741440,5960704,6351360,7314432,0,0,0,3065344,3742464,5961728,6352384,7315456,0,0,0,
793856,705196,2049324,727372,1925964,0,0,0,3066368,3743488,5962752,6353408,7316480,0,0,0,3067392,3744512,5963776,6354432,7588864,0,0,0,3165696,3745536,5964800,6626816,7589888,0,0,0,3166720,3882752,5965824,6627840,7590912,0,0,0,
793984,705324,2059820,727500,1926092,0,0,0,3167744,3883776,3810560,6628864,7591936,0,0,0,3168768,3884800,3811584,6629888,7592960,0,0,0,3169792,3885824,3812608,6630912,7865344,0,0,0,3268096,3886848,3813632,6903296,7866368,0,0,0,
794112,705452,2059948,727628,1926220,0,0,0,3269120,3887872,3814656,6904320,7867392,0,0,0,3270144,3888896,3815680,6905344,7868416,0,0,0,3271168,4026112,3816704,6906368,7869440,0,0,0,3272192,4027136,3953920,6907392,8141824,0,0,0,
794240,705580,2060076,727756,1926348,0,0,0,3370496,4028160,3954944,7179776,8142848,0,0,0,3371520,4029184,3955968,7180800,8143872,0,0,0,3372544,4030208,3956992,7181824,8144896,0,0,0,3373568,4031232,3958016,7182848,8145920,0,0,0,
794368,705708,2060204,727884,1930316,0,0,0,3374592,4032256,3959040,7183872,6206720,0,0,0,3472896,4169472,3960064,7456256,6207744,0,0,0,3473920,4170496,4097280,7457280,6208768,0,0,0,3474944,4171520,4098304,7458304,6209792,0,0,0,
794496,705836,2060332,728012,1930444,0,0,0,3475968,4172544,4099328,7459328,6210816,0,0,0,3476992,4173568,4100352,7460352,6483200,0,0,0,3575296,4174592,4101376,7732736,6484224,0,0,0,3576320,4175616,4102400,7733760,6485248,0,0,0,
794624,705964,2060460,728140,1930572,0,0,0,3577344,4312832,4103424,7734784,6486272,0,0,0,3578368,4313856,4240640,7735808,6487296,0,0,0,3579392,4314880,4241664,7736832,6759680,0,0,0,3677696,4315904,4242688,8009216,6760704,0,0,0,
794752,706092,2060588,728268,1930700,0,0,0,3678720,4316928,4243712,8010240,6761728,0,0,0,3679744,4317952,4244736,8011264,6762752,0,0,0,3680768,4318976,4245760,8012288,6763776,0,0,0,3681792,4456192,4246784,8013312,7036160,0,0,0,
803840,706220,2060716,732236,1930828,0,0,0,2961152,4457216,4384000,6074112,7037184,0,0,0,2962176,4458240,4385024,6075136,7038208,0,0,0,2963200,4459264,4386048,6076160,7039232,0,0,0,2964224,4460288,4387072,6077184,7040256,0,0,0,
803968,706348,2060844,732364,1930956,0,0,0,2965248,4461312,4388096,6078208,7312640,0,0,0,3063552,4462336,4389120,6350592,7313664,0,0,0,3064576,4599552,4390144,6351616,7314688,0,0,0,3065600,4600576,4527360,6352640,7315712,0,0,0,
804096,706476,2060972,732492,1931084,0,0,0,3066624,4601600,4528384,6353664,7316736,0,0,0,3067648,4602624,4529408,6354688,7589120,0,0,0,3165952,4603648,4530432,6627072,7590144,0,0,0,3166976,4604672,4531456,6628096,7591168,0,0,0,
804224,706604,2061100,732620,1931212,0,0,0,3168000,4605696,4532480,6629120,7592192,0,0,0,3169024,4742912,4533504,6630144,7593216,0,0,0,3170048,4743936,4670720,6631168,7865600,0,0,0,3268352,4744960,4671744,6903552,7866624,0,0,0,
804352,706732,2061228,732748,1931340,0,0,0,3269376,4745984,4672768,6904576,7867648,0,0,0,3270400,4747008,4673792,6905600,7868672,0,0,0,3271424,4748032,4674816,6906624,7869696,0,0,0,3272448,4749056,4675840,6907648,8142080,0,0,0,
804480,706860,2061356,732876,1931468,0,0,0,3370752,4886272,4676864,7180032,8143104,0,0,0,3371776,4887296,4814080,7181056,8144128,0,0,0,3372800,4888320,4815104,7182080,8145152,0,0,0,3373824,4889344,4816128,7183104,8146176,0,0,0,
804608,706988,2061484,733004,1935436,0,0,0,3374848,4890368,4817152,7184128,6206976,0,0,0,3473152,4891392,4818176,7456512,6208000,0,0,0,3474176,4892416,4819200,7457536,6209024,0,0,0,3475200,5029632,4820224,7458560,6210048,0,0,0,
804736,707116,2061612,733132,1935564,0,0,0,3476224,5030656,4957440,7459584,6211072,0,0,0,3477248,5031680,4958464,7460608,6483456,0,0,0,3575552,5032704,4959488,7732992,6484480,0,0,0,3576576,5033728,4960512,7734016,6485504,0,0,0,
804864,707244,2061740,733260,1935692,0,0,0,3577600,5034752,4961536,7735040,6486528,0,0,0,3578624,5035776,4962560,7736064,6487552,0,0,0,3579648,5172992,4963584,7737088,6759936,0,0,0,3677952,5174016,5100800,8009472,6760960,0,0,0,
804992,707372,2061868,733388,1935820,0,0,0,3678976,5175040,5101824,8010496,6761984,0,0,0,3680000,5176064,5102848,8011520,6763008,0,0,0,3681024,5177088,5103872,8012544,6764032,0,0,0,3682048,5178112,5104896,8013568,7036416,0,0,0,
20496,707500,2061996,757836,1935948,0,0,0,2965504,5179136,5105920,6078464,7037440,0,0,0,2966528,5316352,5106944,6079488,7038464,0,0,0,2967552,5317376,5244160,6080512,7039488,0,0,0,2968576,5318400,5245184,6081536,7040512,0,0,0,
20624,707628,2062124,757964,1936076,0,0,0,2969600,5319424,5246208,6082560,7312896,0,0,0,3067904,5320448,5247232,6354944,7313920,0,0,0,3068928,5321472,5248256,6355968,7314944,0,0,0,3069952,5322496,5249280,6356992,7315968,0,0,0,
20752,707756,2062252,758092,1936204,0,0,0,3070976,5459712,5250304,6358016,7316992,0,0,0,3072000,5460736,5387520,6359040,7589376,0,0,0,3170304,5461760,5388544,6631424,7590400,0,0,0,3171328,5462784,5389568,6632448,7591424,0,0,0,
20880,707884,2062380,758220,1936332,0,0,0,3172352,5463808,5390592,6633472,7592448,0,0,0,3173376,5464832,5391616,6634496,7593472,0,0,0,3174400,5603072,5392640,6635520,7865856,0,0,0,3272704,5604096,5393664,6907904,7866880,0,0,0,
21008,708012,2062508,758348,1936460,0,0,0,3273728,5605120,5530880,6908928,7867904,0,0,0,3274752,5606144,5531904,6909952,7868928,0,0,0,3275776,5607168,5532928,6910976,7869952,0,0,0,3276800,5608192,5533952,6912000,8142336,0,0,0,
21136,708140,2062636,758476,1936588,0,0,0,3375104,5746432,5534976,7184384,8143360,0,0,0,3376128,5747456,5536000,7185408,8144384,0,0,0,3377152,5748480,5674240,7186432,8145408,0,0,0,3378176,5749504,5675264,7187456,8146432,0,0,0,
21264,708268,2062764,758604,1940556,0,0,0,3379200,5750528,5676288,7188480,6207232,0,0,0,3477504,5751552,5677312,7460864,6208256,0,0,0,3478528,5889792,5678336,7461888,6209280,0,0,0,3479552,5890816,5679360,7462912,6210304,0,0,0,
21392,708396,2062892,758732,1940684,0,0,0,3480576,5891840,5817600,7463936,6211328,0,0,0,3481600,5892864,5818624,7464960,6483712,0,0,0,3579904,5893888,5819648,7737344,6484736,0,0,0,3580928,5894912,5820672,7738368,6485760,0,0,0,
21520,829484,2063020,758860,1940812,0,0,0,3581952,3745792,5821696,7739392,6486784,0,0,0,3582976,3746816,5822720,7740416,6487808,0,0,0,3584000,3747840,5960960,7741440,6760192,0,0,0,3682304,3748864,5961984,8013824,6761216,0,0,0,
21648,829612,2063148,758988,1940940,0,0,0,3683328,3749888,5963008,8014848,6762240,0,0,0,3684352,3750912,5964032,8015872,6763264,0,0,0,3685376,3751936,5965056,8016896,6764288,0,0,0,3686400,3889152,5966080,8017920,7036672,0,0,0,
71696,829740,2073644,762956,1941068,0,0,0,2965760,3890176,3810816,6078720,7037696,0,0,0,2966784,3891200,3811840,6079744,7038720,0,0,0,2967808,3892224,3812864,6080768,7039744,0,0,0,2968832,3893248,3813888,6081792,7040768,0,0,0,
71824,829868,2073772,763084,1941196,0,0,0,2969856,3894272,3814912,6082816,7313152,0,0,0,3068160,3895296,3815936,6355200,7314176,0,0,0,3069184,4032512,3816960,6356224,7315200,0,0,0,3070208,4033536,3954176,6357248,7316224,0,0,0,
71952,829996,2073900,763212,1941324,0,0,0,3071232,4034560,3955200,6358272,7317248,0,0,0,3072256,4035584,3956224,6359296,7589632,0,0,0,3170560,4036608,3957248,6631680,7590656,0,0,0,3171584,4037632,3958272,6632704,7591680,0,0,0,
72080,830124,2074028,763340,1941452,0,0,0,3172608,4038656,3959296,6633728,7592704,0,0,0,3173632,4175872,3960320,6634752,7593728,0,0,0,3174656,4176896,4097536,6635776,7866112,0,0,0,3272960,4177920,4098560,6908160,7867136,0,0,0,
72208,830252,2074156,763468,1941580,0,0,0,3273984,4178944,4099584,6909184,7868160,0,0,0,3275008,4179968,4100608,6910208,7869184,0,0,0,3276032,4180992,4101632,6911232,7870208,0,0,0,3277056,4182016,4102656,6912256,8142592,0,0,0,
72336,830380,2074284,763596,1941708,0,0,0,3375360,4319232,4103680,7184640,8143616,0,0,0,3376384,4320256,4240896,7185664,8144640,0,0,0,3377408,4321280,4241920,7186688,8145664,0,0,0,3378432,4322304,4242944,7187712,8146688,0,0,0,
72464,830508,2074412,763724,1966156,0,0,0,3379456,4323328,4243968,7188736,6211584,0,0,0,3477760,4324352,4244992,7461120,6212608,0,0,0,3478784,4325376,4246016,7462144,6213632,0,0,0,3479808,4462592,4247040,7463168,6214656,0,0,0,
72592,830636,2074540,763852,1966284,0,0,0,3480832,4463616,4384256,7464192,6215680,0,0,0,3481856,4464640,4385280,7465216,6488064,0,0,0,3580160,4465664,4386304,7737600,6489088,0,0,0,3581184,4466688,4387328,7738624,6490112,0,0,0,
72720,830764,2074668,763980,1966412,0,0,0,3582208,4467712,4388352,7739648,6491136,0,0,0,3583232,4468736,4389376,7740672,6492160,0,0,0,3584256,4605952,4390400,7741696,6764544,0,0,0,3682560,4606976,4527616,8014080,6765568,0,0,0,
72848,830892,2074796,764108,1966540,0,0,0,3683584,4608000,4528640,8015104,6766592,0,0,0,3684608,4609024,4529664,8016128,6767616,0,0,0,3685632,4610048,4530688,8017152,6768640,0,0,0,3686656,4611072,4531712,8018176,7041024,0,0,0,
122896,831020,2074924,768076,1966668,0,0,0,2966016,4612096,4532736,6078976,7042048,0,0,0,2967040,4749312,4533760,6080000,7043072,0,0,0,2968064,4750336,4670976,6081024,7044096,0,0,0,2969088,4751360,4672000,6082048,7045120,0,0,0,
123024,831148,2075052,768204,1966796,0,0,0,2970112,4752384,4673024,6083072,7317504,0,0,0,3068416,4753408,4674048,6355456,7318528,0,0,0,3069440,4754432,4675072,6356480,7319552,0,0,0,3070464,4755456,4676096,6357504,7320576,0,0,0,
123152,831276,2075180,768332,1966924,0,0,0,3071488,4892672,4677120,6358528,7321600,0,0,0,3072512,4893696,4814336,6359552,7593984,0,0,0,3170816,4894720,4815360,6631936,7595008,0,0,0,3171840,4895744,4816384,6632960,7596032,0,0,0,
123280,831404,2075308,768460,1967052,0,0,0,3172864,4896768,4817408,6633984,7597056,0,0,0,3173888,4897792,4818432,6635008,7598080,0,0,0,3174912,4898816,4819456,6636032,7870464,0,0,0,3273216,5036032,4820480,6908416,7871488,0,0,0,
123408,831532,2075436,768588,1967180,0,0,0,3274240,5037056,4957696,6909440,7872512,0,0,0,3275264,5038080,4958720,6910464,7873536,0,0,0,3276288,5039104,4959744,6911488,7874560,0,0,0,3277312,5040128,4960768,6912512,8146944,0,0,0,
123536,831660,2075564,768716,1967308,0,0,0,3375616,5041152,4961792,7184896,8147968,0,0,0,3376640,5042176,4962816,7185920,8148992,0,0,0,3377664,5179392,4963840,7186944,8150016,0,0,0,3378688,5180416,5101056,7187968,8151040,0,0,0,
123664,831788,2075692,768844,1971276,0,0,0,3379712,5181440,5102080,7188992,6211840,0,0,0,3478016,5182464,5103104,7461376,6212864,0,0,0,3479040,5183488,5104128,7462400,6213888,0,0,0,3480064,5184512,5105152,7463424,6214912,0,0,0,
123792,831916,2075820,768972,1971404,0,0,0,3481088,5185536,5106176,7464448,6215936,0,0,0,3482112,5322752,5107200,7465472,6488320,0,0,0,3580416,5323776,5244416,7737856,6489344,0,0,0,3581440,5324800,5245440,7738880,6490368,0,0,0,
123920,832044,2075948,769100,1971532,0,0,0,3582464,5325824,5246464,7739904,6491392,0,0,0,3583488,5326848,5247488,7740928,6492416,0,0,0,3584512,5327872,5248512,7741952,6764800,0,0,0,3682816,5328896,5249536,8014336,6765824,0,0,0,
124048,832172,2076076,769228,1971660,0,0,0,3683840,5466112,5250560,8015360,6766848,0,0,0,3684864,5467136,5387776,8016384,6767872,0,0,0,3685888,5468160,5388800,8017408,6768896,0,0,0,3686912,5469184,5389824,8018432,7041280,0,0,0,
174096,832300,2076204,773196,1971788,0,0,0,2966272,5470208,5390848,6079232,7042304,0,0,0,2967296,5471232,5391872,6080256,7043328,0,0,0,2968320,5609472,5392896,6081280,7044352,0,0,0,2969344,5610496,5393920,6082304,7045376,0,0,0,
174224,832428,2076332,773324,1971916,0,0,0,2970368,5611520,5531136,6083328,7317760,0,0,0,3068672,5612544,5532160,6355712,7318784,0,0,0,3069696,5613568,5533184,6356736,7319808,0,0,0,3070720,5614592,5534208,6357760,7320832,0,0,0,
174352,832556,2076460,773452,1972044,0,0,0,3071744,5752832,5535232,6358784,7321856,0,0,0,3072768,5753856,5536256,6359808,7594240,0,0,0,3171072,5754880,5674496,6632192,7595264,0,0,0,3172096,5755904,5675520,6633216,7596288,0,0,0,
174480,832684,2076588,773580,1972172,0,0,0,3173120,5756928,5676544,6634240,7597312,0,0,0,3174144,5757952,5677568,6635264,7598336,0,0,0,3175168,5896192,5678592,6636288,7870720,0,0,0,3273472,5897216,5679616,6908672,7871744,0,0,0,
174608,832812,2076716,773708,1972300,0,0,0,3274496,5898240,5817856,6909696,7872768,0,0,0,3275520,5899264,5818880,6910720,7873792,0,0,0,3276544,5900288,5819904,6911744,7874816,0,0,0,3277568,5901312,5820928,6912768,8147200,0,0,0,
174736,843308,2076844,773836,1972428,0,0,0,3375872,3746048,5821952,7185152,8148224,0,0,0,3376896,3747072,5822976,7186176,8149248,0,0,0,3377920,3748096,5961216,7187200,8150272,0,0,0,3378944,3749120,5962240,7188224,8151296,0,0,0,
174864,843436,2076972,773964,1976396,0,0,0,3379968,3750144,5963264,7189248,6212096,0,0,0,3478272,3751168,5964288,7461632,6213120,0,0,0,3479296,3752192,5965312,7462656,6214144,0,0,0,3480320,3889408,5966336,7463680,6215168,0,0,0,
174992,843564,2087468,774092,1976524,0,0,0,3481344,3890432,3811072,7464704,6216192,0,0,0,3482368,3891456,3812096,7465728,6488576,0,0,0,3580672,3892480,3813120,7738112,6489600,0,0,0,3581696,3893504,3814144,7739136,6490624,0,0,0,
175120,843692,2087596,774220,1976652,0,0,0,3582720,3894528,3815168,7740160,6491648,0,0,0,3583744,3895552,3816192,7741184,6492672,0,0,0,3584768,4032768,3817216,7742208,6765056,0,0,0,3683072,4033792,3954432,8014592,6766080,0,0,0,
175248,843820,2087724,774348,1976780,0,0,0,3684096,4034816,3955456,8015616,6767104,0,0,0,3685120,4035840,3956480,8016640,6768128,0,0,0,3686144,4036864,3957504,8017664,6769152,0,0,0,3687168,4037888,3958528,8018688,7041536,0,0,0,
225296,843948,2087852,798796,1976908,0,0,0,2970624,4038912,3959552,6083584,7042560,0,0,0,2971648,4176128,3960576,6084608,7043584,0,0,0,2972672,4177152,4097792,6085632,7044608,0,0,0,2973696,4178176,4098816,6086656,7045632,0,0,0,
225424,844076,2087980,798924,1977036,0,0,0,2974720,4179200,4099840,6087680,7318016,0,0,0,3073024,4180224,4100864,6360064,7319040,0,0,0,3074048,4181248,4101888,6361088,7320064,0,0,0,3075072,4182272,4102912,6362112,7321088,0,0,0,
225552,844204,2088108,799052,1977164,0,0,0,3076096,4319488,4103936,6363136,7322112,0,0,0,3077120,4320512,4241152,6364160,7594496,0,0,0,3175424,4321536,4242176,6636544,7595520,0,0,0,3176448,4322560,4243200,6637568,7596544,0,0,0,
225680,844332,2088236,799180,1977292,0,0,0,3177472,4323584,4244224,6638592,7597568,0,0,0,3178496,4324608,4245248,6639616,7598592,0,0,0,3179520,4325632,4246272,6640640,7870976,0,0,0,3277824,4462848,4247296,6913024,7872000,0,0,0,
225808,844460,2088364,799308,1977420,0,0,0,3278848,4463872,4384512,6914048,7873024,0,0,0,3279872,4464896,4385536,6915072,7874048,0,0,0,3280896,4465920,4386560,6916096,7875072,0,0,0,3281920,4466944,4387584,6917120,8147456,0,0,0,
225936,844588,2088492,799436,1977548,0,0,0,3380224,4467968,4388608,7189504,8148480,0,0,0,3381248,4468992,4389632,7190528,8149504,0,0,0,3382272,4606208,4390656,7191552,8150528,0,0,0,3383296,4607232,4527872,7192576,8151552,0,0,0,
226064,844716,2088620,799564,1981516,0,0,0,3384320,4608256,4528896,7193600,6212352,0,0,0,3482624,4609280,4529920,7465984,6213376,0,0,0,3483648,4610304,4530944,7467008,6214400,0,0,0,3484672,4611328,4531968,7468032,6215424,0,0,0,
226192,844844,2088748,799692,1981644,0,0,0,3485696,4612352,4532992,7469056,6216448,0,0,0,3486720,4749568,4534016,7470080,6488832,0,0,0,3585024,4750592,4671232,7742464,6489856,0,0,0,3586048,4751616,4672256,7743488,6490880,0,0,0,
226320,844972,2088876,799820,1981772,0,0,0,3587072,4752640,4673280,7744512,6491904,0,0,0,3588096,4753664,4674304,7745536,6492928,0,0,0,3589120,4754688,4675328,7746560,6765312,0,0,0,3687424,4755712,4676352,8018944,6766336,0,0,0,
226448,845100,2089004,799948,1981900,0,0,0,3688448,4892928,4677376,8019968,6767360,0,0,0,3689472,4893952,4814592,8020992,6768384,0,0,0,3690496,4894976,4815616,8022016,6769408,0,0,0,3691520,4896000,4816640,8023040,7041792,0,0,0,
276496,845228,2089132,803916,1982028,0,0,0,2970880,4897024,4817664,6083840,7042816,0,0,0,2971904,4898048,4818688,6084864,7043840,0,0,0,2972928,4899072,4819712,6085888,7044864,0,0,0,2973952,5036288,4820736,6086912,7045888,0,0,0,
276624,845356,2089260,804044,1982156,0,0,0,2974976,5037312,4957952,6087936,7318272,0,0,0,3073280,5038336,4958976,6360320,7319296,0,0,0,3074304,5039360,4960000,6361344,7320320,0,0,0,3075328,5040384,4961024,6362368,7321344,0,0,0,
276752,845484,2089388,804172,1982284,0,0,0,3076352,5041408,4962048,6363392,7322368,0,0,0,3077376,5042432,4963072,6364416,7594752,0,0,0,3175680,5179648,4964096,6636800,7595776,0,0,0,3176704,5180672,5101312,6637824,7596800,0,0,0,
276880,845612,2089516,804300,1982412,0,0,0,3177728,5181696,5102336,6638848,7597824,0,0,0,3178752,5182720,5103360,6639872,7598848,0,0,0,3179776,5183744,5104384,6640896,7871232,0,0,0,3278080,5184768,5105408,6913280,7872256,0,0,0,
277008,845740,2089644,804428,1982540,0,0,0,3279104,5185792,5106432,6914304,7873280,0,0,0,3280128,5323008,5107456,6915328,7874304,0,0,0,3281152,5324032,5244672,6916352,7875328,0,0,0,3282176,5325056,5245696,6917376,8147712,0,0,0,
277136,845868,2089772,804556,1982668,0,0,0,3380480,5326080,5246720,7189760,8148736,0,0,0,3381504,5327104,5247744,7190784,8149760,0,0,0,3382528,5328128,5248768,7191808,8150784,0,0,0,3383552,5329152,5249792,7192832,8151808,0,0,0,
277264,845996,2089900,804684,2007116,0,0,0,3384576,5466368,5250816,7193856,6216704,0,0,0,3482880,5467392,5388032,7466240,6217728,0,0,0,3483904,5468416,5389056,7467264,6218752,0,0,0,3484928,5469440,5390080,7468288,6219776,0,0,0,
277392,846124,2090028,804812,2007244,0,0,0,3485952,5470464,5391104,7469312,6220800,0,0,0,3486976,5471488,5392128,7470336,6493184,0,0,0,3585280,5609728,5393152,7742720,6494208,0,0,0,3586304,5610752,5394176,7743744,6495232,0,0,0,
277520,846252,2090156,804940,2007372,0,0,0,3587328,5611776,5531392,7744768,6496256,0,0,0,3588352,5612800,5532416,7745792,6497280,0,0,0,3589376,5613824,5533440,7746816,6769664,0,0,0,3687680,5614848,5534464,8019200,6770688,0,0,0,
277648,846380,2090284,805068,2007500,0,0,0,3688704,5753088,5535488,8020224,6771712,0,0,0,3689728,5754112,5536512,8021248,6772736,0,0,0,3690752,5755136,5674752,8022272,6773760,0,0,0,3691776,5756160,5675776,8023296,7046144,0,0,0,
327696,846508,2090412,809036,2007628,0,0,0,2971136,5757184,5676800,6084096,7047168,0,0,0,2972160,5758208,5677824,6085120,7048192,0,0,0,2973184,5896448,5678848,6086144,7049216,0,0,0,2974208,5897472,5679872,6087168,7050240,0,0,0,
327824,846636,2090540,809164,2007756,0,0,0,2975232,5898496,5818112,6088192,7322624,0,0,0,3073536,5899520,5819136,6360576,7323648,0,0,0,3074560,5900544,5820160,6361600,7324672,0,0,0,3075584,5901568,5821184,6362624,7325696,0,0,0,
327952,857132,2090668,809292,2007884,0,0,0,3076608,3746304,5822208,6363648,7326720,0,0,0,3077632,3747328,5823232,6364672,7599104,0,0,0,3175936,3748352,5961472,6637056,7600128,0,0,0,3176960,3749376,5962496,6638080,7601152,0,0,0,
328080,857260,2090796,809420,2008012,0,0,0,3177984,3750400,5963520,6639104,7602176,0,0,0,3179008,3751424,5964544,6640128,7603200,0,0,0,3180032,3752448,5965568,6641152,7875584,0,0,0,3278336,3889664,5966592,6913536,7876608,0,0,0,
328208,857388,221216,809548,2008140,0,0,0,3279360,3890688,3817472,6914560,7877632,0,0,0,3280384,3891712,3818496,6915584,7878656,0,0,0,3281408,3892736,3819520,6916608,7879680,0,0,0,3282432,3893760,3820544,6917632,8152064,0,0,0,
328336,857516,221344,809676,2008268,0,0,0,3380736,3894784,3821568,7190016,8153088,0,0,0,3381760,3895808,3822592,7191040,8154112,0,0,0,3382784,4033024,3823616,7192064,8155136,0,0,0,3383808,4034048,3960832,7193088,8156160,0,0,0,
328464,857644,221472,809804,2012236,0,0,0,3384832,4035072,3961856,7194112,6216960,0,0,0,3483136,4036096,3962880,7466496,6217984,0,0,0,3484160,4037120,3963904,7467520,6219008,0,0,0,3485184,4038144,3964928,7468544,6220032,0,0,0,
328592,857772,221600,809932,2012364,0,0,0,3486208,4039168,3965952,7469568,6221056,0,0,0,3487232,4176384,3966976,7470592,6493440,0,0,0,3585536,4177408,4104192,7742976,6494464,0,0,0,3586560,4178432,4105216,7744000,6495488,0,0,0,
328720,857900,221728,810060,2012492,0,0,0,3587584,4179456,4106240,7745024,6496512,0,0,0,3588608,4180480,4107264,7746048,6497536,0,0,0,3589632,4181504,4108288,7747072,6769920,0,0,0,3687936,4182528,4109312,8019456,6770944,0,0,0,
328848,858028,221856,810188,2012620,0,0,0,3688960,4319744,4110336,8020480,6771968,0,0,0,3689984,4320768,4247552,8021504,6772992,0,0,0,3691008,4321792,4248576,8022528,6774016,0,0,0,3692032,4322816,4249600,8023552,7046400,0,0,0,
378896,858156,221984,814156,2012748,0,0,0,2971392,4323840,4250624,6084352,7047424,0,0,0,2972416,4324864,4251648,6085376,7048448,0,0,0,2973440,4325888,4252672,6086400,7049472,0,0,0,2974464,4463104,4253696,6087424,7050496,0,0,0,
379024,858284,222112,814284,2012876,0,0,0,2975488,4464128,4390912,6088448,7322880,0,0,0,3073792,4465152,4391936,6360832,7323904,0,0,0,3074816,4466176,4392960,6361856,7324928,0,0,0,3075840,4467200,4393984,6362880,7325952,0,0,0,
379152,858412,222240,814412,2013004,0,0,0,3076864,4468224,4395008,6363904,7326976,0,0,0,3077888,4469248,4396032,6364928,7599360,0,0,0,3176192,4606464,4397056,6637312,7600384,0,0,0,3177216,4607488,4534272,6638336,7601408,0,0,0,
379280,858540,222368,814540,2013132,0,0,0,3178240,4608512,4535296,6639360,7602432,0,0,0,3179264,4609536,4536320,6640384,7603456,0,0,0,3180288,4610560,4537344,6641408,7875840,0,0,0,3278592,4611584,4538368,6913792,7876864,0,0,0,
379408,858668,222496,814668,2013260,0,0,0,3279616,4612608,4539392,6914816,7877888,0,0,0,3280640,4749824,4540416,6915840,7878912,0,0,0,3281664,4750848,4677632,6916864,7879936,0,0,0,3282688,4751872,4678656,6917888,8152320,0,0,0,
379536,858796,222624,814796,2013388,0,0,0,3380992,4752896,4679680,7190272,8153344,0,0,0,3382016,4753920,4680704,7191296,8154368,0,0,0,3383040,4754944,4681728,7192320,8155392,0,0,0,3384064,4755968,4682752,7193344,8156416,0,0,0,
379664,858924,222752,814924,2017356,0,0,0,3385088,4893184,4683776,7194368,6217216,0,0,0,3483392,4894208,4820992,7466752,6218240,0,0,0,3484416,4895232,4822016,7467776,6219264,0,0,0,3485440,4896256,4823040,7468800,6220288,0,0,0,
379792,859052,222880,815052,2017484,0,0,0,3486464,4897280,4824064,7469824,6221312,0,0,0,3487488,4898304,4825088,7470848,6493696,0,0,0,3585792,4899328,4826112,7743232,6494720,0,0,0,3586816,5036544,4827136,7744256,6495744,0,0,0,
379920,859180,223008,815180,2017612,0,0,0,3587840,5037568,4964352,7745280,6496768,0,0,0,3588864,5038592,4965376,7746304,6497792,0,0,0,3589888,5039616,4966400,7747328,6770176,0,0,0,3688192,5040640,4967424,8019712,6771200,0,0,0,
380048,859308,223136,815308,2017740,0,0,0,3689216,5041664,4968448,8020736,6772224,0,0,0,3690240,5042688,4969472,8021760,6773248,0,0,0,3691264,5179904,4970496,8022784,6774272,0,0,0,3692288,5180928,5107712,8023808,7046656,0,0,0,
430096,859436,223264,860236,2017868,0,0,0,2975744,5181952,5108736,6088704,7047680,0,0,0,2976768,5182976,5109760,6089728,7048704,0,0,0,2977792,5184000,5110784,6090752,7049728,0,0,0,2978816,5185024,5111808,6091776,7050752,0,0,0,
430224,859564,223392,860364,2017996,0,0,0,2979840,5186048,5112832,6092800,7323136,0,0,0,3078144,5323264,5113856,6365184,7324160,0,0,0,3079168,5324288,5251072,6366208,7325184,0,0,0,3080192,5325312,5252096,6367232,7326208,0,0,0,
430352,859692,223520,860492,2018124,0,0,0,3081216,5326336,5253120,6368256,7327232,0,0,0,3082240,5327360,5254144,6369280,7599616,0,0,0,3180544,5328384,5255168,6641664,7600640,0,0,0,3181568,5329408,5256192,6642688,7601664,0,0,0,
430480,859820,223648,860620,2018252,0,0,0,3182592,5466624,5257216,6643712,7602688,0,0,0,3183616,5467648,5394432,6644736,7603712,0,0,0,3184640,5468672,5395456,6645760,7876096,0,0,0,3282944,5469696,5396480,6918144,7877120,0,0,0,
430608,859948,223776,860748,2018380,0,0,0,3283968,5470720,5397504,6919168,7878144,0,0,0,3284992,5471744,5398528,6920192,7879168,0,0,0,3286016,5609984,5399552,6921216,7880192,0,0,0,3287040,5611008,5400576,6922240,8152576,0,0,0,
430736,860076,223904,860876,2018508,0,0,0,3385344,5612032,5537792,7194624,8153600,0,0,0,3386368,5613056,5538816,7195648,8154624,0,0,0,3387392,5614080,5539840,7196672,8155648,0,0,0,3388416,5615104,5540864,7197696,8156672,0,0,0,
430864,860204,224032,861004,2022476,0,0,0,3389440,5753344,5541888,7198720,6217472,0,0,0,3487744,5754368,5542912,7471104,6218496,0,0,0,3488768,5755392,5681152,7472128,6219520,0,0,0,3489792,5756416,5682176,7473152,6220544,0,0,0,
430992,860332,224160,861132,2022604,0,0,0,3490816,5757440,5683200,7474176,6221568,0,0,0,3491840,5758464,5684224,7475200,6493952,0,0,0,3590144,5896704,5685248,7747584,6494976,0,0,0,3591168,5897728,5686272,7748608,6496000,0,0,0,
431120,860460,224288,861260,2022732,0,0,0,3592192,5898752,5824512,7749632,6497024,0,0,0,3593216,5899776,5825536,7750656,6498048,0,0,0,3594240,5900800,5826560,7751680,6770432,0,0,0,3692544,5901824,5827584,8024064,6771456,0,0,0,
431248,870956,224416,861388,2022860,0,0,0,3693568,3746560,5828608,8025088,6772480,0,0,0,3694592,3747584,5829632,8026112,6773504,0,0,0,3695616,3748608,5967872,8027136,6774528,0,0,0,3696640,3749632,5968896,8028160,7046912,0,0,0,
481296,871084,224544,865356,2022988,0,0,0,2976000,3750656,5969920,6088960,7047936,0,0,0,2977024,3751680,5970944,6089984,7048960,0,0,0,2978048,3752704,5971968,6091008,7049984,0,0,0,2979072,3889920,5972992,6092032,7051008,0,0,0,
481424,871212,248864,865484,2023116,0,0,0,2980096,3890944,3817728,6093056,7323392,0,0,0,3078400,3891968,3818752,6365440,7324416,0,0,0,3079424,3892992,3819776,6366464,7325440,0,0,0,3080448,3894016,3820800,6367488,7326464,0,0,0,
481552,871340,248992,865612,2023244,0,0,0,3081472,3895040,3821824,6368512,7327488,0,0,0,3082496,3896064,3822848,6369536,7599872,0,0,0,3180800,4033280,3823872,6641920,7600896,0,0,0,3181824,4034304,3961088,6642944,7601920,0,0,0,
481680,871468,249120,865740,2023372,0,0,0,3182848,4035328,3962112,6643968,7602944,0,0,0,3183872,4036352,3963136,6644992,7603968,0,0,0,3184896,4037376,3964160,6646016,7876352,0,0,0,3283200,4038400,3965184,6918400,7877376,0,0,0,
481808,871596,249248,865868,2023500,0,0,0,3284224,4039424,3966208,6919424,7878400,0,0,0,3285248,4176640,3967232,6920448,7879424,0,0,0,3286272,4177664,4104448,6921472,7880448,0,0,0,3287296,4178688,4105472,6922496,8152832,0,0,0,
481936,871724,249376,865996,2023628,0,0,0,3385600,4179712,4106496,7194880,8153856,0,0,0,3386624,4180736,4107520,7195904,8154880,0,0,0,3387648,4181760,4108544,7196928,8155904,0,0,0,3388672,4182784,4109568,7197952,8156928,0,0,0,
482064,871852,249504,866124,2048076,0,0,0,3389696,4320000,4110592,7198976,6221824,0,0,0,3488000,4321024,4247808,7471360,6222848,0,0,0,3489024,4322048,4248832,7472384,6223872,0,0,0,3490048,4323072,4249856,7473408,6224896,0,0,0,
482192,871980,249632,866252,2048204,0,0,0,3491072,4324096,4250880,7474432,6225920,0,0,0,3492096,4325120,4251904,7475456,6498304,0,0,0,3590400,4326144,4252928,7747840,6499328,0,0,0,3591424,4463360,4253952,7748864,6500352,0,0,0,
482320,872108,249760,866380,2048332,0,0,0,3592448,4464384,4391168,7749888,6501376,0,0,0,3593472,4465408,4392192,7750912,6502400,0,0,0,3594496,4466432,4393216,7751936,6774784,0,0,0,3692800,4467456,4394240,8024320,6775808,0,0,0,
482448,872236,249888,866508,2048460,0,0,0,3693824,4468480,4395264,8025344,6776832,0,0,0,3694848,4469504,4396288,8026368,6777856,0,0,0,3695872,4606720,4397312,8027392,6778880,0,0,0,3696896,4607744,4534528,8028416,7051264,0,0,0,
532496,872364,250016,870476,2048588,0,0,0,2976256,4608768,4535552,6089216,7052288,0,0,0,2977280,4609792,4536576,6090240,7053312,0,0,0,2978304,4610816,4537600,6091264,7054336,0,0,0,2979328,4611840,4538624,6092288,7055360,0,0,0,
532624,872492,250144,870604,2048716,0,0,0,2980352,4612864,4539648,6093312,7327744,0,0,0,3078656,4750080,4540672,6365696,7328768,0,0,0,3079680,4751104,4677888,6366720,7329792,0,0,0,3080704,4752128,4678912,6367744,7330816,0,0,0,
532752,872620,250272,870732,2048844,0,0,0,3081728,4753152,4679936,6368768,7331840,0,0,0,3082752,4754176,4680960,6369792,7604224,0,0,0,3181056,4755200,4681984,6642176,7605248,0,0,0,3182080,4756224,4683008,6643200,7606272,0,0,0,
532880,872748,250400,870860,2048972,0,0,0,3183104,4893440,4684032,6644224,7607296,0,0,0,3184128,4894464,4821248,6645248,7608320,0,0,0,3185152,4895488,4822272,6646272,7880704,0,0,0,3283456,4896512,4823296,6918656,7881728,0,0,0,
533008,872876,250528,870988,2049100,0,0,0,3284480,4897536,4824320,6919680,7882752,0,0,0,3285504,4898560,4825344,6920704,7883776,0,0,0,3286528,4899584,4826368,6921728,7884800,0,0,0,3287552,5036800,4827392,6922752,8157184,0,0,0,
533136,873004,250656,871116,2049228,0,0,0,3385856,5037824,4964608,7195136,8158208,0,0,0,3386880,5038848,4965632,7196160,8159232,0,0,0,3387904,5039872,4966656,7197184,8160256,0,0,0,3388928,5040896,4967680,7198208,8161280,0,0,0,
533264,873132,250784,871244,2053196,0,0,0,3389952,5041920,4968704,7199232,6222080,0,0,0,3488256,5042944,4969728,7471616,6223104,0,0,0,3489280,5180160,4970752,7472640,6224128,0,0,0,3490304,5181184,5107968,7473664,6225152,0,0,0,
533392,873260,250912,871372,2053324,0,0,0,3491328,5182208,5108992,7474688,6226176,0,0,0,3492352,5183232,5110016,7475712,6498560,0,0,0,3590656,5184256,5111040,7748096,6499584,0,0,0,3591680,5185280,5112064,7749120,6500608,0,0,0,
533520,873388,251040,871500,2053452,0,0,0,3592704,5186304,5113088,7750144,6501632,0,0,0,3593728,5323520,5114112,7751168,6502656,0,0,0,3594752,5324544,5251328,7752192,6775040,0,0,0,3693056,5325568,5252352,8024576,6776064,0,0,0,
533648,873516,251168,871628,2053580,0,0,0,3694080,5326592,5253376,8025600,6777088,0,0,0,3695104,5327616,5254400,8026624,6778112,0,0,0,3696128,5328640,5255424,8027648,6779136,0,0,0,3697152,5329664,5256448,8028672,7051520,0,0,0,
583696,873644,251296,875596,2053708,0,0,0,2976512,5466880,5257472,6089472,7052544,0,0,0,2977536,5467904,5394688,6090496,7053568,0,0,0,2978560,5468928,5395712,6091520,7054592,0,0,0,2979584,5469952,5396736,6092544,7055616,0,0,0,
583824,873772,251424,875724,2053836,0,0,0,2980608,5470976,5397760,6093568,7328000,0,0,0,3078912,5472000,5398784,6365952,7329024,0,0,0,3079936,5610240,5399808,6366976,7330048,0,0,0,3080960,5611264,5400832,6368000,7331072,0,0,0,
583952,873900,251552,875852,2053964,0,0,0,3081984,5612288,5538048,6369024,7332096,0,0,0,3083008,5613312,5539072,6370048,7604480,0,0,0,3181312,5614336,5540096,6642432,7605504,0,0,0,3182336,5615360,5541120,6643456,7606528,0,0,0,
584080,874028,251680,875980,2054092,0,0,0,3183360,5753600,5542144,6644480,7607552,0,0,0,3184384,5754624,5543168,6645504,7608576,0,0,0,3185408,5755648,5681408,6646528,7880960,0,0,0,3283712,5756672,5682432,6918912,7881984,0,0,0,
584208,874156,251808,876108,2054220,0,0,0,3284736,5757696,5683456,6919936,7883008,0,0,0,3285760,5758720,5684480,6920960,7884032,0,0,0,3286784,5896960,5685504,6921984,7885056,0,0,0,3287808,5897984,5686528,6923008,8157440,0,0,0,
584336,874284,251936,876236,2054348,0,0,0,3386112,5899008,5824768,7195392,8158464,0,0,0,3387136,5900032,5825792,7196416,8159488,0,0,0,3388160,5901056,5826816,7197440,8160512,0,0,0,3389184,5902080,5827840,7198464,8161536,0,0,0,
584464,940076,252064,876364,2058316,0,0,0,3390208,3752960,5828864,7199488,6222336,0,0,0,3488512,3753984,5829888,7471872,6223360,0,0,0,3489536,3755008,5968128,7472896,6224384,0,0,0,3490560,3756032,5969152,7473920,6225408,0,0,0,
584592,940204,252192,876492,2058444,0,0,0,3491584,3757056,5970176,7474944,6226432,0,0,0,3492608,3758080,5971200,7475968,6498816,0,0,0,3590912,3759104,5972224,7748352,6499840,0,0,0,3591936,3896320,5973248,7749376,6500864,0,0,0,
584720,940332,497696,876620,2058572,0,0,0,3592960,3897344,3817984,7750400,6501888,0,0,0,3593984,3898368,3819008,7751424,6502912,0,0,0,3595008,3899392,3820032,7752448,6775296,0,0,0,3693312,3900416,3821056,8024832,6776320,0,0,0,
584848,940460,497824,876748,2058700,0,0,0,3694336,3901440,3822080,8025856,6777344,0,0,0,3695360,3902464,3823104,8026880,6778368,0,0,0,3696384,4039680,3824128,8027904,6779392,0,0,0,3697408,4040704,3961344,8028928,7051776,0,0,0,
634896,940588,497952,901196,2058828,0,0,0,2980864,4041728,3962368,6093824,7052800,0,0,0,2981888,4042752,3963392,6094848,7053824,0,0,0,2982912,4043776,3964416,6095872,7054848,0,0,0,2983936,4044800,3965440,6096896,7055872,0,0,0,
635024,940716,498080,901324,2058956,0,0,0,2984960,4045824,3966464,6097920,7328256,0,0,0,3083264,4183040,3967488,6370304,7329280,0,0,0,3084288,4184064,4104704,6371328,7330304,0,0,0,3085312,4185088,4105728,6372352,7331328,0,0,0,
635152,940844,498208,901452,2059084,0,0,0,3086336,4186112,4106752,6373376,7332352,0,0,0,3087360,4187136,4107776,6374400,7604736,0,0,0,3185664,4188160,4108800,6646784,7605760,0,0,0,3186688,4189184,4109824,6647808,7606784,0,0,0,
635280,940972,498336,901580,2059212,0,0,0,3187712,4326400,4110848,6648832,7607808,0,0,0,3188736,4327424,4248064,6649856,7608832,0,0,0,3189760,4328448,4249088,6650880,7881216,0,0,0,3288064,4329472,4250112,6923264,7882240,0,0,0,
635408,941100,498464,901708,2059340,0,0,0,3289088,4330496,4251136,6924288,7883264,0,0,0,3290112,4331520,4252160,6925312,7884288,0,0,0,3291136,4332544,4253184,6926336,7885312,0,0,0,3292160,4469760,4254208,6927360,8157696,0,0,0,
635536,941228,498592,901836,2059468,0,0,0,3390464,4470784,4391424,7199744,8158720,0,0,0,3391488,4471808,4392448,7200768,8159744,0,0,0,3392512,4472832,4393472,7201792,8160768,0,0,0,3393536,4473856,4394496,7202816,8161792,0,0,0,
635664,941356,498720,901964,2063436,0,0,0,3394560,4474880,4395520,7203840,6222592,0,0,0,3492864,4475904,4396544,7476224,6223616,0,0,0,3493888,4613120,4397568,7477248,6224640,0,0,0,3494912,4614144,4534784,7478272,6225664,0,0,0,
635792,941484,498848,902092,2063564,0,0,0,3495936,4615168,4535808,7479296,6226688,0,0,0,3496960,4616192,4536832,7480320,6499072,0,0,0,3595264,4617216,4537856,7752704,6500096,0,0,0,3596288,4618240,4538880,7753728,6501120,0,0,0,
635920,941612,498976,902220,2063692,0,0,0,3597312,4619264,4539904,7754752,6502144,0,0,0,3598336,4756480,4540928,7755776,6503168,0,0,0,3599360,4757504,4678144,7756800,6775552,0,0,0,3697664,4758528,4679168,8029184,6776576,0,0,0,
636048,941740,499104,902348,2063820,0,0,0,3698688,4759552,4680192,8030208,6777600,0,0,0,3699712,4760576,4681216,8031232,6778624,0,0,0,3700736,4761600,4682240,8032256,6779648,0,0,0,3701760,4762624,4683264,8033280,7052032,0,0,0,
686096,941868,499232,906316,2063948,0,0,0,2981120,4899840,4684288,6094080,7053056,0,0,0,2982144,4900864,4821504,6095104,7054080,0,0,0,2983168,4901888,4822528,6096128,7055104,0,0,0,2984192,4902912,4823552,6097152,7056128,0,0,0,
686224,941996,499360,906444,2064076,0,0,0,2985216,4903936,4824576,6098176,7328512,0,0,0,3083520,4904960,4825600,6370560,7329536,0,0,0,3084544,4905984,4826624,6371584,7330560,0,0,0,3085568,5043200,4827648,6372608,7331584,0,0,0,
686352,942124,499488,906572,2064204,0,0,0,3086592,5044224,4964864,6373632,7332608,0,0,0,3087616,5045248,4965888,6374656,7604992,0,0,0,3185920,5046272,4966912,6647040,7606016,0,0,0,3186944,5047296,4967936,6648064,7607040,0,0,0,
686480,942252,499616,906700,2064332,0,0,0,3187968,5048320,4968960,6649088,7608064,0,0,0,3188992,5049344,4969984,6650112,7609088,0,0,0,3190016,5186560,4971008,6651136,7881472,0,0,0,3288320,5187584,5108224,6923520,7882496,0,0,0,
686608,942380,499744,906828,2064460,0,0,0,3289344,5188608,5109248,6924544,7883520,0,0,0,3290368,5189632,5110272,6925568,7884544,0,0,0,3291392,5190656,5111296,6926592,7885568,0,0,0,3292416,5191680,5112320,6927616,8157952,0,0,0,
686736,942508,499872,906956,2064588,0,0,0,3390720,5192704,5113344,7200000,8158976,0,0,0,3391744,5329920,5114368,7201024,8160000,0,0,0,3392768,5330944,5251584,7202048,8161024,0,0,0,3393792,5331968,5252608,7203072,8162048,0,0,0,
686864,942636,500000,907084,2089036,0,0,0,3394816,5332992,5253632,7204096,6226944,0,0,0,3493120,5334016,5254656,7476480,6227968,0,0,0,3494144,5335040,5255680,7477504,6228992,0,0,0,3495168,5336064,5256704,7478528,6230016,0,0,0,
686992,942764,500128,907212,2089164,0,0,0,3496192,5473280,5257728,7479552,6231040,0,0,0,3497216,5474304,5394944,7480576,6503424,0,0,0,3595520,5475328,5395968,7752960,6504448,0,0,0,3596544,5476352,5396992,7753984,6505472,0,0,0,
687120,942892,500256,907340,2089292,0,0,0,3597568,5477376,5398016,7755008,6506496,0,0,0,3598592,5478400,5399040,7756032,6507520,0,0,0,3599616,5616640,5400064,7757056,6779904,0,0,0,3697920,5617664,5401088,8029440,6780928,0,0,0,
687248,943020,500384,907468,2089420,0,0,0,3698944,5618688,5538304,8030464,6781952,0,0,0,3699968,5619712,5539328,8031488,6782976,0,0,0,3700992,5620736,5540352,8032512,6784000,0,0,0,3702016,5621760,5541376,8033536,7056384,0,0,0,
737296,943148,500512,911436,2089548,0,0,0,2981376,5760000,5542400,6094336,7057408,0,0,0,2982400,5761024,5543424,6095360,7058432,0,0,0,2983424,5762048,5681664,6096384,7059456,0,0,0,2984448,5763072,5682688,6097408,7060480,0,0,0,
737424,943276,500640,911564,2089676,0,0,0,2985472,5764096,5683712,6098432,7332864,0,0,0,3083776,5765120,5684736,6370816,7333888,0,0,0,3084800,5903360,5685760,6371840,7334912,0,0,0,3085824,5904384,5686784,6372864,7335936,0,0,0,
737552,943404,500768,911692,2089804,0,0,0,3086848,5905408,5825024,6373888,7336960,0,0,0,3087872,5906432,5826048,6374912,7609344,0,0,0,3186176,5907456,5827072,6647296,7610368,0,0,0,3187200,5908480,5828096,6648320,7611392,0,0,0,
737680,953900,500896,911820,2089932,0,0,0,3188224,3753216,5829120,6649344,7612416,0,0,0,3189248,3754240,5830144,6650368,7613440,0,0,0,3190272,3755264,5968384,6651392,7885824,0,0,0,3288576,3756288,5969408,6923776,7886848,0,0,0,
737808,954028,501024,911948,2090060,0,0,0,3289600,3757312,5970432,6924800,7887872,0,0,0,3290624,3758336,5971456,6925824,7888896,0,0,0,3291648,3759360,5972480,6926848,7889920,0,0,0,3292672,3896576,5973504,6927872,8162304,0,0,0,
737936,954156,525344,912076,2090188,0,0,0,3390976,3897600,3818240,7200256,8163328,0,0,0,3392000,3898624,3819264,7201280,8164352,0,0,0,3393024,3899648,3820288,7202304,8165376,0,0,0,3394048,3900672,3821312,7203328,8166400,0,0,0,
738064,954284,525472,912204,2094156,0,0,0,3395072,3901696,3822336,7204352,6227200,0,0,0,3493376,3902720,3823360,7476736,6228224,0,0,0,3494400,4039936,3824384,7477760,6229248,0,0,0,3495424,4040960,3961600,7478784,6230272,0,0,0,
738192,954412,525600,912332,2094284,0,0,0,3496448,4041984,3962624,7479808,6231296,0,0,0,3497472,4043008,3963648,7480832,6503680,0,0,0,3595776,4044032,3964672,7753216,6504704,0,0,0,3596800,4045056,3965696,7754240,6505728,0,0,0,
738320,954540,525728,912460,2094412,0,0,0,3597824,4046080,3966720,7755264,6506752,0,0,0,3598848,4183296,3967744,7756288,6507776,0,0,0,3599872,4184320,4104960,7757312,6780160,0,0,0,3698176,4185344,4105984,8029696,6781184,0,0,0,
738448,954668,525856,912588,2094540,0,0,0,3699200,4186368,4107008,8030720,6782208,0,0,0,3700224,4187392,4108032,8031744,6783232,0,0,0,3701248,4188416,4109056,8032768,6784256,0,0,0,3702272,4189440,4110080,8033792,7056640,0,0,0,
788496,954796,525984,916556,2094668,0,0,0,2981632,4326656,4111104,6094592,7057664,0,0,0,2982656,4327680,4248320,6095616,7058688,0,0,0,2983680,4328704,4249344,6096640,7059712,0,0,0,2984704,4329728,4250368,6097664,7060736,0,0,0,
788624,954924,526112,916684,2094796,0,0,0,2985728,4330752,4251392,6098688,7333120,0,0,0,3084032,4331776,4252416,6371072,7334144,0,0,0,3085056,4332800,4253440,6372096,7335168,0,0,0,3086080,4470016,4254464,6373120,7336192,0,0,0,
788752,955052,526240,916812,2094924,0,0,0,3087104,4471040,4391680,6374144,7337216,0,0,0,3088128,4472064,4392704,6375168,7609600,0,0,0,3186432,4473088,4393728,6647552,7610624,0,0,0,3187456,4474112,4394752,6648576,7611648,0,0,0,
788880,955180,526368,916940,2095052,0,0,0,3188480,4475136,4395776,6649600,7612672,0,0,0,3189504,4476160,4396800,6650624,7613696,0,0,0,3190528,4613376,4397824,6651648,7886080,0,0,0,3288832,4614400,4535040,6924032,7887104,0,0,0,
789008,955308,526496,917068,2095180,0,0,0,3289856,4615424,4536064,6925056,7888128,0,0,0,3290880,4616448,4537088,6926080,7889152,0,0,0,3291904,4617472,4538112,6927104,7890176,0,0,0,3292928,4618496,4539136,6928128,8162560,0,0,0,
789136,955436,526624,917196,2095308,0,0,0,3391232,4619520,4540160,7200512,8163584,0,0,0,3392256,4756736,4541184,7201536,8164608,0,0,0,3393280,4757760,4678400,7202560,8165632,0,0,0,3394304,4758784,4679424,7203584,8166656,0,0,0,
789264,955564,526752,917324,2099276,0,0,0,3395328,4759808,4680448,7204608,6227456,0,0,0,3493632,4760832,4681472,7476992,6228480,0,0,0,3494656,4761856,4682496,7478016,6229504,0,0,0,3495680,4762880,4683520,7479040,6230528,0,0,0,
789392,955692,526880,917452,2099404,0,0,0,3496704,4900096,4684544,7480064,6231552,0,0,0,3497728,4901120,4821760,7481088,6503936,0,0,0,3596032,4902144,4822784,7753472,6504960,0,0,0,3597056,4903168,4823808,7754496,6505984,0,0,0,
789520,955820,527008,917580,2099532,0,0,0,3598080,4904192,4824832,7755520,6507008,0,0,0,3599104,4905216,4825856,7756544,6508032,0,0,0,3600128,4906240,4826880,7757568,6780416,0,0,0,3698432,5043456,4827904,8029952,6781440,0,0,0,
789648,955948,527136,917708,2099660,0,0,0,3699456,5044480,4965120,8030976,6782464,0,0,0,3700480,5045504,4966144,8032000,6783488,0,0,0,3701504,5046528,4967168,8033024,6784512,0,0,0,3702528,5047552,4968192,8034048,7056896,0,0,0,
1634208,956076,527264,942156,2099788,0,0,0,5698304,5048576,4969216,6098944,7057920,0,0,0,5699328,5049600,4970240,6099968,7058944,0,0,0,5700352,5186816,4971264,6100992,7059968,0,0,0,5701376,5187840,5108480,6102016,7060992,0,0,0,
1634336,956204,527392,942284,2099916,0,0,0,5839616,5188864,5109504,6103040,7333376,0,0,0,5840640,5189888,5110528,6375424,7334400,0,0,0,5841664,5190912,5111552,6376448,7335424,0,0,0,5842688,5191936,5112576,6377472,7336448,0,0,0,
1634464,956332,527520,942412,2100044,0,0,0,5843712,5192960,5113600,6378496,7337472,0,0,0,5844736,5330176,5114624,6379520,7609856,0,0,0,5982976,5331200,5251840,6651904,7610880,0,0,0,5984000,5332224,5252864,6652928,7611904,0,0,0,
1634592,956460,527648,942540,2100172,0,0,0,5985024,5333248,5253888,6653952,7612928,0,0,0,5986048,5334272,5254912,6654976,7613952,0,0,0,5987072,5335296,5255936,6656000,7886336,0,0,0,5988096,5336320,5256960,6928384,7887360,0,0,0,
1880096,956588,527776,942668,2100300,0,0,0,3838976,5473536,5257984,6929408,7888384,0,0,0,3840000,5474560,5395200,6930432,7889408,0,0,0,3841024,5475584,5396224,6931456,7890432,0,0,0,3842048,5476608,5397248,6932480,8162816,0,0,0,
1880224,956716,527904,942796,2100428,0,0,0,3843072,5477632,5398272,7204864,8163840,0,0,0,3844096,5478656,5399296,7205888,8164864,0,0,0,3845120,5616896,5400320,7206912,8165888,0,0,0,3982336,5617920,5401344,7207936,8166912,0,0,0,
1880352,956844,528032,942924,2104396,0,0,0,3983360,5618944,5538560,7208960,6227712,0,0,0,3984384,5619968,5539584,7481344,6228736,0,0,0,3985408,5620992,5540608,7482368,6229760,0,0,0,3986432,5622016,5541632,7483392,6230784,0,0,0,
1880480,956972,528160,943052,2104524,0,0,0,3987456,5760256,5542656,7484416,6231808,0,0,0,3988480,5761280,5543680,7485440,6504192,0,0,0,4125696,5762304,5681920,7757824,6505216,0,0,0,4126720,5763328,5682944,7758848,6506240,0,0,0,
1880608,957100,528288,943180,2104652,0,0,0,4127744,5764352,5683968,7759872,6507264,0,0,0,4128768,5765376,5684992,7760896,6508288,0,0,0,4129792,5903616,5686016,7761920,6780672,0,0,0,4130816,5904640,5687040,8034304,6781696,0,0,0,
1880736,957228,528416,943308,2104780,0,0,0,4131840,5905664,5825280,8035328,6782720,0,0,0,4269056,5906688,5826304,8036352,6783744,0,0,0,4270080,5907712,5827328,8037376,6784768,0,0,0,4271104,5908736,5828352,8038400,7057152,0,0,0,
1880864,967724,528544,947276,2104908,0,0,0,4272128,3753472,5829376,6099200,7058176,0,0,0,4273152,3754496,5830400,6100224,7059200,0,0,0,4274176,3755520,5968640,6101248,7060224,0,0,0,4275200,3756544,5969664,6102272,7061248,0,0,0,
1880992,967852,528672,947404,2105036,0,0,0,4412416,3757568,5970688,6103296,7333632,0,0,0,4413440,3758592,5971712,6375680,7334656,0,0,0,4414464,3759616,5972736,6376704,7335680,0,0,0,4415488,3896832,5973760,6377728,7336704,0,0,0,
1881120,967980,774176,947532,2105164,0,0,0,4416512,3897856,3824640,6378752,7337728,0,0,0,4417536,3898880,3825664,6379776,7610112,0,0,0,4418560,3899904,3826688,6652160,7611136,0,0,0,4555776,3900928,3827712,6653184,7612160,0,0,0,
1881248,968108,774304,947660,2105292,0,0,0,4556800,3901952,3828736,6654208,7613184,0,0,0,4557824,3902976,3829760,6655232,7614208,0,0,0,4558848,4040192,3830784,6656256,7886592,0,0,0,4559872,4041216,3968000,6928640,7887616,0,0,0,
1881376,968236,774432,947788,2105420,0,0,0,4560896,4042240,3969024,6929664,7888640,0,0,0,4561920,4043264,3970048,6930688,7889664,0,0,0,4699136,4044288,3971072,6931712,7890688,0,0,0,4700160,4045312,3972096,6932736,8163072,0,0,0,
1881504,968364,774560,947916,2105548,0,0,0,4701184,4046336,3973120,7205120,8164096,0,0,0,4702208,4183552,3974144,7206144,8165120,0,0,0,4703232,4184576,4111360,7207168,8166144,0,0,0,4704256,4185600,4112384,7208192,8167168,0,0,0,
1881632,968492,774688,948044,2129996,0,0,0,4705280,4186624,4113408,7209216,6232064,0,0,0,4842496,4187648,4114432,7481600,6233088,0,0,0,4843520,4188672,4115456,7482624,6234112,0,0,0,4844544,4189696,4116480,7483648,6235136,0,0,0,
1881760,968620,774816,948172,2130124,0,0,0,4845568,4326912,4117504,7484672,6236160,0,0,0,4846592,4327936,4254720,7485696,6508544,0,0,0,4847616,4328960,4255744,7758080,6509568,0,0,0,4848640,4329984,4256768,7759104,6510592,0,0,0,
1881888,968748,774944,948300,2130252,0,0,0,4985856,4331008,4257792,7760128,6511616,0,0,0,4986880,4332032,4258816,7761152,6512640,0,0,0,4987904,4333056,4259840,7762176,6785024,0,0,0,4988928,4470272,4260864,8034560,6786048,0,0,0,
1882016,968876,775072,948428,2130380,0,0,0,4989952,4471296,4398080,8035584,6787072,0,0,0,4990976,4472320,4399104,8036608,6788096,0,0,0,4992000,4473344,4400128,8037632,6789120,0,0,0,5129216,4474368,4401152,8038656,7061504,0,0,0,
1882144,969004,775200,952396,2130508,0,0,0,5130240,4475392,4402176,6099456,7062528,0,0,0,5131264,4476416,4403200,6100480,7063552,0,0,0,5132288,4613632,4404224,6101504,7064576,0,0,0,5133312,4614656,4541440,6102528,7065600,0,0,0,
1882272,969132,775328,952524,2130636,0,0,0,5134336,4615680,4542464,6103552,7337984,0,0,0,5135360,4616704,4543488,6375936,7339008,0,0,0,5272576,4617728,4544512,6376960,7340032,0,0,0,5273600,4618752,4545536,6377984,7341056,0,0,0,
1882400,969260,775456,952652,2130764,0,0,0,5274624,4619776,4546560,6379008,7342080,0,0,0,5275648,4756992,4547584,6380032,7614464,0,0,0,5276672,4758016,4684800,6652416,7615488,0,0,0,5277696,4759040,4685824,6653440,7616512,0,0,0,
1882528,969388,775584,952780,2130892,0,0,0,5278720,4760064,4686848,6654464,7617536,0,0,0,5415936,4761088,4687872,6655488,7618560,0,0,0,5416960,4762112,4688896,6656512,7890944,0,0,0,5417984,4763136,4689920,6928896,7891968,0,0,0,
1882656,969516,775712,952908,2131020,0,0,0,5419008,4900352,4690944,6929920,7892992,0,0,0,5420032,4901376,4828160,6930944,7894016,0,0,0,5421056,4902400,4829184,6931968,7895040,0,0,0,5422080,4903424,4830208,6932992,8167424,0,0,0,
1882784,969644,775840,953036,2131148,0,0,0,5559296,4904448,4831232,7205376,8168448,0,0,0,5560320,4905472,4832256,7206400,8169472,0,0,0,5561344,4906496,4833280,7207424,8170496,0,0,0,5562368,5043712,4834304,7208448,8171520,0,0,0,
1882912,969772,775968,953164,2135116,0,0,0,5563392,5044736,4971520,7209472,6232320,0,0,0,5564416,5045760,4972544,7481856,6233344,0,0,0,5702656,5046784,4973568,7482880,6234368,0,0,0,5703680,5047808,4974592,7483904,6235392,0,0,0,
1883040,969900,776096,953292,2135244,0,0,0,5704704,5048832,4975616,7484928,6236416,0,0,0,5705728,5049856,4976640,7485952,6508800,0,0,0,5706752,5187072,4977664,7758336,6509824,0,0,0,5707776,5188096,5114880,7759360,6510848,0,0,0,
1883168,970028,776224,953420,2135372,0,0,0,5846016,5189120,5115904,7760384,6511872,0,0,0,5847040,5190144,5116928,7761408,6512896,0,0,0,5848064,5191168,5117952,7762432,6785280,0,0,0,5849088,5192192,5118976,8034816,6786304,0,0,0,
1883296,970156,776352,953548,2135500,0,0,0,5850112,5193216,5120000,8035840,6787328,0,0,0,5851136,5330432,5121024,8036864,6788352,0,0,0,5989376,5331456,5258240,8037888,6789376,0,0,0,5990400,5332480,5259264,8038912,7061760,0,0,0,
1883424,970284,776480,957516,2135628,0,0,0,5991424,5333504,5260288,6099712,7062784,0,0,0,5992448,5334528,5261312,6100736,7063808,0,0,0,5993472,5335552,5262336,6101760,7064832,0,0,0,5994496,5336576,5263360,6102784,7065856,0,0,0,
1907744,970412,776608,957644,2135756,0,0,0,3839232,5473792,5264384,6103808,7338240,0,0,0,3840256,5474816,5401600,6376192,7339264,0,0,0,3841280,5475840,5402624,6377216,7340288,0,0,0,3842304,5476864,5403648,6378240,7341312,0,0,0,
1907872,970540,776736,957772,2135884,0,0,0,3843328,5477888,5404672,6379264,7342336,0,0,0,3844352,5478912,5405696,6380288,7614720,0,0,0,3845376,5617152,5406720,6652672,7615744,0,0,0,3982592,5618176,5407744,6653696,7616768,0,0,0,
1908000,970668,776864,957900,2136012,0,0,0,3983616,5619200,5544960,6654720,7617792,0,0,0,3984640,5620224,5545984,6655744,7618816,0,0,0,3985664,5621248,5547008,6656768,7891200,0,0,0,3986688,5622272,5548032,6929152,7892224,0,0,0,
1908128,970796,776992,958028,2136140,0,0,0,3987712,5760512,5549056,6930176,7893248,0,0,0,3988736,5761536,5550080,6931200,7894272,0,0,0,4125952,5762560,5688320,6932224,7895296,0,0,0,4126976,5763584,5689344,6933248,8167680,0,0,0,
1908256,970924,777120,958156,2136268,0,0,0,4128000,5764608,5690368,7205632,8168704,0,0,0,4129024,5765632,5691392,7206656,8169728,0,0,0,4130048,5903872,5692416,7207680,8170752,0,0,0,4131072,5904896,5693440,7208704,8171776,0,0,0,
1908384,971052,777248,958284,2140236,0,0,0,4132096,5905920,5831680,7209728,6232576,0,0,0,4269312,5906944,5832704,7482112,6233600,0,0,0,4270336,5907968,5833728,7483136,6234624,0,0,0,4271360,5908992,5834752,7484160,6235648,0,0,0,
1908512,981548,777376,958412,2140364,0,0,0,4272384,3753728,5835776,7485184,6236672,0,0,0,4273408,3754752,5836800,7486208,6509056,0,0,0,4274432,3755776,5975040,7758592,6510080,0,0,0,4275456,3756800,5976064,7759616,6511104,0,0,0,
1908640,981676,777504,958540,2140492,0,0,0,4412672,3757824,5977088,7760640,6512128,0,0,0,4413696,3758848,5978112,7761664,6513152,0,0,0,4414720,3759872,5979136,7762688,6785536,0,0,0,4415744,3897088,5980160,8035072,6786560,0,0,0,
1908768,981804,801824,958668,2140620,0,0,0,4416768,3898112,3824896,8036096,6787584,0,0,0,4417792,3899136,3825920,8037120,6788608,0,0,0,4418816,3900160,3826944,8038144,6789632,0,0,0,4556032,3901184,3827968,8039168,7062016,0,0,0,
1908896,981932,801952,1003596,2140748,0,0,0,4557056,3902208,3828992,6104064,7063040,0,0,0,4558080,3903232,3830016,6105088,7064064,0,0,0,4559104,4040448,3831040,6106112,7065088,0,0,0,4560128,4041472,3968256,6107136,7066112,0,0,0,
1909024,982060,802080,1003724,2140876,0,0,0,4561152,4042496,3969280,6108160,7338496,0,0,0,4562176,4043520,3970304,6380544,7339520,0,0,0,4699392,4044544,3971328,6381568,7340544,0,0,0,4700416,4045568,3972352,6382592,7341568,0,0,0,
1909152,982188,802208,1003852,2141004,0,0,0,4701440,4046592,3973376,6383616,7342592,0,0,0,4702464,4183808,3974400,6384640,7614976,0,0,0,4703488,4184832,4111616,6657024,7616000,0,0,0,4704512,4185856,4112640,6658048,7617024,0,0,0,
1909280,982316,802336,1003980,2141132,0,0,0,4705536,4186880,4113664,6659072,7618048,0,0,0,4842752,4187904,4114688,6660096,7619072,0,0,0,4843776,4188928,4115712,6661120,7891456,0,0,0,4844800,4189952,4116736,6933504,7892480,0,0,0,
1909408,982444,802464,1004108,2141260,0,0,0,4845824,4327168,4117760,6934528,7893504,0,0,0,4846848,4328192,4254976,6935552,7894528,0,0,0,4847872,4329216,4256000,6936576,7895552,0,0,0,4848896,4330240,4257024,6937600,8167936,0,0,0,
1909536,982572,802592,1004236,2141388,0,0,0,4986112,4331264,4258048,7209984,8168960,0,0,0,4987136,4332288,4259072,7211008,8169984,0,0,0,4988160,4333312,4260096,7212032,8171008,0,0,0,4989184,4470528,4261120,7213056,8172032,0,0,0,
1909664,982700,802720,1004364,2145356,0,0,0,4990208,4471552,4398336,7214080,6232832,0,0,0,4991232,4472576,4399360,7486464,6233856,0,0,0,4992256,4473600,4400384,7487488,6234880,0,0,0,5129472,4474624,4401408,7488512,6235904,0,0,0,
1909792,982828,802848,1004492,2145484,0,0,0,5130496,4475648,4402432,7489536,6236928,0,0,0,5131520,4476672,4403456,7490560,6509312,0,0,0,5132544,4613888,4404480,7762944,6510336,0,0,0,5133568,4614912,4541696,7763968,6511360,0,0,0,
1909920,982956,802976,1004620,2145612,0,0,0,5134592,4615936,4542720,7764992,6512384,0,0,0,5135616,4616960,4543744,7766016,6513408,0,0,0,5272832,4617984,4544768,7767040,6785792,0,0,0,5273856,4619008,4545792,8039424,6786816,0,0,0,
1910048,983084,803104,1004748,2145740,0,0,0,5274880,4620032,4546816,8040448,6787840,0,0,0,5275904,4757248,4547840,8041472,6788864,0,0,0,5276928,4758272,4685056,8042496,6789888,0,0,0,5277952,4759296,4686080,8043520,7062272,0,0,0,
1910176,983212,803232,1008716,2145868,0,0,0,5278976,4760320,4687104,6104320,7063296,0,0,0,5416192,4761344,4688128,6105344,7064320,0,0,0,5417216,4762368,4689152,6106368,7065344,0,0,0,5418240,4763392,4690176,6107392,7066368,0,0,0,
1910304,983340,803360,1008844,2145996,0,0,0,5419264,4900608,4691200,6108416,7338752,0,0,0,5420288,4901632,4828416,6380800,7339776,0,0,0,5421312,4902656,4829440,6381824,7340800,0,0,0,5422336,4903680,4830464,6382848,7341824,0,0,0,
1910432,983468,803488,1008972,2146124,0,0,0,5559552,4904704,4831488,6383872,7342848,0,0,0,5560576,4905728,4832512,6384896,7615232,0,0,0,5561600,4906752,4833536,6657280,7616256,0,0,0,5562624,5043968,4834560,6658304,7617280,0,0,0,
1910560,983596,803616,1009100,2146252,0,0,0,5563648,5044992,4971776,6659328,7618304,0,0,0,5564672,5046016,4972800,6660352,7619328,0,0,0,5702912,5047040,4973824,6661376,7891712,0,0,0,5703936,5048064,4974848,6933760,7892736,0,0,0,
1910688,983724,803744,1009228,2146380,0,0,0,5704960,5049088,4975872,6934784,7893760,0,0,0,5705984,5050112,4976896,6935808,7894784,0,0,0,5707008,5187328,4977920,6936832,7895808,0,0,0,5708032,5188352,5115136,6937856,8168192,0,0,0,
1910816,983852,803872,1009356,2146508,0,0,0,5846272,5189376,5116160,7210240,8169216,0,0,0,5847296,5190400,5117184,7211264,8170240,0,0,0,5848320,5191424,5118208,7212288,8171264,0,0,0,5849344,5192448,5119232,7213312,8172288,0,0,0,
1910944,983980,804000,1009484,2170956,0,0,0,5850368,5193472,5120256,7214336,6237184,0,0,0,5851392,5330688,5121280,7486720,6238208,0,0,0,5989632,5331712,5258496,7487744,6239232,0,0,0,5990656,5332736,5259520,7488768,6240256,0,0,0,
1911072,984108,804128,1009612,2171084,0,0,0,5991680,5333760,5260544,7489792,6241280,0,0,0,5992704,5334784,5261568,7490816,6513664,0,0,0,5993728,5335808,5262592,7763200,6514688,0,0,0,5994752,5336832,5263616,7764224,6515712,0,0,0,
2156576,984236,804256,1009740,2171212,0,0,0,3839488,5474048,5264640,7765248,6516736,0,0,0,3840512,5475072,5401856,7766272,6517760,0,0,0,3841536,5476096,5402880,7767296,6790144,0,0,0,3842560,5477120,5403904,8039680,6791168,0,0,0,
2156704,984364,804384,1009868,2171340,0,0,0,3843584,5478144,5404928,8040704,6792192,0,0,0,3844608,5479168,5405952,8041728,6793216,0,0,0,3845632,5617408,5406976,8042752,6794240,0,0,0,3982848,5618432,5408000,8043776,7066624,0,0,0,
2156832,984492,804512,1013836,2171468,0,0,0,3983872,5619456,5545216,6104576,7067648,0,0,0,3984896,5620480,5546240,6105600,7068672,0,0,0,3985920,5621504,5547264,6106624,7069696,0,0,0,3986944,5622528,5548288,6107648,7070720,0,0,0,
2156960,984620,804640,1013964,2171596,0,0,0,3987968,5760768,5549312,6108672,7343104,0,0,0,3988992,5761792,5550336,6381056,7344128,0,0,0,4126208,5762816,5688576,6382080,7345152,0,0,0,4127232,5763840,5689600,6383104,7346176,0,0,0,
2157088,984748,804768,1014092,2171724,0,0,0,4128256,5764864,5690624,6384128,7347200,0,0,0,4129280,5765888,5691648,6385152,7619584,0,0,0,4130304,5904128,5692672,6657536,7620608,0,0,0,4131328,5905152,5693696,6658560,7621632,0,0,0,
2157216,984876,804896,1014220,2171852,0,0,0,4132352,5906176,5831936,6659584,7622656,0,0,0,4269568,5907200,5832960,6660608,7623680,0,0,0,4270592,5908224,5833984,6661632,7896064,0,0,0,4271616,5909248,5835008,6934016,7897088,0,0,0,
2157344,1105964,805024,1014348,2171980,0,0,0,4272640,3760128,5836032,6935040,7898112,0,0,0,4273664,3761152,5837056,6936064,7899136,0,0,0,4274688,3762176,5975296,6937088,7900160,0,0,0,4275712,3763200,5976320,6938112,8172544,0,0,0,
2157472,1106092,805152,1014476,2172108,0,0,0,4412928,3764224,5977344,7210496,8173568,0,0,0,4413952,3765248,5978368,7211520,8174592,0,0,0,4414976,3766272,5979392,7212544,8175616,0,0,0,4416000,3903488,5980416,7213568,8176640,0,0,0,
2157600,1106220,1050656,1014604,2176076,0,0,0,4417024,3904512,3825152,7214592,6237440,0,0,0,4418048,3905536,3826176,7486976,6238464,0,0,0,4419072,3906560,3827200,7488000,6239488,0,0,0,4556288,3907584,3828224,7489024,6240512,0,0,0,
2157728,1106348,1050784,1014732,2176204,0,0,0,4557312,3908608,3829248,7490048,6241536,0,0,0,4558336,3909632,3830272,7491072,6513920,0,0,0,4559360,4046848,3831296,7763456,6514944,0,0,0,4560384,4047872,3968512,7764480,6515968,0,0,0,
2157856,1106476,1050912,1014860,2176332,0,0,0,4561408,4048896,3969536,7765504,6516992,0,0,0,4562432,4049920,3970560,7766528,6518016,0,0,0,4699648,4050944,3971584,7767552,6790400,0,0,0,4700672,4051968,3972608,8039936,6791424,0,0,0,
2157984,1106604,1051040,1014988,2176460,0,0,0,4701696,4052992,3973632,8040960,6792448,0,0,0,4702720,4190208,3974656,8041984,6793472,0,0,0,4703744,4191232,4111872,8043008,6794496,0,0,0,4704768,4192256,4112896,8044032,7066880,0,0,0,
2158112,1106732,1051168,1018956,2176588,0,0,0,4705792,4193280,4113920,6104832,7067904,0,0,0,4843008,4194304,4114944,6105856,7068928,0,0,0,4844032,4195328,4115968,6106880,7069952,0,0,0,4845056,4196352,4116992,6107904,7070976,0,0,0,
2158240,1106860,1051296,1019084,2176716,0,0,0,4846080,4333568,4118016,6108928,7343360,0,0,0,4847104,4334592,4255232,6381312,7344384,0,0,0,4848128,4335616,4256256,6382336,7345408,0,0,0,4849152,4336640,4257280,6383360,7346432,0,0,0,
2158368,1106988,1051424,1019212,2176844,0,0,0,4986368,4337664,4258304,6384384,7347456,0,0,0,4987392,4338688,4259328,6385408,7619840,0,0,0,4988416,4339712,4260352,6657792,7620864,0,0,0,4989440,4476928,4261376,6658816,7621888,0,0,0,
2158496,1107116,1051552,1019340,2176972,0,0,0,4990464,4477952,4398592,6659840,7622912,0,0,0,4991488,4478976,4399616,6660864,7623936,0,0,0,4992512,4480000,4400640,6661888,7896320,0,0,0,5129728,4481024,4401664,6934272,7897344,0,0,0,
2158624,1107244,1051680,1019468,2177100,0,0,0,5130752,4482048,4402688,6935296,7898368,0,0,0,5131776,4483072,4403712,6936320,7899392,0,0,0,5132800,4620288,4404736,6937344,7900416,0,0,0,5133824,4621312,4541952,6938368,8172800,0,0,0,
2158752,1107372,1051808,1019596,2177228,0,0,0,5134848,4622336,4542976,7210752,8173824,0,0,0,5135872,4623360,4544000,7211776,8174848,0,0,0,5273088,4624384,4545024,7212800,8175872,0,0,0,5274112,4625408,4546048,7213824,8176896,0,0,0,
2158880,1107500,1051936,1019724,2181196,0,0,0,5275136,4626432,4547072,7214848,6237696,0,0,0,5276160,4763648,4548096,7487232,6238720,0,0,0,5277184,4764672,4685312,7488256,6239744,0,0,0,5278208,4765696,4686336,7489280,6240768,0,0,0,
2159008,1107628,1052064,1019852,2181324,0,0,0,5279232,4766720,4687360,7490304,6241792,0,0,0,5416448,4767744,4688384,7491328,6514176,0,0,0,5417472,4768768,4689408,7763712,6515200,0,0,0,5418496,4769792,4690432,7764736,6516224,0,0,0,
2159136,1107756,1052192,1019980,2181452,0,0,0,5419520,4907008,4691456,7765760,6517248,0,0,0,5420544,4908032,4828672,7766784,6518272,0,0,0,5421568,4909056,4829696,7767808,6790656,0,0,0,5422592,4910080,4830720,8040192,6791680,0,0,0,
2159264,1107884,1052320,1020108,2181580,0,0,0,5559808,4911104,4831744,8041216,6792704,0,0,0,5560832,4912128,4832768,8042240,6793728,0,0,0,5561856,4913152,4833792,8043264,6794752,0,0,0,5562880,5050368,4834816,8044288,7067136,0,0,0,
2159392,1108012,1052448,1044556,2181708,0,0,0,5563904,5051392,4972032,6109184,7068160,0,0,0,5564928,5052416,4973056,6110208,7069184,0,0,0,5703168,5053440,4974080,6111232,7070208,0,0,0,5704192,5054464,4975104,6112256,7071232,0,0,0,
2159520,1108140,1052576,1044684,2181836,0,0,0,5705216,5055488,4976128,6113280,7343616,0,0,0,5706240,5056512,4977152,6385664,7344640,0,0,0,5707264,5193728,4978176,6386688,7345664,0,0,0,5708288,5194752,5115392,6387712,7346688,0,0,0,
2159648,1108268,1052704,1044812,2181964,0,0,0,5846528,5195776,5116416,6388736,7347712,0,0,0,5847552,5196800,5117440,6389760,7620096,0,0,0,5848576,5197824,5118464,6662144,7621120,0,0,0,5849600,5198848,5119488,6663168,7622144,0,0,0,
2159776,1108396,1052832,1044940,2182092,0,0,0,5850624,5199872,5120512,6664192,7623168,0,0,0,5851648,5337088,5121536,6665216,7624192,0,0,0,5989888,5338112,5258752,6666240,7896576,0,0,0,5990912,5339136,5259776,6938624,7897600,0,0,0,
2159904,1108524,1052960,1045068,2182220,0,0,0,5991936,5340160,5260800,6939648,7898624,0,0,0,5992960,5341184,5261824,6940672,7899648,0,0,0,5993984,5342208,5262848,6941696,7900672,0,0,0,5995008,5343232,5263872,6942720,8173056,0,0,0,
2184224,1108652,1053088,1045196,2182348,0,0,0,3839744,5480448,5264896,7215104,8174080,0,0,0,3840768,5481472,5402112,7216128,8175104,0,0,0,3841792,5482496,5403136,7217152,8176128,0,0,0,3842816,5483520,5404160,7218176,8177152,0,0,0,
2184352,1108780,1053216,1045324,2186316,0,0,0,3843840,5484544,5405184,7219200,6237952,0,0,0,3844864,5485568,5406208,7491584,6238976,0,0,0,3845888,5623808,5407232,7492608,6240000,0,0,0,3983104,5624832,5408256,7493632,6241024,0,0,0,
2184480,1108908,1053344,1045452,2186444,0,0,0,3984128,5625856,5545472,7494656,6242048,0,0,0,3985152,5626880,5546496,7495680,6514432,0,0,0,3986176,5627904,5547520,7768064,6515456,0,0,0,3987200,5628928,5548544,7769088,6516480,0,0,0,
2184608,1109036,1053472,1045580,2186572,0,0,0,3988224,5767168,5549568,7770112,6517504,0,0,0,3989248,5768192,5550592,7771136,6518528,0,0,0,4126464,5769216,5688832,7772160,6790912,0,0,0,4127488,5770240,5689856,8044544,6791936,0,0,0,
2184736,1109164,1053600,1045708,2186700,0,0,0,4128512,5771264,5690880,8045568,6792960,0,0,0,4129536,5772288,5691904,8046592,6793984,0,0,0,4130560,5910528,5692928,8047616,6795008,0,0,0,4131584,5911552,5693952,8048640,7067392,0,0,0,
2184864,1109292,1053728,1049676,2186828,0,0,0,4132608,5912576,5832192,6109440,7068416,0,0,0,4269824,5913600,5833216,6110464,7069440,0,0,0,4270848,5914624,5834240,6111488,7070464,0,0,0,4271872,5915648,5835264,6112512,7071488,0,0,0,
2184992,1119788,1053856,1049804,2186956,0,0,0,4272896,3760384,5836288,6113536,7343872,0,0,0,4273920,3761408,5837312,6385920,7344896,0,0,0,4274944,3762432,5975552,6386944,7345920,0,0,0,4275968,3763456,5976576,6387968,7346944,0,0,0,
2185120,1119916,1053984,1049932,2187084,0,0,0,4413184,3764480,5977600,6388992,7347968,0,0,0,4414208,3765504,5978624,6390016,7620352,0,0,0,4415232,3766528,5979648,6662400,7621376,0,0,0,4416256,3903744,5980672,6663424,7622400,0,0,0,
2185248,1120044,1078304,1050060,2187212,0,0,0,4417280,3904768,3825408,6664448,7623424,0,0,0,4418304,3905792,3826432,6665472,7624448,0,0,0,4419328,3906816,3827456,6666496,7896832,0,0,0,4556544,3907840,3828480,6938880,7897856,0,0,0,
2185376,1120172,1078432,1050188,2187340,0,0,0,4557568,3908864,3829504,6939904,7898880,0,0,0,4558592,3909888,3830528,6940928,7899904,0,0,0,4559616,4047104,3831552,6941952,7900928,0,0,0,4560640,4048128,3968768,6942976,8173312,0,0,0,
2185504,1120300,1078560,1050316,2187468,0,0,0,4561664,4049152,3969792,7215360,8174336,0,0,0,4562688,4050176,3970816,7216384,8175360,0,0,0,4699904,4051200,3971840,7217408,8176384,0,0,0,4700928,4052224,3972864,7218432,8177408,0,0,0,
2185632,1120428,1078688,1050444,122944,0,0,0,4701952,4053248,3973888,7219456,6242304,0,0,0,4702976,4190464,3974912,7491840,6243328,0,0,0,4704000,4191488,4112128,7492864,6244352,0,0,0,4705024,4192512,4113152,7493888,6245376,0,0,0,
2185760,1120556,1078816,1050572,123072,0,0,0,4706048,4193536,4114176,7494912,6246400,0,0,0,4843264,4194560,4115200,7495936,6518784,0,0,0,4844288,4195584,4116224,7768320,6519808,0,0,0,4845312,4196608,4117248,7769344,6520832,0,0,0,
2185888,1120684,1078944,1050700,123200,0,0,0,4846336,4333824,4118272,7770368,6521856,0,0,0,4847360,4334848,4255488,7771392,6522880,0,0,0,4848384,4335872,4256512,7772416,6795264,0,0,0,4849408,4336896,4257536,8044800,6796288,0,0,0,
2186016,1120812,1079072,1050828,123328,0,0,0,4986624,4337920,4258560,8045824,6797312,0,0,0,4987648,4338944,4259584,8046848,6798336,0,0,0,4988672,4339968,4260608,8047872,6799360,0,0,0,4989696,4477184,4261632,8048896,7071744,0,0,0,
2186144,1120940,1079200,1054796,123456,0,0,0,4990720,4478208,4398848,6109696,7072768,0,0,0,4991744,4479232,4399872,6110720,7073792,0,0,0,4992768,4480256,4400896,6111744,7074816,0,0,0,5129984,4481280,4401920,6112768,7075840,0,0,0,
2186272,1121068,1079328,1054924,123584,0,0,0,5131008,4482304,4402944,6113792,7348224,0,0,0,5132032,4483328,4403968,6386176,7349248,0,0,0,5133056,4620544,4404992,6387200,7350272,0,0,0,5134080,4621568,4542208,6388224,7351296,0,0,0,
2186400,1121196,1079456,1055052,123712,0,0,0,5135104,4622592,4543232,6389248,7352320,0,0,0,5136128,4623616,4544256,6390272,7624704,0,0,0,5273344,4624640,4545280,6662656,7625728,0,0,0,5274368,4625664,4546304,6663680,7626752,0,0,0,
2186528,1121324,1079584,1055180,123840,0,0,0,5275392,4626688,4547328,6664704,7627776,0,0,0,5276416,4763904,4548352,6665728,7628800,0,0,0,5277440,4764928,4685568,6666752,7901184,0,0,0,5278464,4765952,4686592,6939136,7902208,0,0,0,
2186656,1121452,1079712,1055308,123968,0,0,0,5279488,4766976,4687616,6940160,7903232,0,0,0,5416704,4768000,4688640,6941184,7904256,0,0,0,5417728,4769024,4689664,6942208,7905280,0,0,0,5418752,4770048,4690688,6943232,8177664,0,0,0,
2186784,1121580,1079840,1055436,124096,0,0,0,5419776,4907264,4691712,7215616,8178688,0,0,0,5420800,4908288,4828928,7216640,8179712,0,0,0,5421824,4909312,4829952,7217664,8180736,0,0,0,5422848,4910336,4830976,7218688,8181760,0,0,0,
2186912,1121708,1079968,1055564,133184,0,0,0,5560064,4911360,4832000,7219712,6242560,0,0,0,5561088,4912384,4833024,7492096,6243584,0,0,0,5562112,4913408,4834048,7493120,6244608,0,0,0,5563136,5050624,4835072,7494144,6245632,0,0,0,
2187040,1121836,1080096,1055692,133312,0,0,0,5564160,5051648,4972288,7495168,6246656,0,0,0,5565184,5052672,4973312,7496192,6519040,0,0,0,5703424,5053696,4974336,7768576,6520064,0,0,0,5704448,5054720,4975360,7769600,6521088,0,0,0,
2187168,1121964,1080224,1055820,133440,0,0,0,5705472,5055744,4976384,7770624,6522112,0,0,0,5706496,5056768,4977408,7771648,6523136,0,0,0,5707520,5193984,4978432,7772672,6795520,0,0,0,5708544,5195008,5115648,8045056,6796544,0,0,0,
2187296,1122092,1080352,1055948,133568,0,0,0,5846784,5196032,5116672,8046080,6797568,0,0,0,5847808,5197056,5117696,8047104,6798592,0,0,0,5848832,5198080,5118720,8048128,6799616,0,0,0,5849856,5199104,5119744,8049152,7072000,0,0,0,
2187424,1122220,1080480,1059916,133696,0,0,0,5850880,5200128,5120768,6109952,7073024,0,0,0,5851904,5337344,5121792,6110976,7074048,0,0,0,5990144,5338368,5259008,6112000,7075072,0,0,0,5991168,5339392,5260032,6113024,7076096,0,0,0,
2187552,1122348,1080608,1060044,133824,0,0,0,5992192,5340416,5261056,6114048,7348480,0,0,0,5993216,5341440,5262080,6386432,7349504,0,0,0,5994240,5342464,5263104,6387456,7350528,0,0,0,5995264,5343488,5264128,6388480,7351552,0,0,0,
984128,1122476,1080736,1060172,133952,0,0,0,7918592,5480704,5265152,6389504,7352576,0,0,0,7919616,5481728,5402368,6390528,7624960,0,0,0,7920640,5482752,5403392,6662912,7625984,0,0,0,8193024,5483776,5404416,6663936,7627008,0,0,0,
984256,1122604,1080864,1060300,134080,0,0,0,8194048,5484800,5405440,6664960,7628032,0,0,0,8195072,5485824,5406464,6665984,7629056,0,0,0,8196096,5624064,5407488,6667008,7901440,0,0,0,8197120,5625088,5408512,6939392,7902464,0,0,0,
993344,1122732,1080992,1060428,134208,0,0,0,6257920,5626112,5545728,6940416,7903488,0,0,0,6258944,5627136,5546752,6941440,7904512,0,0,0,6259968,5628160,5547776,6942464,7905536,0,0,0,6260992,5629184,5548800,6943488,8177920,0,0,0,
993472,1122860,1081120,1060556,134336,0,0,0,6262016,5767424,5549824,7215872,8178944,0,0,0,6534400,5768448,5550848,7216896,8179968,0,0,0,6535424,5769472,5689088,7217920,8180992,0,0,0,6536448,5770496,5690112,7218944,8182016,0,0,0,
993600,1122988,1081248,1060684,266304,0,0,0,6537472,5771520,5691136,7219968,6242816,0,0,0,6538496,5772544,5692160,7492352,6243840,0,0,0,6810880,5910784,5693184,7493376,6244864,0,0,0,6811904,5911808,5694208,7494400,6245888,0,0,0,
993728,1123116,1081376,1060812,266432,0,0,0,6812928,5912832,5832448,7495424,6246912,0,0,0,6813952,5913856,5833472,7496448,6519296,0,0,0,6814976,5914880,5834496,7768832,6520320,0,0,0,7087360,5915904,5835520,7769856,6521344,0,0,0,
993856,1133612,1081504,1060940,266560,0,0,0,7088384,3760640,5836544,7770880,6522368,0,0,0,7089408,3761664,5837568,7771904,6523392,0,0,0,7090432,3762688,5975808,7772928,6795776,0,0,0,7091456,3763712,5976832,8045312,6796800,0,0,0,
993984,1133740,1081632,1061068,266688,0,0,0,7363840,3764736,5977856,8046336,6797824,0,0,0,7364864,3765760,5978880,8047360,6798848,0,0,0,7365888,3766784,5979904,8048384,6799872,0,0,0,7366912,3904000,5980928,8049408,7072256,0,0,0,
994112,1133868,1327136,1085516,266816,0,0,0,7367936,3905024,3831808,6114304,7073280,0,0,0,7640320,3906048,3832832,6115328,7074304,0,0,0,7641344,3907072,3833856,6116352,7075328,0,0,0,7642368,3908096,3834880,6117376,7076352,0,0,0,
994240,1133996,1327264,1085644,266944,0,0,0,7643392,3909120,3835904,6118400,7348736,0,0,0,7644416,3910144,3836928,6390784,7349760,0,0,0,7916800,4047360,3837952,6391808,7350784,0,0,0,7917824,4048384,3975168,6392832,7351808,0,0,0,
994368,1134124,1327392,1085772,267072,0,0,0,7918848,4049408,3976192,6393856,7352832,0,0,0,7919872,4050432,3977216,6394880,7625216,0,0,0,7920896,4051456,3978240,6667264,7626240,0,0,0,8193280,4052480,3979264,6668288,7627264,0,0,0,
994496,1134252,1327520,1085900,267200,0,0,0,8194304,4053504,3980288,6669312,7628288,0,0,0,8195328,4190720,3981312,6670336,7629312,0,0,0,8196352,4191744,4118528,6671360,7901696,0,0,0,8197376,4192768,4119552,6943744,7902720,0,0,0,
1126464,1134380,1327648,1086028,267328,0,0,0,6258176,4193792,4120576,6944768,7903744,0,0,0,6259200,4194816,4121600,6945792,7904768,0,0,0,6260224,4195840,4122624,6946816,7905792,0,0,0,6261248,4196864,4123648,6947840,8178176,0,0,0,
1126592,1134508,1327776,1086156,267456,0,0,0,6262272,4334080,4124672,7220224,8179200,0,0,0,6534656,4335104,4261888,7221248,8180224,0,0,0,6535680,4336128,4262912,7222272,8181248,0,0,0,6536704,4337152,4263936,7223296,8182272,0,0,0,
1126720,1134636,1327904,1086284,276544,0,0,0,6537728,4338176,4264960,7224320,6243072,0,0,0,6538752,4339200,4265984,7496704,6244096,0,0,0,6811136,4340224,4267008,7497728,6245120,0,0,0,6812160,4477440,4268032,7498752,6246144,0,0,0,
1126848,1134764,1328032,1086412,276672,0,0,0,6813184,4478464,4405248,7499776,6247168,0,0,0,6814208,4479488,4406272,7500800,6519552,0,0,0,6815232,4480512,4407296,7773184,6520576,0,0,0,7087616,4481536,4408320,7774208,6521600,0,0,0,
1126976,1134892,1328160,1086540,276800,0,0,0,7088640,4482560,4409344,7775232,6522624,0,0,0,7089664,4483584,4410368,7776256,6523648,0,0,0,7090688,4620800,4411392,7777280,6796032,0,0,0,7091712,4621824,4548608,8049664,6797056,0,0,0,
1127104,1135020,1328288,1086668,276928,0,0,0,7364096,4622848,4549632,8050688,6798080,0,0,0,7365120,4623872,4550656,8051712,6799104,0,0,0,7366144,4624896,4551680,8052736,6800128,0,0,0,7367168,4625920,4552704,8053760,7072512,0,0,0,
1127232,1135148,1328416,1090636,277056,0,0,0,7368192,4626944,4553728,6114560,7073536,0,0,0,7640576,4764160,4554752,6115584,7074560,0,0,0,7641600,4765184,4691968,6116608,7075584,0,0,0,7642624,4766208,4692992,6117632,7076608,0,0,0,
1127360,1135276,1328544,1090764,277184,0,0,0,7643648,4767232,4694016,6118656,7348992,0,0,0,7644672,4768256,4695040,6391040,7350016,0,0,0,7917056,4769280,4696064,6392064,7351040,0,0,0,7918080,4770304,4697088,6393088,7352064,0,0,0,
1127488,1135404,1328672,1090892,277312,0,0,0,7919104,4907520,4698112,6394112,7353088,0,0,0,7920128,4908544,4835328,6395136,7625472,0,0,0,7921152,4909568,4836352,6667520,7626496,0,0,0,8193536,4910592,4837376,6668544,7627520,0,0,0,
1127616,1135532,1328800,1091020,277440,0,0,0,8194560,4911616,4838400,6669568,7628544,0,0,0,8195584,4912640,4839424,6670592,7629568,0,0,0,8196608,4913664,4840448,6671616,7901952,0,0,0,8197632,5050880,4841472,6944000,7902976,0,0,0,
1136704,1135660,1328928,1091148,277568,0,0,0,6258432,5051904,4978688,6945024,7904000,0,0,0,6259456,5052928,4979712,6946048,7905024,0,0,0,6260480,5053952,4980736,6947072,7906048,0,0,0,6261504,5054976,4981760,6948096,8178432,0,0,0,
1136832,1135788,1329056,1091276,277696,0,0,0,6262528,5056000,4982784,7220480,8179456,0,0,0,6534912,5057024,4983808,7221504,8180480,0,0,0,6535936,5194240,4984832,7222528,8181504,0,0,0,6536960,5195264,5122048,7223552,8182528,0,0,0,
1136960,1135916,1329184,1091404,409664,0,0,0,6537984,5196288,5123072,7224576,6247424,0,0,0,6539008,5197312,5124096,7496960,6248448,0,0,0,6811392,5198336,5125120,7497984,6249472,0,0,0,6812416,5199360,5126144,7499008,6250496,0,0,0,
1137088,1136044,1329312,1091532,409792,0,0,0,6813440,5200384,5127168,7500032,6251520,0,0,0,6814464,5337600,5128192,7501056,6523904,0,0,0,6815488,5338624,5265408,7773440,6524928,0,0,0,7087872,5339648,5266432,7774464,6525952,0,0,0,
1137216,1136172,1329440,1091660,409920,0,0,0,7088896,5340672,5267456,7775488,6526976,0,0,0,7089920,5341696,5268480,7776512,6528000,0,0,0,7090944,5342720,5269504,7777536,6800384,0,0,0,7091968,5343744,5270528,8049920,6801408,0,0,0,
1137344,1136300,1329568,1091788,410048,0,0,0,7364352,5480960,5271552,8050944,6802432,0,0,0,7365376,5481984,5408768,8051968,6803456,0,0,0,7366400,5483008,5409792,8052992,6804480,0,0,0,7367424,5484032,5410816,8054016,7076864,0,0,0,
1137472,1136428,1329696,1095756,410176,0,0,0,7368448,5485056,5411840,6114816,7077888,0,0,0,7640832,5486080,5412864,6115840,7078912,0,0,0,7641856,5624320,5413888,6116864,7079936,0,0,0,7642880,5625344,5414912,6117888,7080960,0,0,0,
1137600,1136556,1329824,1095884,410304,0,0,0,7643904,5626368,5552128,6118912,7353344,0,0,0,7644928,5627392,5553152,6391296,7354368,0,0,0,7917312,5628416,5554176,6392320,7355392,0,0,0,7918336,5629440,5555200,6393344,7356416,0,0,0,
1137728,1136684,1329952,1096012,410432,0,0,0,7919360,5767680,5556224,6394368,7357440,0,0,0,7920384,5768704,5557248,6395392,7629824,0,0,0,7921408,5769728,5695488,6667776,7630848,0,0,0,8193792,5770752,5696512,6668800,7631872,0,0,0,
1137856,1136812,1330080,1096140,410560,0,0,0,8194816,5771776,5697536,6669824,7632896,0,0,0,8195840,5772800,5698560,6670848,7633920,0,0,0,8196864,5911040,5699584,6671872,7906304,0,0,0,8197888,5912064,5700608,6944256,7907328,0,0,0,
1269824,1136940,1330208,1096268,410688,0,0,0,6262784,5913088,5838848,6945280,7908352,0,0,0,6263808,5914112,5839872,6946304,7909376,0,0,0,6264832,5915136,5840896,6947328,7910400,0,0,0,6265856,5916160,5841920,6948352,8182784,0,0,0,
1269952,1147436,1330336,1096396,410816,0,0,0,6266880,3760896,5842944,7220736,8183808,0,0,0,6539264,3761920,5843968,7221760,8184832,0,0,0,6540288,3762944,5982208,7222784,8185856,0,0,0,6541312,3763968,5983232,7223808,8186880,0,0,0,
1270080,1147564,1330464,1096524,419904,0,0,0,6542336,3764992,5984256,7224832,6247680,0,0,0,6543360,3766016,5985280,7497216,6248704,0,0,0,6815744,3767040,5986304,7498240,6249728,0,0,0,6816768,3904256,5987328,7499264,6250752,0,0,0,
1270208,1147692,1354784,1096652,420032,0,0,0,6817792,3905280,3832064,7500288,6251776,0,0,0,6818816,3906304,3833088,7501312,6524160,0,0,0,6819840,3907328,3834112,7773696,6525184,0,0,0,7092224,3908352,3835136,7774720,6526208,0,0,0,
1270336,1147820,1354912,1096780,420160,0,0,0,7093248,3909376,3836160,7775744,6527232,0,0,0,7094272,3910400,3837184,7776768,6528256,0,0,0,7095296,4047616,3838208,7777792,6800640,0,0,0,7096320,4048640,3975424,8050176,6801664,0,0,0,
1270464,1147948,1355040,1096908,420288,0,0,0,7368704,4049664,3976448,8051200,6802688,0,0,0,7369728,4050688,3977472,8052224,6803712,0,0,0,7370752,4051712,3978496,8053248,6804736,0,0,0,7371776,4052736,3979520,8054272,7077120,0,0,0,
1270592,1148076,1355168,1100876,420416,0,0,0,7372800,4053760,3980544,6115072,7078144,0,0,0,7645184,4190976,3981568,6116096,7079168,0,0,0,7646208,4192000,4118784,6117120,7080192,0,0,0,7647232,4193024,4119808,6118144,7081216,0,0,0,
1270720,1148204,1355296,1101004,420544,0,0,0,7648256,4194048,4120832,6119168,7353600,0,0,0,7649280,4195072,4121856,6391552,7354624,0,0,0,7921664,4196096,4122880,6392576,7355648,0,0,0,7922688,4197120,4123904,6393600,7356672,0,0,0,
1270848,1148332,1355424,1101132,420672,0,0,0,7923712,4334336,4124928,6394624,7357696,0,0,0,7924736,4335360,4262144,6395648,7630080,0,0,0,7925760,4336384,4263168,6668032,7631104,0,0,0,8198144,4337408,4264192,6669056,7632128,0,0,0,
1270976,1148460,1355552,1101260,420800,0,0,0,8199168,4338432,4265216,6670080,7633152,0,0,0,8200192,4339456,4266240,6671104,7634176,0,0,0,8201216,4340480,4267264,6672128,7906560,0,0,0,8202240,4477696,4268288,6944512,7907584,0,0,0,
1280064,1148588,1355680,1101388,420928,0,0,0,6263040,4478720,4405504,6945536,7908608,0,0,0,6264064,4479744,4406528,6946560,7909632,0,0,0,6265088,4480768,4407552,6947584,7910656,0,0,0,6266112,4481792,4408576,6948608,8183040,0,0,0,
1280192,1148716,1355808,1101516,421056,0,0,0,6267136,4482816,4409600,7220992,8184064,0,0,0,6539520,4483840,4410624,7222016,8185088,0,0,0,6540544,4621056,4411648,7223040,8186112,0,0,0,6541568,4622080,4548864,7224064,8187136,0,0,0,
1280320,1148844,1355936,1101644,553024,0,0,0,6542592,4623104,4549888,7225088,6247936,0,0,0,6543616,4624128,4550912,7497472,6248960,0,0,0,6816000,4625152,4551936,7498496,6249984,0,0,0,6817024,4626176,4552960,7499520,6251008,0,0,0,
1280448,1148972,1356064,1101772,553152,0,0,0,6818048,4627200,4553984,7500544,6252032,0,0,0,6819072,4764416,4555008,7501568,6524416,0,0,0,6820096,4765440,4692224,7773952,6525440,0,0,0,7092480,4766464,4693248,7774976,6526464,0,0,0,
1280576,1149100,1356192,1101900,553280,0,0,0,7093504,4767488,4694272,7776000,6527488,0,0,0,7094528,4768512,4695296,7777024,6528512,0,0,0,7095552,4769536,4696320,7778048,6800896,0,0,0,7096576,4770560,4697344,8050432,6801920,0,0,0,
1280704,1149228,1356320,1102028,553408,0,0,0,7368960,4907776,4698368,8051456,6802944,0,0,0,7369984,4908800,4835584,8052480,6803968,0,0,0,7371008,4909824,4836608,8053504,6804992,0,0,0,7372032,4910848,4837632,8054528,7077376,0,0,0,
1280832,1149356,1356448,1146956,553536,0,0,0,7373056,4911872,4838656,6119424,7078400,0,0,0,7645440,4912896,4839680,6120448,7079424,0,0,0,7646464,4913920,4840704,6121472,7080448,0,0,0,7647488,5051136,4841728,6122496,7081472,0,0,0,
1280960,1149484,1356576,1147084,553664,0,0,0,7648512,5052160,4978944,6123520,7353856,0,0,0,7649536,5053184,4979968,6395904,7354880,0,0,0,7921920,5054208,4980992,6396928,7355904,0,0,0,7922944,5055232,4982016,6397952,7356928,0,0,0,
1281088,1149612,1356704,1147212,553792,0,0,0,7923968,5056256,4983040,6398976,7357952,0,0,0,7924992,5057280,4984064,6400000,7630336,0,0,0,7926016,5194496,4985088,6672384,7631360,0,0,0,8198400,5195520,5122304,6673408,7632384,0,0,0,
1281216,1149740,1356832,1147340,553920,0,0,0,8199424,5196544,5123328,6674432,7633408,0,0,0,8200448,5197568,5124352,6675456,7634432,0,0,0,8201472,5198592,5125376,6676480,7906816,0,0,0,8202496,5199616,5126400,6948864,7907840,0,0,0,
1413184,1149868,1356960,1147468,554048,0,0,0,6263296,5200640,5127424,6949888,7908864,0,0,0,6264320,5337856,5128448,6950912,7909888,0,0,0,6265344,5338880,5265664,6951936,7910912,0,0,0,6266368,5339904,5266688,6952960,8183296,0,0,0,
1413312,1149996,1357088,1147596,554176,0,0,0,6267392,5340928,5267712,7225344,8184320,0,0,0,6539776,5341952,5268736,7226368,8185344,0,0,0,6540800,5342976,5269760,7227392,8186368,0,0,0,6541824,5344000,5270784,7228416,8187392,0,0,0,
1413440,1150124,1357216,1147724,563264,0,0,0,6542848,5481216,5271808,7229440,6248192,0,0,0,6543872,5482240,5409024,7501824,6249216,0,0,0,6816256,5483264,5410048,7502848,6250240,0,0,0,6817280,5484288,5411072,7503872,6251264,0,0,0,
1413568,1150252,1357344,1147852,563392,0,0,0,6818304,5485312,5412096,7504896,6252288,0,0,0,6819328,5486336,5413120,7505920,6524672,0,0,0,6820352,5624576,5414144,7778304,6525696,0,0,0,7092736,5625600,5415168,7779328,6526720,0,0,0,
1413696,1150380,1357472,1147980,563520,0,0,0,7093760,5626624,5552384,7780352,6527744,0,0,0,7094784,5627648,5553408,7781376,6528768,0,0,0,7095808,5628672,5554432,7782400,6801152,0,0,0,7096832,5629696,5555456,8054784,6802176,0,0,0,
1413824,1150508,1357600,1148108,563648,0,0,0,7369216,5767936,5556480,8055808,6803200,0,0,0,7370240,5768960,5557504,8056832,6804224,0,0,0,7371264,5769984,5695744,8057856,6805248,0,0,0,7372288,5771008,5696768,8058880,7077632,0,0,0,
1413952,1150636,1357728,1152076,563776,0,0,0,7373312,5772032,5697792,6119680,7078656,0,0,0,7645696,5773056,5698816,6120704,7079680,0,0,0,7646720,5911296,5699840,6121728,7080704,0,0,0,7647744,5912320,5700864,6122752,7081728,0,0,0,
1414080,1150764,1357856,1152204,563904,0,0,0,7648768,5913344,5839104,6123776,7354112,0,0,0,7649792,5914368,5840128,6396160,7355136,0,0,0,7922176,5915392,5841152,6397184,7356160,0,0,0,7923200,5916416,5842176,6398208,7357184,0,0,0,
1414208,1216556,1357984,1152332,564032,0,0,0,7924224,3767296,5843200,6399232,7358208,0,0,0,7925248,3768320,5844224,6400256,7630592,0,0,0,7926272,3769344,5982464,6672640,7631616,0,0,0,8198656,3770368,5983488,6673664,7632640,0,0,0,
1414336,1216684,1358112,1152460,564160,0,0,0,8199680,3771392,5984512,6674688,7633664,0,0,0,8200704,3772416,5985536,6675712,7634688,0,0,0,8201728,3773440,5986560,6676736,7907072,0,0,0,8202752,3910656,5987584,6949120,7908096,0,0,0,
1423424,1216812,1603616,1152588,564288,0,0,0,6263552,3911680,3832320,6950144,7909120,0,0,0,6264576,3912704,3833344,6951168,7910144,0,0,0,6265600,3913728,3834368,6952192,7911168,0,0,0,6266624,3914752,3835392,6953216,8183552,0,0,0,
1423552,1216940,1603744,1152716,564416,0,0,0,6267648,3915776,3836416,7225600,8184576,0,0,0,6540032,3916800,3837440,7226624,8185600,0,0,0,6541056,4054016,3838464,7227648,8186624,0,0,0,6542080,4055040,3975680,7228672,8187648,0,0,0,
1423680,1217068,1603872,1152844,696384,0,0,0,6543104,4056064,3976704,7229696,6252544,0,0,0,6544128,4057088,3977728,7502080,6253568,0,0,0,6816512,4058112,3978752,7503104,6254592,0,0,0,6817536,4059136,3979776,7504128,6255616,0,0,0,
1423808,1217196,1604000,1152972,696512,0,0,0,6818560,4060160,3980800,7505152,6256640,0,0,0,6819584,4197376,3981824,7506176,6529024,0,0,0,6820608,4198400,4119040,7778560,6530048,0,0,0,7092992,4199424,4120064,7779584,6531072,0,0,0,
1423936,1217324,1604128,1153100,696640,0,0,0,7094016,4200448,4121088,7780608,6532096,0,0,0,7095040,4201472,4122112,7781632,6533120,0,0,0,7096064,4202496,4123136,7782656,6805504,0,0,0,7097088,4203520,4124160,8055040,6806528,0,0,0,
1424064,1217452,1604256,1153228,696768,0,0,0,7369472,4340736,4125184,8056064,6807552,0,0,0,7370496,4341760,4262400,8057088,6808576,0,0,0,7371520,4342784,4263424,8058112,6809600,0,0,0,7372544,4343808,4264448,8059136,7081984,0,0,0,
1424192,1217580,1604384,1157196,696896,0,0,0,7373568,4344832,4265472,6119936,7083008,0,0,0,7645952,4345856,4266496,6120960,7084032,0,0,0,7646976,4346880,4267520,6121984,7085056,0,0,0,7648000,4484096,4268544,6123008,7086080,0,0,0,
1424320,1217708,1604512,1157324,697024,0,0,0,7649024,4485120,4405760,6124032,7358464,0,0,0,7650048,4486144,4406784,6396416,7359488,0,0,0,7922432,4487168,4407808,6397440,7360512,0,0,0,7923456,4488192,4408832,6398464,7361536,0,0,0,
1424448,1217836,1604640,1157452,697152,0,0,0,7924480,4489216,4409856,6399488,7362560,0,0,0,7925504,4490240,4410880,6400512,7634944,0,0,0,7926528,4627456,4411904,6672896,7635968,0,0,0,8198912,4628480,4549120,6673920,7636992,0,0,0,
1424576,1217964,1604768,1157580,697280,0,0,0,8199936,4629504,4550144,6674944,7638016,0,0,0,8200960,4630528,4551168,6675968,7639040,0,0,0,8201984,4631552,4552192,6676992,7911424,0,0,0,8203008,4632576,4553216,6949376,7912448,0,0,0,
1556544,1218092,1604896,1157708,697408,0,0,0,6267904,4633600,4554240,6950400,7913472,0,0,0,6268928,4770816,4555264,6951424,7914496,0,0,0,6269952,4771840,4692480,6952448,7915520,0,0,0,6270976,4772864,4693504,6953472,8187904,0,0,0,
1556672,1218220,1605024,1157836,697536,0,0,0,6272000,4773888,4694528,7225856,8188928,0,0,0,6544384,4774912,4695552,7226880,8189952,0,0,0,6545408,4775936,4696576,7227904,8190976,0,0,0,6546432,4776960,4697600,7228928,8192000,0,0,0,
1556800,1218348,1605152,1157964,706624,0,0,0,6547456,4914176,4698624,7229952,6252800,0,0,0,6548480,4915200,4835840,7502336,6253824,0,0,0,6820864,4916224,4836864,7503360,6254848,0,0,0,6821888,4917248,4837888,7504384,6255872,0,0,0,
1556928,1218476,1605280,1158092,706752,0,0,0,6822912,4918272,4838912,7505408,6256896,0,0,0,6823936,4919296,4839936,7506432,6529280,0,0,0,6824960,4920320,4840960,7778816,6530304,0,0,0,7097344,5057536,4841984,7779840,6531328,0,0,0,
1557056,1218604,1605408,1158220,706880,0,0,0,7098368,5058560,4979200,7780864,6532352,0,0,0,7099392,5059584,4980224,7781888,6533376,0,0,0,7100416,5060608,4981248,7782912,6805760,0,0,0,7101440,5061632,4982272,8055296,6806784,0,0,0,
1557184,1218732,1605536,1158348,707008,0,0,0,7373824,5062656,4983296,8056320,6807808,0,0,0,7374848,5063680,4984320,8057344,6808832,0,0,0,7375872,5200896,4985344,8058368,6809856,0,0,0,7376896,5201920,5122560,8059392,7082240,0,0,0,
1557312,1218860,1605664,1162316,707136,0,0,0,7377920,5202944,5123584,6120192,7083264,0,0,0,7650304,5203968,5124608,6121216,7084288,0,0,0,7651328,5204992,5125632,6122240,7085312,0,0,0,7652352,5206016,5126656,6123264,7086336,0,0,0,
1557440,1218988,1605792,1162444,707264,0,0,0,7653376,5207040,5127680,6124288,7358720,0,0,0,7654400,5344256,5128704,6396672,7359744,0,0,0,7926784,5345280,5265920,6397696,7360768,0,0,0,7927808,5346304,5266944,6398720,7361792,0,0,0,
1557568,1219116,1605920,1162572,707392,0,0,0,7928832,5347328,5267968,6399744,7362816,0,0,0,7929856,5348352,5268992,6400768,7635200,0,0,0,7930880,5349376,5270016,6673152,7636224,0,0,0,8203264,5350400,5271040,6674176,7637248,0,0,0,
1557696,1219244,1606048,1162700,707520,0,0,0,8204288,5487616,5272064,6675200,7638272,0,0,0,8205312,5488640,5409280,6676224,7639296,0,0,0,8206336,5489664,5410304,6677248,7911680,0,0,0,8207360,5490688,5411328,6949632,7912704,0,0,0,
1566784,1219372,1606176,1162828,707648,0,0,0,6268160,5491712,5412352,6950656,7913728,0,0,0,6269184,5492736,5413376,6951680,7914752,0,0,0,6270208,5630976,5414400,6952704,7915776,0,0,0,6271232,5632000,5415424,6953728,8188160,0,0,0,
1566912,1219500,1606304,1162956,707776,0,0,0,6272256,5633024,5552640,7226112,8189184,0,0,0,6544640,5634048,5553664,7227136,8190208,0,0,0,6545664,5635072,5554688,7228160,8191232,0,0,0,6546688,5636096,5555712,7229184,8192256,0,0,0,
1567040,1219628,1606432,1163084,839744,0,0,0,6547712,5774336,5556736,7230208,6253056,0,0,0,6548736,5775360,5557760,7502592,6254080,0,0,0,6821120,5776384,5696000,7503616,6255104,0,0,0,6822144,5777408,5697024,7504640,6256128,0,0,0,
1567168,1219756,1606560,1163212,839872,0,0,0,6823168,5778432,5698048,7505664,6257152,0,0,0,6824192,5779456,5699072,7506688,6529536,0,0,0,6825216,5917696,5700096,7779072,6530560,0,0,0,7097600,5918720,5701120,7780096,6531584,0,0,0,
1567296,1219884,1606688,1163340,840000,0,0,0,7098624,5919744,5839360,7781120,6532608,0,0,0,7099648,5920768,5840384,7782144,6533632,0,0,0,7100672,5921792,5841408,7783168,6806016,0,0,0,7101696,5922816,5842432,8055552,6807040,0,0,0,
1567424,1230380,1606816,1163468,840128,0,0,0,7374080,3767552,5843456,8056576,6808064,0,0,0,7375104,3768576,5844480,8057600,6809088,0,0,0,7376128,3769600,5982720,8058624,6810112,0,0,0,7377152,3770624,5983744,8059648,7082496,0,0,0,
1567552,1230508,1606944,1187916,840256,0,0,0,7378176,3771648,5984768,6124544,7083520,0,0,0,7650560,3772672,5985792,6125568,7084544,0,0,0,7651584,3773696,5986816,6126592,7085568,0,0,0,7652608,3910912,5987840,6127616,7086592,0,0,0,
1567680,1230636,1631264,1188044,840384,0,0,0,7653632,3911936,3832576,6128640,7358976,0,0,0,7654656,3912960,3833600,6401024,7360000,0,0,0,7927040,3913984,3834624,6402048,7361024,0,0,0,7928064,3915008,3835648,6403072,7362048,0,0,0,
1567808,1230764,1631392,1188172,840512,0,0,0,7929088,3916032,3836672,6404096,7363072,0,0,0,7930112,3917056,3837696,6405120,7635456,0,0,0,7931136,4054272,3838720,6677504,7636480,0,0,0,8203520,4055296,3975936,6678528,7637504,0,0,0,
1567936,1230892,1631520,1188300,840640,0,0,0,8204544,4056320,3976960,6679552,7638528,0,0,0,8205568,4057344,3977984,6680576,7639552,0,0,0,8206592,4058368,3979008,6681600,7911936,0,0,0,8207616,4059392,3980032,6953984,7912960,0,0,0,
1699904,1231020,1631648,1188428,840768,0,0,0,6268416,4060416,3981056,6955008,7913984,0,0,0,6269440,4197632,3982080,6956032,7915008,0,0,0,6270464,4198656,4119296,6957056,7916032,0,0,0,6271488,4199680,4120320,6958080,8188416,0,0,0,
1700032,1231148,1631776,1188556,840896,0,0,0,6272512,4200704,4121344,7230464,8189440,0,0,0,6544896,4201728,4122368,7231488,8190464,0,0,0,6545920,4202752,4123392,7232512,8191488,0,0,0,6546944,4203776,4124416,7233536,8192512,0,0,0,
1700160,1231276,1631904,1188684,849984,0,0,0,6547968,4340992,4125440,7234560,6253312,0,0,0,6548992,4342016,4262656,7506944,6254336,0,0,0,6821376,4343040,4263680,7507968,6255360,0,0,0,6822400,4344064,4264704,7508992,6256384,0,0,0,
1700288,1231404,1632032,1188812,850112,0,0,0,6823424,4345088,4265728,7510016,6257408,0,0,0,6824448,4346112,4266752,7511040,6529792,0,0,0,6825472,4347136,4267776,7783424,6530816,0,0,0,7097856,4484352,4268800,7784448,6531840,0,0,0,
1700416,1231532,1632160,1188940,850240,0,0,0,7098880,4485376,4406016,7785472,6532864,0,0,0,7099904,4486400,4407040,7786496,6533888,0,0,0,7100928,4487424,4408064,7787520,6806272,0,0,0,7101952,4488448,4409088,8059904,6807296,0,0,0,
1700544,1231660,1632288,1189068,850368,0,0,0,7374336,4489472,4410112,8060928,6808320,0,0,0,7375360,4490496,4411136,8061952,6809344,0,0,0,7376384,4627712,4412160,8062976,6810368,0,0,0,7377408,4628736,4549376,8064000,7082752,0,0,0,
1700672,1231788,1632416,1193036,850496,0,0,0,7378432,4629760,4550400,6124800,7083776,0,0,0,7650816,4630784,4551424,6125824,7084800,0,0,0,7651840,4631808,4552448,6126848,7085824,0,0,0,7652864,4632832,4553472,6127872,7086848,0,0,0,
1700800,1231916,1632544,1193164,850624,0,0,0,7653888,4633856,4554496,6128896,7359232,0,0,0,7654912,4771072,4555520,6401280,7360256,0,0,0,7927296,4772096,4692736,6402304,7361280,0,0,0,7928320,4773120,4693760,6403328,7362304,0,0,0,
1700928,1232044,1632672,1193292,850752,0,0,0,7929344,4774144,4694784,6404352,7363328,0,0,0,7930368,4775168,4695808,6405376,7635712,0,0,0,7931392,4776192,4696832,6677760,7636736,0,0,0,8203776,4777216,4697856,6678784,7637760,0,0,0,
1701056,1232172,1632800,1193420,850880,0,0,0,8204800,4914432,4698880,6679808,7638784,0,0,0,8205824,4915456,4836096,6680832,7639808,0,0,0,8206848,4916480,4837120,6681856,7912192,0,0,0,8207872,4917504,4838144,6954240,7913216,0,0,0,
1710144,1232300,1632928,1193548,851008,0,0,0,6268672,4918528,4839168,6955264,7914240,0,0,0,6269696,4919552,4840192,6956288,7915264,0,0,0,6270720,4920576,4841216,6957312,7916288,0,0,0,6271744,5057792,4842240,6958336,8188672,0,0,0,
1710272,1232428,1633056,1193676,851136,0,0,0,6272768,5058816,4979456,7230720,8189696,0,0,0,6545152,5059840,4980480,7231744,8190720,0,0,0,6546176,5060864,4981504,7232768,8191744,0,0,0,6547200,5061888,4982528,7233792,8192768,0,0,0,
1710400,1232556,1633184,1193804,983104,0,0,0,6548224,5062912,4983552,7234816,6257664,0,0,0,6549248,5063936,4984576,7507200,6258688,0,0,0,6821632,5201152,4985600,7508224,6259712,0,0,0,6822656,5202176,5122816,7509248,6260736,0,0,0,
1710528,1232684,1633312,1193932,983232,0,0,0,6823680,5203200,5123840,7510272,6261760,0,0,0,6824704,5204224,5124864,7511296,6534144,0,0,0,6825728,5205248,5125888,7783680,6535168,0,0,0,7098112,5206272,5126912,7784704,6536192,0,0,0,
1710656,1232812,1633440,1194060,983360,0,0,0,7099136,5207296,5127936,7785728,6537216,0,0,0,7100160,5344512,5128960,7786752,6538240,0,0,0,7101184,5345536,5266176,7787776,6810624,0,0,0,7102208,5346560,5267200,8060160,6811648,0,0,0,
1710784,1232940,1633568,1194188,983488,0,0,0,7374592,5347584,5268224,8061184,6812672,0,0,0,7375616,5348608,5269248,8062208,6813696,0,0,0,7376640,5349632,5270272,8063232,6814720,0,0,0,7377664,5350656,5271296,8064256,7087104,0,0,0,
1710912,1233068,1633696,1198156,983616,0,0,0,7378688,5487872,5272320,6125056,7088128,0,0,0,7651072,5488896,5409536,6126080,7089152,0,0,0,7652096,5489920,5410560,6127104,7090176,0,0,0,7653120,5490944,5411584,6128128,7091200,0,0,0,
1711040,1233196,1633824,1198284,983744,0,0,0,7654144,5491968,5412608,6129152,7363584,0,0,0,7655168,5492992,5413632,6401536,7364608,0,0,0,7927552,5631232,5414656,6402560,7365632,0,0,0,7928576,5632256,5415680,6403584,7366656,0,0,0,
1711168,1233324,1633952,1198412,983872,0,0,0,7929600,5633280,5552896,6404608,7367680,0,0,0,7930624,5634304,5553920,6405632,7640064,0,0,0,7931648,5635328,5554944,6678016,7641088,0,0,0,8204032,5636352,5555968,6679040,7642112,0,0,0,
1711296,1233452,1634080,1198540,984000,0,0,0,8205056,5774592,5556992,6680064,7643136,0,0,0,8206080,5775616,5558016,6681088,7644160,0,0,0,8207104,5776640,5696256,6682112,7916544,0,0,0,8208128,5777664,5697280,6954496,7917568,0,0,0
};

__global__ __launch_bounds__(256, 1) void sequential_preLN__sliding_local_dilated__dense_activation_gelu__prompt_kernel(const void* x, const void* wqkv, const void* wo, const void* w1,
    const void* b1, const void* w2, const void* b2, void* out, void* ws) {
  __shared__ __attribute__((aligned(16))) char lds[163840];
  unsigned tid = threadIdx.x, bid = blockIdx.x;
  const void* tb = (const void*)retile_units;
  const void* ka = (const void*)__builtin_amdgcn_kernarg_segment_ptr();
  asm volatile("" ::"v"((unsigned)(size_t)(__attribute__((address_space(3))) char*)lds));
  asm volatile(
    ".p2align 3\n"
    "s_load_dwordx4 s[8:11], s[0:1], 0\n"
    "s_load_dwordx2 s[4:5], s[0:1], 64\n"
    "s_getpc_b64 s[98:99]\n"
    "Lpc_%=:\n"
    "v_lshrrev_b32 v4, 6, v0\n"
    "v_and_b32 v0, 63, v0\n"
    "s_lshr_b32 s47, s2, 3\n"
    "v_lshl_add_u32 v5, s47, 2, v4\n"
    "v_lshl_add_u32 v5, v0, 7, v5\n"
    "v_lshlrev_b32_e64 v5, 7, v5\n"
    "v_min_u32 v5, Lend_%=-Lpc_%=-4, v5\n"
    "global_load_dword v3, v5, s[98:99]\n"
    "v_readfirstlane_b32 s36, v4\n"
    "s_and_b32 s46, s2, 7\n"
    "s_lshr_b32 s47, s2, 3\n"
    "s_lshr_b32 s48, s46, 1\n"
    "s_lshr_b32 s49, s47, 3\n"
    "s_lshl2_add_u32 s37, s48, s49\n"
    "s_and_b32 s48, s46, 1\n"
    "s_and_b32 s49, s47, 7\n"
    "s_lshl3_add_u32 s38, s48, s49\n"
    "v_and_b32 v8, 15, v0\n"
    "v_mul_u32_u24 v9, 52, v8\n"
    "v_lshrrev_b32 v9, 8, v9\n"
    "v_mul_u32_u24 v9, 1200, v9\n"
    "v_lshl_add_u32 v9, v8, 4, v9\n"
    "v_lshrrev_b32_e64 v10, 4, v0\n"
    "v_mul_u32_u24 v11, 3840, v10\n"
    "v_add_u32 v9, v9, v11\n"
    "v_mov_b32 v11, 1073741824\n"
    "v_cmp_eq_u32 vcc, 15, v8\n"
    "s_mul_i32 s49, s37, 5\n"
    "s_add_u32 s65, s49, s36\n"
    "s_mul_i32 s49, s65, 30720\n"
    "s_mul_i32 s50, s38, 80\n"
    "s_add_u32 s49, s49, s50\n"
    "s_lshl_b32 s51, s36, 11\n"
    "s_add_u32 s51, s51, 92160\n"
    "s_add_u32 s50, s49, 15360\n"
    "s_mov_b32 m0, s51\n"
    "v_cndmask_b32 v9, v9, v11, vcc\n"
    "s_mov_b32 s14, 2457600\n"
    "s_mov_b32 s15, 0x20000\n"
    "s_waitcnt lgkmcnt(0)\n"
    "s_mov_b64 s[12:13], s[10:11]\n"
    "buffer_load_dwordx4 v9, s[12:15], s49 offen lds\n"
    "s_add_u32 m0, s51, 1024\n"
    "s_mov_b32 s10, 2621440\n"
    "buffer_load_dwordx4 v9, s[12:15], s50 offen lds\n"
    "s_cmp_lg_u32 s36, 0\n"
    "s_cbranch_scc1 Lw01_%=\n"
    "s_mov_b32 m0, 100352\n"
    "s_add_u32 s49, s49, 122880\n"
    "buffer_load_dwordx4 v9, s[12:15], s49 offen lds\n"
    "s_mov_b32 m0, 101376\n"
    "s_add_u32 s49, s49, 15360\n"
    "buffer_load_dwordx4 v9, s[12:15], s49 offen lds\n"
    "Lw01_%=:\n"
    "s_lshl_b32 s48, s37, 7\n"
    "s_and_b32 s52, s38, 7\n"
    "s_lshl4_add_u32 s53, s52, s48\n"
    "s_lshr_b32 s52, s38, 3\n"
    "s_add_u32 s53, s53, s52\n"
    "s_lshl2_add_u32 s53, s36, s53\n"
    "s_mul_i32 s92, s53, 1280\n"
    "s_mov_b32 s11, 0x20000\n"
    "v_lshrrev_b32 v5, 5, v0\n"
    "v_and_b32 v6, 31, v0\n"
    "v_lshlrev_b32 v12, 3, v6\n"
    "v_mul_u32_u24 v7, 2560, v5\n"
    "v_add_u32_e64 v65, v7, v12\n"
    "buffer_load_dwordx2 v[196:197], v65, s[8:11], s92 offen offset:0\n"
    "buffer_load_dwordx2 v[198:199], v65, s[8:11], s92 offen offset:256\n"
    "buffer_load_dwordx2 v[200:201], v65, s[8:11], s92 offen offset:512\n"
    "buffer_load_dwordx2 v[202:203], v65, s[8:11], s92 offen offset:768\n"
    "buffer_load_dwordx2 v[204:205], v65, s[8:11], s92 offen offset:1024\n"
    "s_and_b32 s49, s36, 1\n"
    "s_lshl_b32 s49, s49, 2\n"
    "v_lshrrev_b32 v11, 3, v0\n"
    "v_and_b32 v12, 7, v0\n"
    "v_xor_b32 v12, v12, v10\n"
    "v_xor_b32 v12, s49, v12\n"
    "v_mul_u32_u24 v11, 2560, v11\n"
    "v_lshl_add_u32 v11, v12, 4, v11\n"
    "s_lshl4_add_u32 s49, s36, s48\n"
    "s_mul_i32 s49, s49, 1280\n"
    "v_add_u32 v96, s49, v11\n"
    "v_add_u32 v97, 81920, v96\n"
    "v_add_u32 v98, 1280, v96\n"
    "v_add_u32 v99, 83200, v96\n"
    "s_sub_u32 s49, s48, 16\n"
    "s_max_i32 s49, s49, 0\n"
    "s_add_u32 s49, s49, s36\n"
    "s_mul_i32 s49, s49, 1280\n"
    "s_cmp_lt_u32 s36, 2\n"
    "s_cselect_b32 s49, s49, 1073741824\n"
    "v_add_u32 v100, s49, v11\n"
    "s_lshl_b32 s3, s36, 10\n"
    "s_and_b32 s69, s38, 7\n"
    "s_mul_i32 s69, s69, 160\n"
    "s_andn2_b32 s69, s69, 127\n"
    "s_lshl_b32 s93, s69, 7\n"
    "s_load_dwordx2 s[16:17], s[0:1], 16\n"
    "s_load_dwordx2 s[20:21], s[0:1], 24\n"
    "s_load_dwordx2 s[70:71], s[0:1], 32\n"
    "s_load_dwordx2 s[24:25], s[0:1], 40\n"
    "s_load_dwordx2 s[34:35], s[0:1], 48\n"
    "s_load_dwordx2 s[28:29], s[0:1], 56\n"
    "v_lshlrev_b32 v1, 4, v0\n"
    "v_lshlrev_b32_e64 v2, 3, v0\n"
    "s_mov_b32 s6, 21184512\n"
    "s_mov_b32 s7, 0x20000\n"
    "v_mov_b32_e64 v12, 0\n"
    "global_load_dwordx2 v[14:15], v12, s[4:5] sc1\n"
    "s_lshl2_add_u32 s95, s47, s36\n"
    "s_lshl_b32 s95, s95, 7\n"
    "v_lshl_add_u32 v5, s36, 1, v5\n"
    "s_and_b32 s52, s38, 1\n"
    "v_lshl_add_u32 v5, s52, 3, v5\n"
    "v_lshlrev_b32 v12, 3, v5\n"
    "v_lshl_add_u32 v12, v6, 2, v12\n"
    "v_lshlrev_b32_e64 v13, 6, v6\n"
    "v_lshl_add_u32 v13, v5, 2, v13\n"
    "v_cmp_gt_u32 vcc, 2, v6\n"
    "s_lshl_b32 s52, s37, 11\n"
    "s_lshr_b32 s54, s38, 1\n"
    "s_lshl_b32 s54, s54, 8\n"
    "s_add_u32 s52, s52, s54\n"
    "v_cndmask_b32 v64, v13, v12, vcc\n"
    "v_cmp_gt_u32 vcc, 4, v6\n"
    "v_mov_b32 v13, 1073741824\n"
    "v_and_b32_e64 v12, 1, v0\n"
    "s_add_u32 s94, s52, 196608\n"
    "v_cndmask_b32 v64, v13, v64, vcc\n"
    "v_cmp_eq_u32 s[90:91], 1, v12\n"
    "v_and_b32 v5, 3, v0\n"
    "v_bfe_u32 v11, v0, 2, 2\n"
    "v_mul_u32_u24 v12, 80, v10\n"
    "v_lshl_add_u32 v12, v5, 3, v12\n"
    "v_mul_u32_u24 v13, 80, v5\n"
    "v_add_u32 v13, 32, v13\n"
    "v_cmp_eq_u32 vcc, 3, v10\n"
    "v_lshl_add_u32 v11, v11, 8, s51\n"
    "s_mov_b32 s39, s37\n"
    "s_lshr_b32 s40, s38, 3\n"
    "s_and_b32 s41, s38, 7\n"
    "v_cndmask_b32 v12, v12, v13, vcc\n"
    "v_add_u32_e64 v7, 40, v12\n"
    "v_mov_b32 v13, 240\n"
    "v_and_b32 v68, 51, v0\n"
    "v_cmp_eq_u32 vcc, 51, v68\n"
    "v_lshlrev_b32 v69, 10, v10\n"
    "s_nop 0\n"
    "v_cndmask_b32 v12, v12, v13, vcc\n"
    "v_cndmask_b32 v7, v7, v13, vcc\n"
    "v_add_u32 v68, v11, v12\n"
    "v_add_u32 v70, v11, v7\n"
    "v_lshl_add_u32 v69, v8, 4, v69\n"
    "s_mul_i32 s50, s38, 163840\n"
    "s_add_u32 s50, s50, 262144\n"
    "s_lshr_b32 s49, s65, 2\n"
    "s_lshl_b32 s49, s49, 13\n"
    "s_add_u32 s44, s50, s49\n"
    "s_and_b32 s49, s65, 3\n"
    "s_lshl_b32 s49, s49, 8\n"
    "s_add_u32 s44, s44, s49\n"
    "s_add_u32 s65, s65, 4\n"
    "s_lshr_b32 s49, s65, 2\n"
    "s_lshl_b32 s49, s49, 13\n"
    "s_add_u32 s46, s50, s49\n"
    "s_and_b32 s49, s65, 3\n"
    "s_lshl_b32 s49, s49, 8\n"
    "s_add_u32 s46, s46, s49\n"
    "s_add_u32 s65, s65, 4\n"
    "s_add_u32 s45, s44, 4096\n"
    "s_add_u32 s47, s46, 4096\n"
    "s_mov_b32 s18, 819200\n"
    "s_mov_b32 s19, 0x20000\n"
    "s_mov_b32 s22, 2211840\n"
    "s_mov_b32 s23, 0x20000\n"
    "s_mov_b32 s26, 2211840\n"
    "s_mov_b32 s27, 0x20000\n"
    "s_mov_b32 s30, 2621440\n"
    "s_mov_b32 s31, 0x20000\n"
    "s_waitcnt vmcnt(6)\n"
    "s_cmp_lg_u32 s36, 0\n"
    "s_cbranch_scc1 Lw02_%=\n"
    "ds_read_b64_tr_b16 v[40:41], v68 offset:8192\n"
    "ds_read_b64_tr_b16 v[42:43], v68 offset:9216\n"
    "ds_read_b64_tr_b16 v[44:45], v70 offset:8192\n"
    "ds_read_b64_tr_b16 v[46:47], v70 offset:9216\n"
    "s_waitcnt lgkmcnt(0)\n"
    "buffer_store_dwordx4 v[40:43], v69, s[4:7], s46 offen sc1\n"
    "s_waitcnt lgkmcnt(0)\n"
    "buffer_store_dwordx4 v[44:47], v69, s[4:7], s47 offen sc1\n"
    "Lw02_%=:\n"
    "ds_read_b64_tr_b16 v[32:33], v68 offset:0\n"
    "ds_read_b64_tr_b16 v[34:35], v68 offset:1024\n"
    "ds_read_b64_tr_b16 v[36:37], v70 offset:0\n"
    "ds_read_b64_tr_b16 v[38:39], v70 offset:1024\n"
    "s_waitcnt lgkmcnt(0)\n"
    "buffer_store_dwordx4 v[32:35], v69, s[4:7], s44 offen sc1\n"
    "buffer_store_dwordx4 v[36:39], v69, s[4:7], s45 offen sc1\n"
    "s_lshl2_add_u32 s52, s2, s36\n"
    "s_mul_i32 s52, s52, 160\n"
    "s_add_u32 s66, s66, s52\n"
    "s_addc_u32 s67, s67, 0\n"
    "s_load_dwordx8 s[56:63], s[66:67], 0\n"
    "v_lshlrev_b32 v12, 5, v10\n"
    "global_load_dwordx4 v[130:133], v12, s[66:67] offset:32\n"
    "global_load_dword v134, v12, s[66:67] offset:48\n"
    "s_waitcnt vmcnt(9)\n"
    "v_lshlrev_b32 v72, 16, v196\n"
    "s_add_u32 m0, s3, 0\n"
    "s_nop 0\n"
    "buffer_load_dwordx4 v96, s[8:11], s69 offen lds\n"
    "v_and_b32 v73, 0xffff0000, v196\n"
    "v_lshlrev_b32_e64 v74, 16, v197\n"
    "v_and_b32 v75, 0xffff0000, v197\n"
    "s_waitcnt vmcnt(9)\n"
    "v_lshlrev_b32 v76, 16, v198\n"
    "v_and_b32 v77, 0xffff0000, v198\n"
    "v_lshlrev_b32_e64 v78, 16, v199\n"
    "v_and_b32 v79, 0xffff0000, v199\n"
    "s_waitcnt vmcnt(8)\n"
    "v_lshlrev_b32 v80, 16, v200\n"
    "v_and_b32 v81, 0xffff0000, v200\n"
    "v_lshlrev_b32_e64 v82, 16, v201\n"
    "v_and_b32 v83, 0xffff0000, v201\n"
    "s_waitcnt vmcnt(7)\n"
    "v_lshlrev_b32 v84, 16, v202\n"
    "s_add_u32 m0, s3, 4096\n"
    "s_nop 0\n"
    "buffer_load_dwordx4 v97, s[8:11], s69 offen lds\n"
    "v_and_b32 v85, 0xffff0000, v202\n"
    "v_lshlrev_b32 v86, 16, v203\n"
    "v_and_b32 v87, 0xffff0000, v203\n"
    "s_waitcnt vmcnt(7)\n"
    "v_lshlrev_b32 v88, 16, v204\n"
    "v_and_b32 v89, 0xffff0000, v204\n"
    "v_lshlrev_b32_e64 v90, 16, v205\n"
    "v_and_b32 v91, 0xffff0000, v205\n"
    "v_pk_add_f32 v[52:53], v[72:73], v[74:75]\n"
    "v_pk_add_f32 v[54:55], v[76:77], v[78:79]\n"
    "v_pk_add_f32 v[56:57], v[80:81], v[82:83]\n"
    "v_pk_add_f32 v[58:59], v[84:85], v[86:87]\n"
    "v_pk_add_f32 v[26:27], v[88:89], v[90:91]\n"
    "s_add_u32 m0, s3, 8192\n"
    "s_nop 0\n"
    "buffer_load_dwordx4 v98, s[8:11], s69 offen lds\n"
    "v_pk_add_f32 v[52:53], v[52:53], v[54:55]\n"
    "v_pk_add_f32 v[56:57], v[56:57], v[58:59]\n"
    "v_pk_add_f32 v[52:53], v[52:53], v[26:27]\n"
    "v_pk_add_f32 v[52:53], v[52:53], v[56:57]\n"
    "v_add_f32 v52, v52, v53\n"
    "s_nop 1\n"
    "v_add_f32_dpp v52, v52, v52 row_ror:8 row_mask:0xf bank_mask:0xf\n"
    "s_nop 1\n"
    "v_add_f32_dpp v52, v52, v52 row_ror:4 row_mask:0xf bank_mask:0xf\n"
    "s_nop 1\n"
    "v_add_f32_dpp v52, v52, v52 row_ror:2 row_mask:0xf bank_mask:0xf\n"
    "s_nop 1\n"
    "v_add_f32_dpp v52, v52, v52 row_ror:1 row_mask:0xf bank_mask:0xf\n"
    "v_mov_b32 v53, v52\n"
    "s_nop 1\n"
    "v_permlane16_swap_b32_e64 v52, v53\n"
    "s_add_u32 m0, s3, 12288\n"
    "s_nop 0\n"
    "buffer_load_dwordx4 v99, s[8:11], s69 offen lds\n"
    "v_add_f32 v52, v52, v53\n"
    "v_mul_f32 v52, 0xbacccccd, v52\n"
    "v_pk_add_f32 v[72:73], v[72:73], v[52:53] op_sel_hi:[1,0]\n"
    "v_pk_add_f32 v[74:75], v[74:75], v[52:53] op_sel_hi:[1,0]\n"
    "v_pk_add_f32 v[76:77], v[76:77], v[52:53] op_sel_hi:[1,0]\n"
    "v_pk_add_f32 v[78:79], v[78:79], v[52:53] op_sel_hi:[1,0]\n"
    "v_pk_mul_f32 v[54:55], v[72:73], v[72:73]\n"
    "v_pk_mul_f32 v[56:57], v[74:75], v[74:75]\n"
    "v_pk_add_f32 v[80:81], v[80:81], v[52:53] op_sel_hi:[1,0]\n"
    "v_pk_add_f32 v[82:83], v[82:83], v[52:53] op_sel_hi:[1,0]\n"
    "v_pk_fma_f32 v[54:55], v[76:77], v[76:77], v[54:55]\n"
    "v_pk_fma_f32 v[56:57], v[78:79], v[78:79], v[56:57]\n"
    "s_add_u32 m0, s3, 16384\n"
    "s_cmp_lt_u32 s36, 2\n"
    "s_cselect_b32 m0, m0, 162816\n"
    "s_nop 0\n"
    "buffer_load_dwordx4 v100, s[8:11], s69 offen lds\n"
    "s_add_u32 s69, s69, 128\n"
    "v_pk_add_f32 v[84:85], v[84:85], v[52:53] op_sel_hi:[1,0]\n"
    "v_pk_add_f32 v[86:87], v[86:87], v[52:53] op_sel_hi:[1,0]\n"
    "v_pk_fma_f32 v[54:55], v[80:81], v[80:81], v[54:55]\n"
    "v_pk_fma_f32 v[56:57], v[82:83], v[82:83], v[56:57]\n"
    "v_pk_add_f32 v[88:89], v[88:89], v[52:53] op_sel_hi:[1,0]\n"
    "v_pk_add_f32 v[90:91], v[90:91], v[52:53] op_sel_hi:[1,0]\n"
    "v_pk_fma_f32 v[54:55], v[84:85], v[84:85], v[54:55]\n"
    "v_pk_fma_f32 v[56:57], v[86:87], v[86:87], v[56:57]\n"
    "v_pk_fma_f32 v[54:55], v[88:89], v[88:89], v[54:55]\n"
    "v_pk_fma_f32 v[56:57], v[90:91], v[90:91], v[56:57]\n"
    "v_pk_add_f32 v[54:55], v[54:55], v[56:57]\n"
    "v_add_f32_e64 v54, v54, v55\n"
    "s_cmp_lg_u32 s69, 1280\n"
    "s_cselect_b32 s69, s69, 0\n"
    "s_add_u32 m0, s3, 18432\n"
    "s_nop 0\n"
    "buffer_load_dwordx4 v96, s[8:11], s69 offen lds\n"
    "v_add_f32_dpp v54, v54, v54 row_ror:8 row_mask:0xf bank_mask:0xf\n"
    "s_nop 1\n"
    "v_add_f32_dpp v54, v54, v54 row_ror:4 row_mask:0xf bank_mask:0xf\n"
    "s_nop 1\n"
    "v_add_f32_dpp v54, v54, v54 row_ror:2 row_mask:0xf bank_mask:0xf\n"
    "s_nop 1\n"
    "v_add_f32_dpp v54, v54, v54 row_ror:1 row_mask:0xf bank_mask:0xf\n"
    "v_mov_b32 v55, v54\n"
    "s_nop 1\n"
    "v_permlane16_swap_b32 v54, v55\n"
    "v_add_f32_e64 v54, v54, v55\n"
    "v_mov_b32 v55, 0x3727c5ac\n"
    "v_fmac_f32 v55, 0x3acccccd, v54\n"
    "v_rsq_f32_e64 v54, v55\n"
    "s_add_u32 m0, s3, 22528\n"
    "s_nop 0\n"
    "buffer_load_dwordx4 v97, s[8:11], s69 offen lds\n"
    "v_mul_f32 v52, v52, v54\n"
    "v_cndmask_b32 v54, v54, v52, s[90:91]\n"
    "buffer_store_dword v54, v64, s[4:7], s94 offen sc1\n"
    "s_waitcnt vmcnt(12)\n"
    "v_readfirstlane_b32 s42, v14\n"
    "v_readfirstlane_b32 s43, v15\n"
    "s_add_u32 s42, s42, 1\n"
    "s_addc_u32 s43, s43, 0\n"
    "v_mov_b32 v28, s42\n"
    "v_mov_b32 v29, s43\n"
    "s_lshl4_add_u32 s50, s37, s38\n"
    "s_lshl_b32 s50, s50, 10\n"
    "s_add_u32 s50, s50, 20922368\n"
    "s_add_u32 m0, s3, 26624\n"
    "s_nop 0\n"
    "buffer_load_dwordx4 v98, s[8:11], s69 offen lds\n"
    "v_mov_b32 v30, s50\n"
    "s_waitcnt vmcnt(11)\n"
    "s_barrier\n"
    "s_cmp_lg_u32 s36, 0\n"
    "s_cbranch_scc1 Lf13_%=\n"
    "s_mov_b64 exec, 1\n"
    "global_store_dwordx2 v30, v[28:29], s[4:5] sc1\n"
    "s_mov_b64 exec, -1\n"
    "Lf13_%=:\n"
    "s_lshl_b32 s68, s36, 12\n"
    "s_add_u32 m0, s3, 30720\n"
    "s_nop 0\n"
    "buffer_load_dwordx4 v99, s[8:11], s69 offen lds\n"
    "s_add_u32 s68, s68, 124928\n"
    "v_bfe_u32 v11, v0, 2, 2\n"
    "v_and_b32 v5, 3, v0\n"
    "v_lshlrev_b32_e64 v138, 7, v11\n"
    "v_lshl_add_u32 v138, v10, 5, v138\n"
    "v_lshl_add_u32 v138, v5, 3, v138\n"
    "v_add_u32 v138, s68, v138\n"
    "v_lshrrev_b32 v11, 3, v0\n"
    "s_add_u32 m0, s3, 34816\n"
    "s_cmp_lt_u32 s36, 2\n"
    "s_cselect_b32 m0, m0, 162816\n"
    "s_nop 0\n"
    "buffer_load_dwordx4 v100, s[8:11], s69 offen lds\n"
    "s_add_u32 s69, s69, 128\n"
    "v_and_b32 v5, 7, v0\n"
    "v_lshlrev_b32 v5, 4, v5\n"
    "v_lshrrev_b32_e64 v12, 5, v0\n"
    "v_mul_u32_u24 v140, 1280, v11\n"
    "v_add_u32_e64 v140, v140, v5\n"
    "v_mul_u32_u24 v141, 1280, v12\n"
    "v_mul_u32_u24 v142, 3456, v11\n"
    "v_add_u32_e64 v142, v142, v5\n"
    "s_cmp_lg_u32 s69, 1280\n"
    "s_cselect_b32 s69, s69, 0\n"
    "s_add_u32 m0, s3, 36864\n"
    "s_nop 0\n"
    "buffer_load_dwordx4 v96, s[8:11], s69 offen lds\n"
    "v_mul_u32_u24 v143, 3456, v12\n"
    "s_waitcnt lgkmcnt(0)\n"
    "s_and_b32 s52, s60, 31\n"
    "s_andn2_b32 s64, s60, 127\n"
    "v_mad_u32_u24 v128, v141, s52, v140\n"
    "s_and_b32 s52, s59, 31\n"
    "s_andn2_b32 s63, s59, 127\n"
    "v_mad_u32_u24 v127, v141, s52, v140\n"
    "s_and_b32 s52, s58, 31\n"
    "s_add_u32 m0, s3, 40960\n"
    "s_nop 0\n"
    "buffer_load_dwordx4 v97, s[8:11], s69 offen lds\n"
    "s_andn2_b32 s62, s58, 127\n"
    "v_mad_u32_u24 v126, v143, s52, v142\n"
    "s_and_b32 s52, s57, 31\n"
    "s_andn2_b32 s61, s57, 127\n"
    "v_mad_u32_u24 v125, v143, s52, v142\n"
    "s_and_b32 s52, s56, 31\n"
    "s_andn2_b32 s60, s56, 127\n"
    "v_mad_u32_u24 v124, v141, s52, v140\n"
    "s_add_u32 m0, s3, 45056\n"
    "s_nop 0\n"
    "buffer_load_dwordx4 v98, s[8:11], s69 offen lds\n"
    "v_mad_u32_u24 v11, v143, s52, v142\n"
    "s_bitcmp1_b32 s56, 5\n"
    "s_cselect_b64 vcc, -1, 0\n"
    "s_cselect_b64 s[52:53], s[20:21], s[16:17]\n"
    "s_cselect_b64 s[54:55], s[22:23], s[18:19]\n"
    "s_bitcmp1_b32 s56, 6\n"
    "s_cselect_b64 s[52:53], s[24:25], s[52:53]\n"
    "s_cselect_b64 s[54:55], s[26:27], s[54:55]\n"
    "s_add_u32 m0, s3, 49152\n"
    "s_nop 0\n"
    "buffer_load_dwordx4 v99, s[8:11], s69 offen lds\n"
    "v_cndmask_b32 v124, v124, v11, vcc\n"
    "s_waitcnt vmcnt(15)\n"
    "v_lshl_add_u32 v135, v8, 4, v134\n"
    "v_lshl_add_u32 v134, v8, 4, v133\n"
    "v_lshl_add_u32 v133, v8, 4, v132\n"
    "v_lshl_add_u32 v132, v8, 4, v131\n"
    "v_lshl_add_u32 v131, v8, 4, v130\n"
    "s_add_u32 m0, s68, 0\n"
    "s_nop 0\n"
    "buffer_load_dwordx4 v124, s[52:55], s60 offen lds\n"
    "s_add_u32 m0, s68, 1024\n"
    "s_nop 0\n"
    "buffer_load_dwordx4 v125, s[20:23], s61 offen lds\n"
    "s_add_u32 m0, s68, 2048\n"
    "s_nop 0\n"
    "buffer_load_dwordx4 v126, s[20:23], s62 offen lds\n"
    "s_add_u32 m0, s68, 3072\n"
    "s_nop 0\n"
    "buffer_load_dwordx4 v127, s[24:27], s63 offen lds\n"
    "s_add_u32 m0, s68, 16384\n"
    "s_nop 0\n"
    "buffer_load_dwordx4 v128, s[24:27], s64 offen lds\n"
    "s_mov_b32 s59, s93\n"
    "s_add_u32 m0, s3, 53248\n"
    "s_cmp_lt_u32 s36, 2\n"
    "s_cselect_b32 m0, m0, 162816\n"
    "s_nop 0\n"
    "buffer_load_dwordx4 v100, s[8:11], s69 offen lds\n"
    "s_add_u32 s69, s69, 128\n"
    "s_lshl_b32 s50, s36, 12\n"
    "s_lshl_b32 s51, s36, 11\n"
    "s_mul_i32 s48, s38, 163840\n"
    "s_add_u32 s48, s48, 262144\n"
    "s_cmp_lg_u32 s69, 1280\n"
    "s_cselect_b32 s69, s69, 0\n"
    "s_add_u32 m0, s3, 55296\n"
    "s_nop 0\n"
    "buffer_load_dwordx4 v96, s[8:11], s69 offen lds\n"
    "s_add_u32 s44, s48, s50\n"
    "v_add_u32 v101, s44, v1\n"
    "s_add_u32 s44, s48, s51\n"
    "v_add_u32_e64 v102, s44, v1\n"
    "s_add_u32 m0, s3, 59392\n"
    "s_nop 0\n"
    "buffer_load_dwordx4 v97, s[8:11], s69 offen lds\n"
    "s_add_u32 s13, s59, 8192\n"
    "s_add_u32 s14, s50, 92160\n"
    "s_add_u32 s15, s51, 92160\n"
    "s_add_u32 s12, s59, 16384\n"
    "s_add_u32 m0, s3, 63488\n"
    "s_nop 0\n"
    "buffer_load_dwordx4 v98, s[8:11], s69 offen lds\n"
    "s_cmp_lg_u32 s12, 163840\n"
    "s_cselect_b32 s12, s12, 0\n"
    "v_lshl_add_u32 v70, v8, 4, s38\n"
    "v_lshlrev_b32 v70, 10, v70\n"
    "s_add_u32 m0, s3, 67584\n"
    "s_nop 0\n"
    "buffer_load_dwordx4 v99, s[8:11], s69 offen lds\n"
    "v_add_u32 v70, 20922368, v70\n"
    "s_lshr_b32 s44, s36, 1\n"
    "s_and_b32 s45, s36, 1\n"
    "v_bfe_u32 v5, v0, 1, 3\n"
    "s_add_u32 m0, s3, 71680\n"
    "s_cmp_lt_u32 s36, 2\n"
    "s_cselect_b32 m0, m0, 162816\n"
    "s_nop 0\n"
    "buffer_load_dwordx4 v100, s[8:11], s69 offen lds\n"
    "s_add_u32 s69, s69, 128\n"
    "v_xor_b32 v5, v5, v10\n"
    "v_lshlrev_b32_e64 v5, 4, v5\n"
    "v_lshl_add_u32 v5, v8, 7, v5\n"
    "s_lshl_b32 s48, s44, 13\n"
    "s_cmp_lg_u32 s69, 1280\n"
    "s_cselect_b32 s69, s69, 0\n"
    "s_add_u32 m0, s3, 73728\n"
    "s_nop 0\n"
    "buffer_load_dwordx4 v96, s[8:11], s69 offen lds\n"
    "v_add_u32 v103, s48, v5\n"
    "v_xor_b32 v104, 64, v103\n"
    "v_add_u32 v105, 55296, v103\n"
    "v_add_u32 v106, 55296, v104\n"
    "s_add_u32 m0, s3, 77824\n"
    "s_nop 0\n"
    "buffer_load_dwordx4 v97, s[8:11], s69 offen lds\n"
    "v_add_u32 v107, 16384, v5\n"
    "v_xor_b32 v108, 64, v107\n"
    "v_add_u32 v109, 55296, v107\n"
    "v_add_u32 v110, 55296, v108\n"
    "s_add_u32 m0, s3, 81920\n"
    "s_nop 0\n"
    "buffer_load_dwordx4 v98, s[8:11], s69 offen lds\n"
    "s_lshl_b32 s48, s45, 12\n"
    "s_add_u32 s48, s48, 92160\n"
    "v_add_u32 v111, s48, v1\n"
    "v_cmp_eq_u32 s[96:97], 0, v8\n"
    "s_add_u32 m0, s3, 86016\n"
    "s_nop 0\n"
    "buffer_load_dwordx4 v99, s[8:11], s69 offen lds\n"
    "s_lshl_b32 s48, s44, 11\n"
    "v_add_u32 v176, s48, v111\n"
    "v_subrev_u32 v177, s48, v111\n"
    "v_mov_b32 v228, 0x3f803f80\n"
    "s_add_u32 m0, s3, 90112\n"
    "s_cmp_lt_u32 s36, 2\n"
    "s_cselect_b32 m0, m0, 162816\n"
    "s_nop 0\n"
    "buffer_load_dwordx4 v100, s[8:11], s69 offen lds\n"
    "s_add_u32 s69, s69, 128\n"
    "s_cmp_lg_u32 s69, 1280\n"
    "s_cselect_b32 s69, s69, 0\n"
    "v_mov_b32 v236, -1\n"
    "v_mov_b32 v229, 0x3f803f80\n"
    "v_mov_b32_e64 v237, -1\n"
    "v_mov_b32 v230, 0x3f803f80\n"
    "v_mov_b32_e64 v238, -1\n"
    "v_mov_b32 v231, 0x3f803f80\n"
    "v_mov_b32 v239, -1\n"
    "v_mov_b32 v11, 1073741824\n"
    "v_cmp_gt_u32 vcc, 48, v0\n"
    "s_lshl4_add_u32 s48, s37, s38\n"
    "s_lshl2_add_u32 s48, s48, s36\n"
    "s_mul_i32 s58, s48, 6912\n"
    "s_add_u32 s58, s58, 13582336\n"
    "v_cndmask_b32 v235, v11, v1, vcc\n"
    "v_cmp_gt_u32 vcc, 32, v0\n"
    "s_mul_i32 s57, s48, 2560\n"
    "s_add_u32 s56, s57, 10829824\n"
    "s_add_u32 s57, s57, 8208384\n"
    "v_cndmask_b32 v234, v11, v1, vcc\n"
    "v_cmp_gt_u32 vcc, 8, v0\n"
    "s_lshl_b32 s65, s48, 7\n"
    "s_add_u32 s65, s65, 13451264\n"
    "v_cndmask_b32 v123, v11, v1, vcc\n"
    "s_lshl2_add_u32 s48, s40, s36\n"
    "s_lshl_b32 s48, s48, 11\n"
    "v_lshl_add_u32 v5, v8, 7, s48\n"
    "s_mul_i32 s48, s41, 160\n"
    "s_and_b32 s48, s48, 127\n"
    "v_lshl_add_u32 v11, v10, 3, s48\n"
    "v_bfe_u32 v12, v0, 1, 3\n"
    "v_lshlrev_b32 v12, 4, v12\n"
    "v_add_u32 v13, 0, v11\n"
    "v_lshrrev_b32 v240, 7, v13\n"
    "v_mul_u32_u24 v240, 18432, v240\n"
    "v_and_b32 v13, 127, v13\n"
    "v_xor_b32 v13, v13, v12\n"
    "v_add3_u32 v240, v240, v13, v5\n"
    "v_add_u32 v13, 32, v11\n"
    "v_lshrrev_b32_e64 v241, 7, v13\n"
    "v_mul_u32_u24 v241, 18432, v241\n"
    "v_and_b32 v13, 127, v13\n"
    "v_xor_b32 v13, v13, v12\n"
    "v_add3_u32 v241, v241, v13, v5\n"
    "v_add_u32 v13, 64, v11\n"
    "v_lshrrev_b32_e64 v242, 7, v13\n"
    "v_mul_u32_u24 v242, 18432, v242\n"
    "v_and_b32 v13, 127, v13\n"
    "v_xor_b32 v13, v13, v12\n"
    "v_add3_u32 v242, v242, v13, v5\n"
    "v_add_u32 v13, 96, v11\n"
    "v_lshrrev_b32 v243, 7, v13\n"
    "v_mul_u32_u24 v243, 18432, v243\n"
    "v_and_b32 v13, 127, v13\n"
    "v_xor_b32 v13, v13, v12\n"
    "v_add3_u32 v243, v243, v13, v5\n"
    "v_add_u32 v13, 128, v11\n"
    "v_lshrrev_b32 v244, 7, v13\n"
    "v_mul_u32_u24 v244, 18432, v244\n"
    "v_and_b32 v13, 127, v13\n"
    "v_xor_b32 v13, v13, v12\n"
    "v_add3_u32 v244, v244, v13, v5\n"
    "s_cmp_lg_u32 s36, 0\n"
    "s_cbranch_scc1 Ld14_%=\n"
    "s_mov_b32 s48, 65536\n"
    "Lp5_%=:\n"
    "global_load_dwordx2 v[20:21], v70, s[4:5] sc1\n"
    "s_waitcnt vmcnt(0)\n"
    "v_cmp_ne_u64 vcc, s[42:43], v[20:21]\n"
    "s_nop 0\n"
    "s_cbranch_vccz Ld14_%=\n"
    "s_sub_u32 s48, s48, 1\n"
    "s_cbranch_scc0 Lp5_%=\n"
    "Ld14_%=:\n"
    "s_barrier\n"
    "s_mov_b32 m0, s15\n"
    "s_nop 0\n"
    "buffer_load_dwordx4 v102, s[4:7], s59 offen offset:0 sc1 lds\n"
    "buffer_load_dwordx4 v102, s[4:7], s59 offen offset:1024 sc1 lds\n"
    "s_add_u32 m0, s15, 8192\n"
    "s_nop 0\n"
    "buffer_load_dwordx4 v102, s[4:7], s13 offen offset:0 sc1 lds\n"
    "buffer_load_dwordx4 v102, s[4:7], s13 offen offset:1024 sc1 lds\n"
    "s_add_u32 m0, s14, 16384\n"
    "s_nop 0\n"
    "buffer_load_dwordx4 v101, s[4:7], s12 offen offset:0 sc1 lds\n"
    "buffer_load_dwordx4 v101, s[4:7], s12 offen offset:1024 sc1 lds\n"
    "buffer_load_dwordx4 v101, s[4:7], s12 offen offset:2048 sc1 lds\n"
    "buffer_load_dwordx4 v101, s[4:7], s12 offen offset:3072 sc1 lds\n"
    "s_add_u32 s12, s12, 16384\n"
    "s_cmp_lg_u32 s12, 163840\n"
    "s_cselect_b32 s12, s12, 0\n"
    "s_waitcnt vmcnt(19)\n"
    "ds_read_b64_tr_b16 v[148:149], v138 offset:0\n"
    "ds_read_b64_tr_b16 v[150:151], v138 offset:512\n"
    "ds_read_b64_tr_b16 v[152:153], v138 offset:1024\n"
    "ds_read_b64_tr_b16 v[154:155], v138 offset:1536\n"
    "ds_read_b64_tr_b16 v[156:157], v138 offset:2048\n"
    "ds_read_b64_tr_b16 v[158:159], v138 offset:2560\n"
    "ds_read_b64_tr_b16 v[160:161], v138 offset:3072\n"
    "ds_read_b64_tr_b16 v[162:163], v138 offset:3584\n"
    "ds_read_b64_tr_b16 v[164:165], v138 offset:16384\n"
    "ds_read_b64_tr_b16 v[166:167], v138 offset:16896\n"
    "s_waitcnt lgkmcnt(0)\n"
    "s_add_u32 m0, s14, 32768\n"
    "s_nop 0\n"
    "buffer_load_dwordx4 v101, s[4:7], s12 offen offset:0 sc1 lds\n"
    "buffer_load_dwordx4 v101, s[4:7], s12 offen offset:1024 sc1 lds\n"
    "s_waitcnt vmcnt(8)\n"
    "s_barrier\n"
    "ds_read_b128 a[16:19], v111 offset:0\n"
    "ds_read_b128 a[0:3], v103 offset:0\n"
    "ds_read_b128 a[20:23], v111 offset:1024\n"
    "ds_read_b128 a[24:27], v111 offset:2048\n"
    "ds_read_b128 a[28:31], v111 offset:3072\n"
    "ds_read_b128 a[4:7], v103 offset:2048\n"
    "ds_read_b128 a[8:11], v103 offset:4096\n"
    "ds_read_b128 a[12:15], v103 offset:6144\n"
    "ds_read_b128 v[168:171], v107 offset:0\n"
    "ds_read_b128 a[64:67], v176 offset:1024\n"
    "ds_read_b128 a[68:71], v177 offset:2048\n"
    "ds_read_b64 a[246:247], v240\n"
    "ds_read_b64 a[248:249], v241\n"
    "ds_read_b64 a[250:251], v242\n"
    "ds_read_b64 a[252:253], v243\n"
    "ds_read_b64 a[254:255], v244\n"
    "s_waitcnt lgkmcnt(0)\n"
    "v_mfma_f32_16x16x32_bf16 v[20:23], a[16:19], a[0:3], 0\n"
    "s_waitcnt vmcnt(6)\n"
    "s_barrier\n"
    "v_mfma_f32_16x16x32_bf16 v[40:43], a[20:23], a[0:3], 0\n"
    "ds_read_b128 a[48:51], v111 offset:8192\n"
    "v_cndmask_b32 v168, v168, v228, s[96:97]\n"
    "v_mfma_f32_16x16x32_bf16 v[60:63], a[0:3], a[24:27], 0\n"
    "ds_read_b128 a[32:35], v104 offset:0\n"
    "v_cndmask_b32 v169, v169, v228, s[96:97]\n"
    "v_mfma_f32_16x16x32_bf16 v[80:83], a[28:31], a[0:3], 0\n"
    "ds_read_b128 a[52:55], v111 offset:9216\n"
    "buffer_load_dwordx4 v101, s[4:7], s12 offen offset:2048 sc1 lds\n"
    "v_mfma_f32_16x16x32_bf16 v[24:27], a[16:19], a[4:7], 0\n"
    "ds_read_b128 a[56:59], v111 offset:10240\n"
    "v_cndmask_b32 v170, v170, v228, s[96:97]\n"
    "v_mfma_f32_16x16x32_bf16 v[44:47], a[20:23], a[4:7], 0\n"
    "ds_read_b128 a[60:63], v111 offset:11264\n"
    "v_cndmask_b32 v171, v171, v228, s[96:97]\n"
    "v_mfma_f32_16x16x32_bf16 v[64:67], a[4:7], a[24:27], 0\n"
    "ds_read_b128 a[36:39], v104 offset:2048\n"
    "v_mfma_f32_16x16x32_bf16 v[84:87], a[28:31], a[4:7], 0\n"
    "ds_read_b128 a[40:43], v104 offset:4096\n"
    "buffer_load_dwordx4 v101, s[4:7], s12 offen offset:3072 sc1 lds\n"
    "v_mfma_f32_16x16x32_bf16 v[28:31], a[16:19], a[8:11], 0\n"
    "ds_read_b128 a[44:47], v104 offset:6144\n"
    "s_add_u32 s12, s12, 16384\n"
    "v_mfma_f32_16x16x32_bf16 v[48:51], a[20:23], a[8:11], 0\n"
    "ds_read_b128 v[172:175], v108 offset:0\n"
    "s_cmp_lg_u32 s12, 163840\n"
    "s_cselect_b32 s12, s12, 0\n"
    "v_mfma_f32_16x16x32_bf16 v[68:71], a[8:11], a[24:27], 0\n"
    "ds_read_b128 a[72:75], v176 offset:9216\n"
    "s_add_u32 m0, s14, 49152\n"
    "v_mfma_f32_16x16x32_bf16 v[88:91], a[28:31], a[8:11], 0\n"
    "ds_read_b128 a[76:79], v177 offset:10240\n"
    "buffer_load_dwordx4 v101, s[4:7], s12 offen offset:0 sc1 lds\n"
    "v_mfma_f32_16x16x32_bf16 v[32:35], a[16:19], a[12:15], 0\n"
    "v_mfma_f32_16x16x32_bf16 v[52:55], a[20:23], a[12:15], 0\n"
    "v_mfma_f32_16x16x32_bf16 v[72:75], a[12:15], a[24:27], 0\n"
    "v_mfma_f32_16x16x32_bf16 v[92:95], a[28:31], a[12:15], 0\n"
    "buffer_load_dwordx4 v101, s[4:7], s12 offen offset:1024 sc1 lds\n"
    "v_mfma_f32_16x16x32_bf16 v[36:39], a[64:67], v[168:171], 0\n"
    "v_mfma_f32_16x16x32_bf16 v[56:59], v[168:171], a[68:71], 0\n"
    "s_waitcnt lgkmcnt(0)\n"
    "v_mfma_f32_16x16x32_bf16 v[20:23], a[48:51], a[32:35], v[20:23]\n"
    "s_waitcnt vmcnt(6)\n"
    "s_barrier\n"
    "v_mfma_f32_16x16x32_bf16 v[40:43], a[52:55], a[32:35], v[40:43]\n"
    "ds_read_b128 a[16:19], v111 offset:16384\n"
    "buffer_load_dwordx4 v101, s[4:7], s12 offen offset:2048 sc1 lds\n"
    "v_mfma_f32_16x16x32_bf16 v[60:63], a[32:35], a[56:59], v[60:63]\n"
    "ds_read_b128 a[0:3], v103 offset:18432\n"
    "v_cndmask_b32 v172, v172, v228, s[96:97]\n"
    "v_mfma_f32_16x16x32_bf16 v[80:83], a[60:63], a[32:35], v[80:83]\n"
    "ds_read_b128 a[20:23], v111 offset:17408\n"
    "v_cndmask_b32 v173, v173, v228, s[96:97]\n"
    "v_mfma_f32_16x16x32_bf16 v[24:27], a[48:51], a[36:39], v[24:27]\n"
    "ds_read_b128 a[24:27], v111 offset:18432\n"
    "v_cndmask_b32 v174, v174, v228, s[96:97]\n"
    "v_mfma_f32_16x16x32_bf16 v[44:47], a[52:55], a[36:39], v[44:47]\n"
    "buffer_store_dwordx4 v[148:151], v131, s[4:7], 0 offen sc1\n"
    "v_mfma_f32_16x16x32_bf16 v[64:67], a[36:39], a[56:59], v[64:67]\n"
    "ds_read_b128 a[28:31], v111 offset:19456\n"
    "v_cndmask_b32 v175, v175, v228, s[96:97]\n"
    "v_mfma_f32_16x16x32_bf16 v[84:87], a[60:63], a[36:39], v[84:87]\n"
    "ds_read_b128 a[4:7], v103 offset:20480\n"
    "s_lshl_b32 s50, s36, 9\n"
    "v_mfma_f32_16x16x32_bf16 v[28:31], a[48:51], a[40:43], v[28:31]\n"
    "ds_read_b128 a[8:11], v103 offset:22528\n"
    "s_add_u32 s50, s50, 40960\n"
    "v_mfma_f32_16x16x32_bf16 v[48:51], a[52:55], a[40:43], v[48:51]\n"
    "ds_read_b128 a[12:15], v103 offset:24576\n"
    "buffer_load_dwordx4 v101, s[4:7], s12 offen offset:3072 sc1 lds\n"
    "v_mfma_f32_16x16x32_bf16 v[68:71], a[40:43], a[56:59], v[68:71]\n"
    "ds_read_b128 v[168:171], v107 offset:18432\n"
    "s_add_u32 s12, s12, 16384\n"
    "v_mfma_f32_16x16x32_bf16 v[88:91], a[60:63], a[40:43], v[88:91]\n"
    "ds_read_b128 a[64:67], v176 offset:17408\n"
    "s_cmp_lg_u32 s12, 163840\n"
    "s_cselect_b32 s12, s12, 0\n"
    "v_mfma_f32_16x16x32_bf16 v[32:35], a[48:51], a[44:47], v[32:35]\n"
    "ds_read_b128 a[68:71], v177 offset:18432\n"
    "s_add_u32 m0, s14, 0\n"
    "v_mfma_f32_16x16x32_bf16 v[52:55], a[52:55], a[44:47], v[52:55]\n"
    "buffer_load_dwordx4 v101, s[4:7], s12 offen offset:0 sc1 lds\n"
    "v_add_u32_e64 v112, s50, v2\n"
    "v_mfma_f32_16x16x32_bf16 v[72:75], a[44:47], a[56:59], v[72:75]\n"
    "buffer_store_dwordx4 v[152:155], v132, s[4:7], 0 offen sc1\n"
    "v_mfma_f32_16x16x32_bf16 v[92:95], a[60:63], a[44:47], v[92:95]\n"
    "s_mul_i32 s22, s41, 102400\n"
    "s_mul_i32 s23, s36, 5120\n"
    "v_mfma_f32_16x16x32_bf16 v[36:39], a[72:75], v[172:175], v[36:39]\n"
    "s_add_u32 s23, s23, s22\n"
    "s_add_u32 s23, s23, 2965504\n"
    "v_mfma_f32_16x16x32_bf16 v[56:59], v[172:175], a[76:79], v[56:59]\n"
    "s_mul_i32 s50, s36, 20480\n"
    "s_add_u32 s22, s22, s50\n"
    "s_waitcnt lgkmcnt(0)\n"
    "v_mfma_f32_16x16x32_bf16 v[20:23], a[16:19], a[0:3], v[20:23]\n"
    "ds_read_b128 a[48:51], v111 offset:24576\n"
    "buffer_load_dwordx4 v101, s[4:7], s12 offen offset:1024 sc1 lds\n"
    "v_mfma_f32_16x16x32_bf16 v[40:43], a[20:23], a[0:3], v[40:43]\n"
    "ds_read_b128 a[32:35], v104 offset:18432\n"
    "v_cndmask_b32 v168, v168, v228, s[96:97]\n"
    "v_mfma_f32_16x16x32_bf16 v[60:63], a[0:3], a[24:27], v[60:63]\n"
    "ds_read_b128 a[52:55], v111 offset:25600\n"
    "v_cndmask_b32 v169, v169, v228, s[96:97]\n"
    "v_mfma_f32_16x16x32_bf16 v[80:83], a[28:31], a[0:3], v[80:83]\n"
    "ds_read_b128 a[56:59], v111 offset:26624\n"
    "v_cndmask_b32 v170, v170, v228, s[96:97]\n"
    "v_mfma_f32_16x16x32_bf16 v[24:27], a[16:19], a[4:7], v[24:27]\n"
    "ds_read_b128 a[60:63], v111 offset:27648\n"
    "buffer_load_dwordx4 v101, s[4:7], s12 offen offset:2048 sc1 lds\n"
    "v_mfma_f32_16x16x32_bf16 v[44:47], a[20:23], a[4:7], v[44:47]\n"
    "buffer_store_dwordx4 v[156:159], v133, s[4:7], 0 offen sc1\n"
    "v_mfma_f32_16x16x32_bf16 v[64:67], a[4:7], a[24:27], v[64:67]\n"
    "ds_read_b128 a[36:39], v104 offset:20480\n"
    "v_cndmask_b32 v171, v171, v228, s[96:97]\n"
    "v_mfma_f32_16x16x32_bf16 v[84:87], a[28:31], a[4:7], v[84:87]\n"
    "ds_read_b128 a[40:43], v104 offset:22528\n"
    "s_add_u32 s22, s22, 2883584\n"
    "v_mfma_f32_16x16x32_bf16 v[28:31], a[16:19], a[8:11], v[28:31]\n"
    "ds_read_b128 a[44:47], v104 offset:24576\n"
    "s_mul_i32 s20, s36, 7168\n"
    "v_mfma_f32_16x16x32_bf16 v[48:51], a[20:23], a[8:11], v[48:51]\n"
    "ds_read_b128 v[172:175], v108 offset:18432\n"
    "buffer_load_dwordx4 v101, s[4:7], s12 offen offset:3072 sc1 lds\n"
    "v_mfma_f32_16x16x32_bf16 v[68:71], a[8:11], a[24:27], v[68:71]\n"
    "ds_read_b128 a[72:75], v176 offset:25600\n"
    "s_add_u32 s12, s12, 16384\n"
    "v_mfma_f32_16x16x32_bf16 v[88:91], a[28:31], a[8:11], v[88:91]\n"
    "ds_read_b128 a[76:79], v177 offset:26624\n"
    "s_cmp_lg_u32 s12, 163840\n"
    "s_cselect_b32 s12, s12, 0\n"
    "v_mfma_f32_16x16x32_bf16 v[32:35], a[16:19], a[12:15], v[32:35]\n"
    "s_add_u32 m0, s3, 0\n"
    "s_mul_i32 s19, s38, 143360\n"
    "v_mfma_f32_16x16x32_bf16 v[52:55], a[20:23], a[12:15], v[52:55]\n"
    "buffer_load_dwordx4 v96, s[8:11], s69 offen lds\n"
    "s_add_u32 m0, s3, 4096\n"
    "v_mfma_f32_16x16x32_bf16 v[72:75], a[12:15], a[24:27], v[72:75]\n"
    "buffer_store_dwordx4 v[160:163], v134, s[4:7], 0 offen sc1\n"
    "v_mfma_f32_16x16x32_bf16 v[92:95], a[28:31], a[12:15], v[92:95]\n"
    "s_add_u32 s19, s19, s20\n"
    "s_add_u32 s19, s19, 3702784\n"
    "v_mfma_f32_16x16x32_bf16 v[36:39], a[64:67], v[168:171], v[36:39]\n"
    "s_mul_i32 s16, s41, 276480\n"
    "s_add_u32 s16, s16, 5996544\n"
    "v_mfma_f32_16x16x32_bf16 v[56:59], v[168:171], a[68:71], v[56:59]\n"
    "s_mul_i32 s17, s36, 10240\n"
    "s_add_u32 s17, s17, s16\n"
    "s_waitcnt lgkmcnt(0)\n"
    "v_mfma_f32_16x16x32_bf16 v[20:23], a[48:51], a[32:35], v[20:23]\n"
    "s_waitcnt vmcnt(13)\n"
    "s_barrier\n"
    "v_mfma_f32_16x16x32_bf16 v[40:43], a[52:55], a[32:35], v[40:43]\n"
    "ds_read_b128 a[16:19], v111 offset:32768\n"
    "buffer_load_dwordx4 v97, s[8:11], s69 offen lds\n"
    "v_mfma_f32_16x16x32_bf16 v[60:63], a[32:35], a[56:59], v[60:63]\n"
    "ds_read_b128 a[0:3], v103 offset:36864\n"
    "s_add_u32 m0, s3, 8192\n"
    "v_mfma_f32_16x16x32_bf16 v[80:83], a[60:63], a[32:35], v[80:83]\n"
    "ds_read_b128 a[20:23], v111 offset:33792\n"
    "v_cndmask_b32 v172, v172, v228, s[96:97]\n"
    "v_mfma_f32_16x16x32_bf16 v[24:27], a[48:51], a[36:39], v[24:27]\n"
    "ds_read_b128 a[24:27], v111 offset:34816\n"
    "v_cndmask_b32 v173, v173, v228, s[96:97]\n"
    "v_mfma_f32_16x16x32_bf16 v[44:47], a[52:55], a[36:39], v[44:47]\n"
    "buffer_store_dwordx4 v[164:167], v135, s[4:7], 0 offen sc1\n"
    "v_mfma_f32_16x16x32_bf16 v[64:67], a[36:39], a[56:59], v[64:67]\n"
    "ds_read_b128 a[28:31], v111 offset:35840\n"
    "v_cndmask_b32 v174, v174, v228, s[96:97]\n"
    "v_mfma_f32_16x16x32_bf16 v[84:87], a[60:63], a[36:39], v[84:87]\n"
    "ds_read_b128 a[4:7], v103 offset:38912\n"
    "v_cndmask_b32 v175, v175, v228, s[96:97]\n"
    "v_mfma_f32_16x16x32_bf16 v[28:31], a[48:51], a[40:43], v[28:31]\n"
    "ds_read_b128 a[8:11], v103 offset:40960\n"
    "s_add_u32 s17, s17, 245760\n"
    "v_mfma_f32_16x16x32_bf16 v[48:51], a[52:55], a[40:43], v[48:51]\n"
    "ds_read_b128 a[12:15], v103 offset:43008\n"
    "buffer_load_dwordx4 v98, s[8:11], s69 offen lds\n"
    "v_mfma_f32_16x16x32_bf16 v[68:71], a[40:43], a[56:59], v[68:71]\n"
    "ds_read_b128 v[168:171], v107 offset:36864\n"
    "s_add_u32 m0, s3, 12288\n"
    "v_mfma_f32_16x16x32_bf16 v[88:91], a[60:63], a[40:43], v[88:91]\n"
    "ds_read_b128 a[64:67], v176 offset:33792\n"
    "ds_read_b128 a[68:71], v177 offset:34816\n"
    "v_mfma_f32_16x16x32_bf16 v[32:35], a[48:51], a[44:47], v[32:35]\n"
    "s_mul_i32 s50, s36, 61440\n"
    "s_add_u32 s16, s16, s50\n"
    "v_mfma_f32_16x16x32_bf16 v[52:55], a[52:55], a[44:47], v[52:55]\n"
    "buffer_load_dwordx4 v99, s[8:11], s69 offen lds\n"
    "s_add_u32 m0, s3, 16384\n"
    "s_cmp_lt_u32 s36, 2\n"
    "s_cselect_b32 m0, m0, 162816\n"
    "v_mfma_f32_16x16x32_bf16 v[72:75], a[44:47], a[56:59], v[72:75]\n"
    "s_cmp_lt_u32 s36, 3\n"
    "s_cselect_b32 s17, s17, 1073741824\n"
    "v_mfma_f32_16x16x32_bf16 v[92:95], a[60:63], a[44:47], v[92:95]\n"
    "s_cmp_lt_u32 s38, 12\n"
    "s_cselect_b32 s26, s19, 1073741824\n"
    "v_mfma_f32_16x16x32_bf16 v[36:39], a[72:75], v[172:175], v[36:39]\n"
    "s_lshl_b32 s50, s39, 7\n"
    "s_lshl_b32 s51, s36, 5\n"
    "v_mfma_f32_16x16x32_bf16 v[56:59], v[172:175], a[76:79], v[56:59]\n"
    "buffer_load_dwordx4 v100, s[8:11], s69 offen lds\n"
    "s_add_u32 s69, s69, 128\n"
    "s_waitcnt lgkmcnt(0)\n"
    "v_mfma_f32_16x16x32_bf16 v[20:23], a[16:19], a[0:3], v[20:23]\n"
    "ds_read_b128 a[48:51], v111 offset:40960\n"
    "s_cmp_lg_u32 s69, 1280\n"
    "s_cselect_b32 s69, s69, 0\n"
    "v_mfma_f32_16x16x32_bf16 v[40:43], a[20:23], a[0:3], v[40:43]\n"
    "ds_read_b128 a[32:35], v104 offset:36864\n"
    "s_add_u32 m0, s14, 16384\n"
    "v_mfma_f32_16x16x32_bf16 v[60:63], a[0:3], a[24:27], v[60:63]\n"
    "ds_read_b128 a[52:55], v111 offset:41984\n"
    "v_cndmask_b32 v168, v168, v228, s[96:97]\n"
    "v_mfma_f32_16x16x32_bf16 v[80:83], a[28:31], a[0:3], v[80:83]\n"
    "ds_read_b128 a[56:59], v111 offset:43008\n"
    "buffer_load_dwordx4 v101, s[4:7], s12 offen offset:0 sc1 lds\n"
    "v_mfma_f32_16x16x32_bf16 v[24:27], a[16:19], a[4:7], v[24:27]\n"
    "ds_read_b128 a[60:63], v111 offset:44032\n"
    "v_cndmask_b32 v169, v169, v228, s[96:97]\n"
    "v_mfma_f32_16x16x32_bf16 v[44:47], a[20:23], a[4:7], v[44:47]\n"
    "ds_read_b128 a[36:39], v104 offset:38912\n"
    "v_cndmask_b32 v170, v170, v228, s[96:97]\n"
    "v_mfma_f32_16x16x32_bf16 v[64:67], a[4:7], a[24:27], v[64:67]\n"
    "ds_read_b128 a[40:43], v104 offset:40960\n"
    "v_cndmask_b32 v171, v171, v228, s[96:97]\n"
    "v_mfma_f32_16x16x32_bf16 v[84:87], a[28:31], a[4:7], v[84:87]\n"
    "ds_read_b128 a[44:47], v104 offset:43008\n"
    "buffer_load_dwordx4 v101, s[4:7], s12 offen offset:1024 sc1 lds\n"
    "v_mfma_f32_16x16x32_bf16 v[28:31], a[16:19], a[8:11], v[28:31]\n"
    "ds_read_b128 v[172:175], v108 offset:36864\n"
    "s_add_u32 s50, s50, s51\n"
    "v_mfma_f32_16x16x32_bf16 v[48:51], a[20:23], a[8:11], v[48:51]\n"
    "ds_read_b128 a[72:75], v176 offset:41984\n"
    "ds_read_b128 a[76:79], v177 offset:43008\n"
    "v_mfma_f32_16x16x32_bf16 v[68:71], a[8:11], a[24:27], v[68:71]\n"
    "s_add_u32 s50, s50, s40\n"
    "s_mul_i32 s25, s50, 1280\n"
    "v_mfma_f32_16x16x32_bf16 v[88:91], a[28:31], a[8:11], v[88:91]\n"
    "buffer_load_dwordx4 v101, s[4:7], s12 offen offset:2048 sc1 lds\n"
    "s_mul_i32 s50, s41, 160\n"
    "v_mfma_f32_16x16x32_bf16 v[32:35], a[16:19], a[12:15], v[32:35]\n"
    "s_add_u32 s25, s25, s50\n"
    "s_lshl_b32 s50, s37, 7\n"
    "v_mfma_f32_16x16x32_bf16 v[52:55], a[20:23], a[12:15], v[52:55]\n"
    "s_add_u32 s50, s50, 43008\n"
    "v_lshl_add_u32 v122, v8, 3, s50\n"
    "v_mfma_f32_16x16x32_bf16 v[72:75], a[12:15], a[24:27], v[72:75]\n"
    "v_bfe_u32 v9, v0, 4, 1\n"
    "v_mul_u32_u24 v11, 20480, v9\n"
    "v_mfma_f32_16x16x32_bf16 v[92:95], a[28:31], a[12:15], v[92:95]\n"
    "buffer_load_dwordx4 v101, s[4:7], s12 offen offset:3072 sc1 lds\n"
    "s_add_u32 s12, s12, 16384\n"
    "v_mfma_f32_16x16x32_bf16 v[36:39], a[64:67], v[168:171], v[36:39]\n"
    "s_cmp_lg_u32 s12, 163840\n"
    "s_cselect_b32 s12, s12, 0\n"
    "v_mfma_f32_16x16x32_bf16 v[56:59], v[168:171], a[68:71], v[56:59]\n"
    "s_add_u32 m0, s3, 18432\n"
    "v_lshrrev_b32 v9, 5, v0\n"
    "s_waitcnt lgkmcnt(0)\n"
    "v_mfma_f32_16x16x32_bf16 v[20:23], a[48:51], a[32:35], v[20:23]\n"
    "s_waitcnt vmcnt(7)\n"
    "s_barrier\n"
    "s_cmp_lg_u32 s36, 0\n"
    "s_cbranch_scc1 Lol6_%=\n"
    "v_mov_b32 v4, s42\n"
    "v_mov_b32_e64 v5, s43\n"
    "s_lshl3_add_u32 s49, s2, 40960\n"
    "v_mov_b32 v6, s49\n"
    "s_mov_b64 exec, 1\n"
    "global_store_dwordx2 v6, v[4:5], s[4:5] sc1\n"
    "s_mov_b64 exec, -1\n"
    "Lol6_%=:\n"
    "v_mfma_f32_16x16x32_bf16 v[40:43], a[52:55], a[32:35], v[40:43]\n"
    "ds_read_b128 a[16:19], v111 offset:49152\n"
    "buffer_load_dwordx4 v96, s[8:11], s69 offen lds\n"
    "v_mfma_f32_16x16x32_bf16 v[60:63], a[32:35], a[56:59], v[60:63]\n"
    "ds_read_b128 a[0:3], v105 offset:0\n"
    "s_add_u32 m0, s3, 22528\n"
    "v_mfma_f32_16x16x32_bf16 v[80:83], a[60:63], a[32:35], v[80:83]\n"
    "ds_read_b128 a[20:23], v111 offset:50176\n"
    "v_cndmask_b32 v172, v172, v228, s[96:97]\n"
    "v_mfma_f32_16x16x32_bf16 v[24:27], a[48:51], a[36:39], v[24:27]\n"
    "ds_read_b128 a[24:27], v111 offset:51200\n"
    "v_cndmask_b32 v173, v173, v228, s[96:97]\n"
    "v_mfma_f32_16x16x32_bf16 v[44:47], a[52:55], a[36:39], v[44:47]\n"
    "ds_read_b128 a[28:31], v111 offset:52224\n"
    "buffer_load_dwordx4 v97, s[8:11], s69 offen lds\n"
    "v_mfma_f32_16x16x32_bf16 v[64:67], a[36:39], a[56:59], v[64:67]\n"
    "ds_read_b128 a[4:7], v105 offset:2048\n"
    "s_add_u32 m0, s3, 26624\n"
    "v_mfma_f32_16x16x32_bf16 v[84:87], a[60:63], a[36:39], v[84:87]\n"
    "ds_read_b128 a[8:11], v105 offset:4096\n"
    "v_cndmask_b32 v174, v174, v228, s[96:97]\n"
    "v_mfma_f32_16x16x32_bf16 v[28:31], a[48:51], a[40:43], v[28:31]\n"
    "ds_read_b128 a[12:15], v105 offset:6144\n"
    "v_cndmask_b32 v175, v175, v228, s[96:97]\n"
    "v_mfma_f32_16x16x32_bf16 v[48:51], a[52:55], a[40:43], v[48:51]\n"
    "ds_read_b128 v[168:171], v109 offset:0\n"
    "buffer_load_dwordx4 v98, s[8:11], s69 offen lds\n"
    "v_mfma_f32_16x16x32_bf16 v[68:71], a[40:43], a[56:59], v[68:71]\n"
    "ds_read_b128 a[64:67], v176 offset:50176\n"
    "s_add_u32 m0, s3, 30720\n"
    "v_mfma_f32_16x16x32_bf16 v[88:91], a[60:63], a[40:43], v[88:91]\n"
    "ds_read_b128 a[68:71], v177 offset:51200\n"
    "v_lshl_add_u32 v11, v9, 8, v11\n"
    "v_mfma_f32_16x16x32_bf16 v[32:35], a[48:51], a[44:47], v[32:35]\n"
    "v_lshl_add_u32 v113, v8, 4, v11\n"
    "v_mul_u32_u24 v11, 20480, v10\n"
    "v_mfma_f32_16x16x32_bf16 v[52:55], a[52:55], a[44:47], v[52:55]\n"
    "buffer_load_dwordx4 v99, s[8:11], s69 offen lds\n"
    "s_add_u32 m0, s3, 34816\n"
    "s_cmp_lt_u32 s36, 2\n"
    "s_cselect_b32 m0, m0, 162816\n"
    "v_mfma_f32_16x16x32_bf16 v[72:75], a[44:47], a[56:59], v[72:75]\n"
    "v_lshl_add_u32 v114, v8, 3, v11\n"
    "s_mul_i32 s50, s36, 640\n"
    "v_mfma_f32_16x16x32_bf16 v[92:95], a[60:63], a[44:47], v[92:95]\n"
    "s_add_u32 s50, s50, 91136\n"
    "v_cmp_eq_u32 s[54:55], 2, v10\n"
    "v_mfma_f32_16x16x32_bf16 v[36:39], a[72:75], v[172:175], v[36:39]\n"
    "v_mov_b32 v11, 93696\n"
    "v_mov_b32_e64 v9, s50\n"
    "v_mfma_f32_16x16x32_bf16 v[56:59], v[172:175], a[76:79], v[56:59]\n"
    "buffer_load_dwordx4 v100, s[8:11], s69 offen lds\n"
    "s_add_u32 s69, s69, 128\n"
    "s_waitcnt lgkmcnt(0)\n"
    "v_mfma_f32_16x16x32_bf16 v[20:23], a[16:19], a[0:3], v[20:23]\n"
    "ds_read_b128 a[48:51], v111 offset:57344\n"
    "s_cmp_lg_u32 s69, 1280\n"
    "s_cselect_b32 s69, s69, 0\n"
    "v_mfma_f32_16x16x32_bf16 v[40:43], a[20:23], a[0:3], v[40:43]\n"
    "ds_read_b128 a[32:35], v106 offset:0\n"
    "s_add_u32 m0, s14, 32768\n"
    "v_mfma_f32_16x16x32_bf16 v[60:63], a[0:3], a[24:27], v[60:63]\n"
    "ds_read_b128 a[52:55], v111 offset:58368\n"
    "v_cndmask_b32 v168, v168, v228, s[96:97]\n"
    "v_mfma_f32_16x16x32_bf16 v[80:83], a[28:31], a[0:3], v[80:83]\n"
    "ds_read_b128 a[56:59], v111 offset:59392\n"
    "buffer_load_dwordx4 v101, s[4:7], s12 offen offset:0 sc1 lds\n"
    "v_mfma_f32_16x16x32_bf16 v[24:27], a[16:19], a[4:7], v[24:27]\n"
    "ds_read_b128 a[60:63], v111 offset:60416\n"
    "v_cndmask_b32 v169, v169, v228, s[96:97]\n"
    "v_mfma_f32_16x16x32_bf16 v[44:47], a[20:23], a[4:7], v[44:47]\n"
    "buffer_store_dwordx4 v[236:239], v1, s[4:7], s57 offen offset:0 sc1\n"
    "v_mfma_f32_16x16x32_bf16 v[64:67], a[4:7], a[24:27], v[64:67]\n"
    "ds_read_b128 a[36:39], v106 offset:2048\n"
    "v_cndmask_b32 v170, v170, v228, s[96:97]\n"
    "v_mfma_f32_16x16x32_bf16 v[84:87], a[28:31], a[4:7], v[84:87]\n"
    "ds_read_b128 a[40:43], v106 offset:4096\n"
    "v_cndmask_b32 v171, v171, v228, s[96:97]\n"
    "v_mfma_f32_16x16x32_bf16 v[28:31], a[16:19], a[8:11], v[28:31]\n"
    "ds_read_b128 a[44:47], v106 offset:6144\n"
    "v_cndmask_b32 v11, v11, v9, s[54:55]\n"
    "v_mfma_f32_16x16x32_bf16 v[48:51], a[20:23], a[8:11], v[48:51]\n"
    "ds_read_b128 v[172:175], v110 offset:0\n"
    "buffer_load_dwordx4 v101, s[4:7], s12 offen offset:1024 sc1 lds\n"
    "v_mfma_f32_16x16x32_bf16 v[68:71], a[8:11], a[24:27], v[68:71]\n"
    "ds_read_b128 a[72:75], v176 offset:58368\n"
    "ds_read_b128 a[76:79], v177 offset:59392\n"
    "v_mfma_f32_16x16x32_bf16 v[88:91], a[28:31], a[8:11], v[88:91]\n"
    "v_lshl_add_u32 v115, v8, 3, v11\n"
    "v_lshrrev_b32_e64 v11, 2, v0\n"
    "v_mfma_f32_16x16x32_bf16 v[32:35], a[16:19], a[12:15], v[32:35]\n"
    "v_lshl_add_u32 v116, v11, 3, s50\n"
    "s_lshl_b32 s50, s37, 11\n"
    "v_mfma_f32_16x16x32_bf16 v[52:55], a[20:23], a[12:15], v[52:55]\n"
    "buffer_load_dwordx4 v101, s[4:7], s12 offen offset:2048 sc1 lds\n"
    "s_lshl_b32 s51, s36, 10\n"
    "v_mfma_f32_16x16x32_bf16 v[72:75], a[12:15], a[24:27], v[72:75]\n"
    "s_add_u32 s50, s50, s51\n"
    "s_add_u32 s50, s50, 196608\n"
    "v_mfma_f32_16x16x32_bf16 v[92:95], a[28:31], a[12:15], v[92:95]\n"
    "v_add_u32 v121, s50, v1\n"
    "s_sub_u32 s50, s37, 1\n"
    "v_mfma_f32_16x16x32_bf16 v[36:39], a[64:67], v[168:171], v[36:39]\n"
    "s_max_i32 s50, s50, 0\n"
    "s_lshl_b32 s50, s50, 11\n"
    "v_mfma_f32_16x16x32_bf16 v[56:59], v[168:171], a[68:71], v[56:59]\n"
    "buffer_load_dwordx4 v101, s[4:7], s12 offen offset:3072 sc1 lds\n"
    "s_add_u32 s12, s12, 16384\n"
    "s_waitcnt lgkmcnt(0)\n"
    "v_mfma_f32_16x16x32_bf16 v[20:23], a[48:51], a[32:35], v[20:23]\n"
    "s_barrier\n"
    "v_mfma_f32_16x16x32_bf16 v[40:43], a[52:55], a[32:35], v[40:43]\n"
    "ds_read_b128 a[16:19], v111 offset:0\n"
    "s_cmp_lg_u32 s12, 163840\n"
    "s_cselect_b32 s12, s12, 0\n"
    "v_mfma_f32_16x16x32_bf16 v[60:63], a[32:35], a[56:59], v[60:63]\n"
    "ds_read_b128 a[0:3], v105 offset:18432\n"
    "s_add_u32 m0, s3, 36864\n"
    "v_mfma_f32_16x16x32_bf16 v[80:83], a[60:63], a[32:35], v[80:83]\n"
    "ds_read_b128 a[20:23], v111 offset:1024\n"
    "buffer_load_dwordx4 v96, s[8:11], s69 offen lds\n"
    "v_mfma_f32_16x16x32_bf16 v[24:27], a[48:51], a[36:39], v[24:27]\n"
    "ds_read_b128 a[24:27], v111 offset:2048\n"
    "s_add_u32 m0, s3, 40960\n"
    "v_mfma_f32_16x16x32_bf16 v[44:47], a[52:55], a[36:39], v[44:47]\n"
    "buffer_store_dwordx4 v[236:239], v1, s[4:7], s57 offen offset:1024 sc1\n"
    "v_mfma_f32_16x16x32_bf16 v[64:67], a[36:39], a[56:59], v[64:67]\n"
    "ds_read_b128 a[28:31], v111 offset:3072\n"
    "v_cndmask_b32 v172, v172, v228, s[96:97]\n"
    "v_mfma_f32_16x16x32_bf16 v[84:87], a[60:63], a[36:39], v[84:87]\n"
    "ds_read_b128 a[4:7], v105 offset:20480\n"
    "v_cndmask_b32 v173, v173, v228, s[96:97]\n"
    "v_mfma_f32_16x16x32_bf16 v[28:31], a[48:51], a[40:43], v[28:31]\n"
    "ds_read_b128 a[8:11], v105 offset:22528\n"
    "v_cndmask_b32 v174, v174, v228, s[96:97]\n"
    "v_mfma_f32_16x16x32_bf16 v[48:51], a[52:55], a[40:43], v[48:51]\n"
    "ds_read_b128 a[12:15], v105 offset:24576\n"
    "buffer_load_dwordx4 v97, s[8:11], s69 offen lds\n"
    "v_mfma_f32_16x16x32_bf16 v[68:71], a[40:43], a[56:59], v[68:71]\n"
    "ds_read_b128 v[168:171], v109 offset:18432\n"
    "s_add_u32 m0, s3, 45056\n"
    "v_mfma_f32_16x16x32_bf16 v[88:91], a[60:63], a[40:43], v[88:91]\n"
    "ds_read_b128 a[64:67], v176 offset:1024\n"
    "ds_read_b128 a[68:71], v177 offset:2048\n"
    "v_mfma_f32_16x16x32_bf16 v[32:35], a[48:51], a[44:47], v[32:35]\n"
    "v_cndmask_b32 v175, v175, v228, s[96:97]\n"
    "s_add_u32 s50, s50, 197376\n"
    "v_mfma_f32_16x16x32_bf16 v[52:55], a[52:55], a[44:47], v[52:55]\n"
    "buffer_load_dwordx4 v98, s[8:11], s69 offen lds\n"
    "s_add_u32 m0, s3, 49152\n"
    "v_mfma_f32_16x16x32_bf16 v[72:75], a[44:47], a[56:59], v[72:75]\n"
    "v_and_b32 v9, 1, v10\n"
    "v_lshlrev_b32 v9, 10, v9\n"
    "v_mfma_f32_16x16x32_bf16 v[92:95], a[60:63], a[44:47], v[92:95]\n"
    "v_lshl_add_u32 v9, v8, 4, v9\n"
    "v_add_u32_e64 v9, s50, v9\n"
    "v_mfma_f32_16x16x32_bf16 v[36:39], a[72:75], v[172:175], v[36:39]\n"
    "s_cmp_lt_u32 s36, 2\n"
    "s_cselect_b64 s[54:55], -1, 0\n"
    "v_mfma_f32_16x16x32_bf16 v[56:59], v[172:175], a[76:79], v[56:59]\n"
    "buffer_load_dwordx4 v99, s[8:11], s69 offen lds\n"
    "s_add_u32 m0, s3, 53248\n"
    "s_cmp_lt_u32 s36, 2\n"
    "s_cselect_b32 m0, m0, 162816\n"
    "s_waitcnt lgkmcnt(0)\n"
    "v_mfma_f32_16x16x32_bf16 v[20:23], a[16:19], a[0:3], v[20:23]\n"
    "ds_read_b128 a[48:51], v111 offset:8192\n"
    "v_cndmask_b32 v168, v168, v228, s[96:97]\n"
    "v_mfma_f32_16x16x32_bf16 v[40:43], a[20:23], a[0:3], v[40:43]\n"
    "ds_read_b128 a[32:35], v106 offset:18432\n"
    "v_cndmask_b32 v169, v169, v228, s[96:97]\n"
    "v_mfma_f32_16x16x32_bf16 v[60:63], a[0:3], a[24:27], v[60:63]\n"
    "ds_read_b128 a[52:55], v111 offset:9216\n"
    "v_cndmask_b32 v170, v170, v228, s[96:97]\n"
    "v_mfma_f32_16x16x32_bf16 v[80:83], a[28:31], a[0:3], v[80:83]\n"
    "ds_read_b128 a[56:59], v111 offset:10240\n"
    "buffer_load_dwordx4 v100, s[8:11], s69 offen lds\n"
    "s_add_u32 s69, s69, 128\n"
    "v_mfma_f32_16x16x32_bf16 v[24:27], a[16:19], a[4:7], v[24:27]\n"
    "ds_read_b128 a[60:63], v111 offset:11264\n"
    "s_cmp_lg_u32 s69, 1280\n"
    "s_cselect_b32 s69, s69, 0\n"
    "v_mfma_f32_16x16x32_bf16 v[44:47], a[20:23], a[4:7], v[44:47]\n"
    "buffer_store_dwordx4 v[236:239], v234, s[4:7], s57 offen offset:2048 sc1\n"
    "v_mfma_f32_16x16x32_bf16 v[64:67], a[4:7], a[24:27], v[64:67]\n"
    "ds_read_b128 a[36:39], v106 offset:20480\n"
    "s_add_u32 m0, s14, 49152\n"
    "v_mfma_f32_16x16x32_bf16 v[84:87], a[28:31], a[4:7], v[84:87]\n"
    "ds_read_b128 a[40:43], v106 offset:22528\n"
    "v_cndmask_b32 v171, v171, v228, s[96:97]\n"
    "v_mfma_f32_16x16x32_bf16 v[28:31], a[16:19], a[8:11], v[28:31]\n"
    "ds_read_b128 a[44:47], v106 offset:24576\n"
    "v_cndmask_b32 v121, v9, v121, s[54:55]\n"
    "v_mfma_f32_16x16x32_bf16 v[48:51], a[20:23], a[8:11], v[48:51]\n"
    "ds_read_b128 v[172:175], v110 offset:18432\n"
    "buffer_load_dwordx4 v101, s[4:7], s12 offen offset:0 sc1 lds\n"
    "v_mfma_f32_16x16x32_bf16 v[68:71], a[8:11], a[24:27], v[68:71]\n"
    "ds_read_b128 a[72:75], v176 offset:9216\n"
    "ds_read_b128 a[76:79], v177 offset:10240\n"
    "v_mfma_f32_16x16x32_bf16 v[88:91], a[28:31], a[8:11], v[88:91]\n"
    "s_lshr_b32 s51, s36, 1\n"
    "s_lshl_b32 s50, s51, 10\n"
    "v_mfma_f32_16x16x32_bf16 v[32:35], a[16:19], a[12:15], v[32:35]\n"
    "s_add_u32 s50, s50, 157696\n"
    "v_lshl_add_u32 v117, v8, 3, s50\n"
    "v_mfma_f32_16x16x32_bf16 v[52:55], a[20:23], a[12:15], v[52:55]\n"
    "buffer_load_dwordx4 v101, s[4:7], s12 offen offset:1024 sc1 lds\n"
    "s_add_u32 s50, s50, 128\n"
    "v_mfma_f32_16x16x32_bf16 v[72:75], a[12:15], a[24:27], v[72:75]\n"
    "v_lshl_add_u32 v119, v10, 4, s50\n"
    "v_bfe_u32 v9, v0, 3, 1\n"
    "v_mfma_f32_16x16x32_bf16 v[92:95], a[28:31], a[12:15], v[92:95]\n"
    "v_and_b32 v11, 7, v0\n"
    "v_lshlrev_b32_e64 v11, 3, v11\n"
    "v_mfma_f32_16x16x32_bf16 v[36:39], a[64:67], v[168:171], v[36:39]\n"
    "v_lshl_add_u32 v9, v9, 8, v11\n"
    "v_add_u32 v118, 159808, v9\n"
    "v_mfma_f32_16x16x32_bf16 v[56:59], v[168:171], a[68:71], v[56:59]\n"
    "buffer_load_dwordx4 v101, s[4:7], s12 offen offset:2048 sc1 lds\n"
    "v_lshrrev_b32 v9, 5, v0\n"
    "s_waitcnt lgkmcnt(0)\n"
    "v_mfma_f32_16x16x32_bf16 v[20:23], a[48:51], a[32:35], v[20:23]\n"
    "s_waitcnt vmcnt(20)\n"
    "s_barrier\n"
    "v_mfma_f32_16x16x32_bf16 v[40:43], a[52:55], a[32:35], v[40:43]\n"
    "ds_read_b128 a[16:19], v111 offset:16384\n"
    "v_cndmask_b32 v172, v172, v228, s[96:97]\n"
    "v_mfma_f32_16x16x32_bf16 v[60:63], a[32:35], a[56:59], v[60:63]\n"
    "ds_read_b128 a[0:3], v103 offset:0\n"
    "v_cndmask_b32 v173, v173, v228, s[96:97]\n"
    "v_mfma_f32_16x16x32_bf16 v[80:83], a[60:63], a[32:35], v[80:83]\n"
    "ds_read_b128 a[20:23], v111 offset:17408\n"
    "buffer_load_dwordx4 v101, s[4:7], s12 offen offset:3072 sc1 lds\n"
    "v_mfma_f32_16x16x32_bf16 v[24:27], a[48:51], a[36:39], v[24:27]\n"
    "ds_read_b128 a[24:27], v111 offset:18432\n"
    "s_add_u32 s12, s12, 16384\n"
    "v_mfma_f32_16x16x32_bf16 v[44:47], a[52:55], a[36:39], v[44:47]\n"
    "buffer_store_dwordx4 v[236:239], v1, s[4:7], s56 offen offset:0 sc1\n"
    "v_mfma_f32_16x16x32_bf16 v[64:67], a[36:39], a[56:59], v[64:67]\n"
    "ds_read_b128 a[28:31], v111 offset:19456\n"
    "s_cmp_lg_u32 s12, 163840\n"
    "s_cselect_b32 s12, s12, 0\n"
    "v_mfma_f32_16x16x32_bf16 v[84:87], a[60:63], a[36:39], v[84:87]\n"
    "ds_read_b128 a[4:7], v103 offset:2048\n"
    "s_add_u32 m0, s3, 55296\n"
    "v_mfma_f32_16x16x32_bf16 v[28:31], a[48:51], a[40:43], v[28:31]\n"
    "ds_read_b128 a[8:11], v103 offset:4096\n"
    "v_cndmask_b32 v174, v174, v228, s[96:97]\n"
    "v_mfma_f32_16x16x32_bf16 v[48:51], a[52:55], a[40:43], v[48:51]\n"
    "ds_read_b128 a[12:15], v103 offset:6144\n"
    "buffer_load_dwordx4 v96, s[8:11], s69 offen lds\n"
    "v_mfma_f32_16x16x32_bf16 v[68:71], a[40:43], a[56:59], v[68:71]\n"
    "ds_read_b128 v[168:171], v107 offset:0\n"
    "s_add_u32 m0, s3, 59392\n"
    "v_mfma_f32_16x16x32_bf16 v[88:91], a[60:63], a[40:43], v[88:91]\n"
    "ds_read_b128 a[64:67], v176 offset:17408\n"
    "ds_read_b128 a[68:71], v177 offset:18432\n"
    "v_mfma_f32_16x16x32_bf16 v[32:35], a[48:51], a[44:47], v[32:35]\n"
    "v_cndmask_b32 v175, v175, v228, s[96:97]\n"
    "v_bfe_u32 v11, v0, 4, 1\n"
    "v_mfma_f32_16x16x32_bf16 v[52:55], a[52:55], a[44:47], v[52:55]\n"
    "buffer_load_dwordx4 v97, s[8:11], s69 offen lds\n"
    "s_add_u32 m0, s3, 63488\n"
    "v_mfma_f32_16x16x32_bf16 v[72:75], a[44:47], a[56:59], v[72:75]\n"
    "v_lshlrev_b32 v11, 4, v11\n"
    "v_lshl_add_u32 v9, v9, 8, v11\n"
    "v_mfma_f32_16x16x32_bf16 v[92:95], a[60:63], a[44:47], v[92:95]\n"
    "v_add_u32 v120, 159904, v9\n"
    "s_mul_i32 s50, s36, 2304\n"
    "v_mfma_f32_16x16x32_bf16 v[36:39], a[72:75], v[172:175], v[36:39]\n"
    "s_add_u32 s50, s50, 124928\n"
    "v_add_u32_e64 v180, s50, v1\n"
    "v_mfma_f32_16x16x32_bf16 v[56:59], v[172:175], a[76:79], v[56:59]\n"
    "buffer_load_dwordx4 v98, s[8:11], s69 offen lds\n"
    "s_add_u32 m0, s3, 67584\n"
    "s_waitcnt lgkmcnt(0)\n"
    "v_mfma_f32_16x16x32_bf16 v[20:23], a[16:19], a[0:3], v[20:23]\n"
    "ds_read_b128 a[48:51], v111 offset:24576\n"
    "v_cndmask_b32 v168, v168, v228, s[96:97]\n"
    "v_mfma_f32_16x16x32_bf16 v[40:43], a[20:23], a[0:3], v[40:43]\n"
    "s_add_u32 s67, s95, Lcatt_%=-Lpc_%=+0\n"
    "s_min_u32 s67, s67, Lend_%=-Lpc_%=-4\n"
    "v_mov_b32 v255, s67\n"
    "global_load_dword v255, v255, s[98:99]\n"
    "ds_read_b128 a[32:35], v104 offset:0\n"
    "v_cndmask_b32 v169, v169, v228, s[96:97]\n"
    "v_mfma_f32_16x16x32_bf16 v[60:63], a[0:3], a[24:27], v[60:63]\n"
    "ds_read_b128 a[52:55], v111 offset:25600\n"
    "v_cndmask_b32 v170, v170, v228, s[96:97]\n"
    "v_mfma_f32_16x16x32_bf16 v[80:83], a[28:31], a[0:3], v[80:83]\n"
    "ds_read_b128 a[56:59], v111 offset:26624\n"
    "buffer_load_dwordx4 v99, s[8:11], s69 offen lds\n"
    "v_mfma_f32_16x16x32_bf16 v[24:27], a[16:19], a[4:7], v[24:27]\n"
    "ds_read_b128 a[60:63], v111 offset:27648\n"
    "s_add_u32 m0, s3, 71680\n"
    "s_cmp_lt_u32 s36, 2\n"
    "s_cselect_b32 m0, m0, 162816\n"
    "v_mfma_f32_16x16x32_bf16 v[44:47], a[20:23], a[4:7], v[44:47]\n"
    "buffer_store_dwordx4 v[236:239], v1, s[4:7], s56 offen offset:1024 sc1\n"
    "v_mfma_f32_16x16x32_bf16 v[64:67], a[4:7], a[24:27], v[64:67]\n"
    "ds_read_b128 a[36:39], v104 offset:2048\n"
    "v_cndmask_b32 v171, v171, v228, s[96:97]\n"
    "v_mfma_f32_16x16x32_bf16 v[84:87], a[28:31], a[4:7], v[84:87]\n"
    "ds_read_b128 a[40:43], v104 offset:4096\n"
    "s_and_b32 s50, s36, 1\n"
    "v_mfma_f32_16x16x32_bf16 v[28:31], a[16:19], a[8:11], v[28:31]\n"
    "ds_read_b128 a[44:47], v104 offset:6144\n"
    "s_mul_i32 s50, s50, 2304\n"
    "v_mfma_f32_16x16x32_bf16 v[48:51], a[20:23], a[8:11], v[48:51]\n"
    "ds_read_b128 v[172:175], v108 offset:0\n"
    "buffer_load_dwordx4 v100, s[8:11], s69 offen lds\n"
    "s_add_u32 s69, s69, 128\n"
    "v_mfma_f32_16x16x32_bf16 v[68:71], a[8:11], a[24:27], v[68:71]\n"
    "ds_read_b128 a[72:75], v176 offset:25600\n"
    "s_cmp_lg_u32 s69, 1280\n"
    "s_cselect_b32 s69, s69, 0\n"
    "v_mfma_f32_16x16x32_bf16 v[88:91], a[28:31], a[8:11], v[88:91]\n"
    "ds_read_b128 a[76:79], v177 offset:26624\n"
    "s_add_u32 m0, s14, 0\n"
    "v_mfma_f32_16x16x32_bf16 v[32:35], a[16:19], a[12:15], v[32:35]\n"
    "s_add_u32 s50, s50, 124928\n"
    "v_add_u32_e64 v181, s50, v1\n"
    "v_mfma_f32_16x16x32_bf16 v[52:55], a[20:23], a[12:15], v[52:55]\n"
    "buffer_load_dwordx4 v101, s[4:7], s12 offen offset:0 sc1 lds\n"
    "v_lshrrev_b32_e64 v9, 2, v1\n"
    "v_mfma_f32_16x16x32_bf16 v[72:75], a[12:15], a[24:27], v[72:75]\n"
    "v_sub_u32 v182, v180, v1\n"
    "v_add_u32 v182, v182, v9\n"
    "v_mfma_f32_16x16x32_bf16 v[92:95], a[28:31], a[12:15], v[92:95]\n"
    "v_lshl_add_u32 v183, v10, 4, s50\n"
    "v_lshlrev_b32_e64 v11, 2, v10\n"
    "v_mfma_f32_16x16x32_bf16 v[36:39], a[64:67], v[168:171], v[36:39]\n"
    "v_sub_u32_e64 v11, v8, v11\n"
    "v_mov_b32 v7, 0xf149f2ca\n"
    "v_mfma_f32_16x16x32_bf16 v[56:59], v[168:171], a[68:71], v[56:59]\n"
    "buffer_load_dwordx4 v101, s[4:7], s12 offen offset:1024 sc1 lds\n"
    "v_mov_b32 v232, 0\n"
    "s_waitcnt lgkmcnt(0)\n"
    "v_mfma_f32_16x16x32_bf16 v[20:23], a[48:51], a[32:35], v[20:23]\n"
    "s_waitcnt vmcnt(14)\n"
    "s_barrier\n"
    "s_cmp_lg_u32 s36, 0\n"
    "s_cbranch_scc1 Lol7_%=\n"
    "v_mov_b32 v4, s42\n"
    "v_mov_b32 v5, s43\n"
    "s_lshl4_add_u32 s49, s37, s38\n"
    "s_lshl3_add_u32 s49, s49, 43008\n"
    "v_mov_b32 v6, s49\n"
    "s_mov_b64 exec, 1\n"
    "global_store_dwordx2 v6, v[4:5], s[4:5] sc1\n"
    "s_mov_b64 exec, -1\n"
    "Lol7_%=:\n"
    "v_mfma_f32_16x16x32_bf16 v[40:43], a[52:55], a[32:35], v[40:43]\n"
    "ds_read_b128 a[16:19], v111 offset:32768\n"
    "v_cndmask_b32 v172, v172, v228, s[96:97]\n"
    "v_mfma_f32_16x16x32_bf16 v[60:63], a[32:35], a[56:59], v[60:63]\n"
    "ds_read_b128 a[0:3], v103 offset:18432\n"
    "v_cndmask_b32 v173, v173, v228, s[96:97]\n"
    "v_mfma_f32_16x16x32_bf16 v[80:83], a[60:63], a[32:35], v[80:83]\n"
    "ds_read_b128 a[20:23], v111 offset:33792\n"
    "buffer_load_dwordx4 v101, s[4:7], s12 offen offset:2048 sc1 lds\n"
    "v_mfma_f32_16x16x32_bf16 v[24:27], a[48:51], a[36:39], v[24:27]\n"
    "ds_read_b128 a[24:27], v111 offset:34816\n"
    "v_cndmask_b32 v174, v174, v228, s[96:97]\n"
    "v_mfma_f32_16x16x32_bf16 v[44:47], a[52:55], a[36:39], v[44:47]\n"
    "buffer_store_dwordx4 v[236:239], v234, s[4:7], s56 offen offset:2048 sc1\n"
    "buffer_store_dwordx4 v[236:239], v123, s[4:7], s65 offen offset:0 sc1\n"
    "v_mfma_f32_16x16x32_bf16 v[64:67], a[36:39], a[56:59], v[64:67]\n"
    "ds_read_b128 a[28:31], v111 offset:35840\n"
    "v_cndmask_b32 v175, v175, v228, s[96:97]\n"
    "v_mfma_f32_16x16x32_bf16 v[84:87], a[60:63], a[36:39], v[84:87]\n"
    "ds_read_b128 a[4:7], v103 offset:20480\n"
    "v_mov_b32_e64 v233, 0\n"
    "v_mfma_f32_16x16x32_bf16 v[28:31], a[48:51], a[40:43], v[28:31]\n"
    "ds_read_b128 a[8:11], v103 offset:22528\n"
    "v_mov_b32_e64 v178, 0\n"
    "v_mfma_f32_16x16x32_bf16 v[48:51], a[52:55], a[40:43], v[48:51]\n"
    "ds_read_b128 a[12:15], v103 offset:24576\n"
    "buffer_load_dwordx4 v101, s[4:7], s12 offen offset:3072 sc1 lds\n"
    "v_mfma_f32_16x16x32_bf16 v[68:71], a[40:43], a[56:59], v[68:71]\n"
    "ds_read_b128 v[168:171], v107 offset:18432\n"
    "s_add_u32 s12, s12, 16384\n"
    "v_mfma_f32_16x16x32_bf16 v[88:91], a[60:63], a[40:43], v[88:91]\n"
    "ds_read_b128 a[64:67], v176 offset:33792\n"
    "s_cmp_lg_u32 s12, 163840\n"
    "s_cselect_b32 s12, s12, 0\n"
    "v_mfma_f32_16x16x32_bf16 v[32:35], a[48:51], a[44:47], v[32:35]\n"
    "ds_read_b128 a[68:71], v177 offset:34816\n"
    "s_add_u32 m0, s3, 73728\n"
    "v_mfma_f32_16x16x32_bf16 v[52:55], a[52:55], a[44:47], v[52:55]\n"
    "buffer_load_dwordx4 v96, s[8:11], s69 offen lds\n"
    "s_add_u32 m0, s3, 77824\n"
    "v_mfma_f32_16x16x32_bf16 v[72:75], a[44:47], a[56:59], v[72:75]\n"
    "v_mov_b32 v179, 0\n"
    "s_lshl_b32 s50, s51, 3\n"
    "v_mfma_f32_16x16x32_bf16 v[92:95], a[60:63], a[44:47], v[92:95]\n"
    "v_add_u32 v252, s50, v11\n"
    "s_lshl1_add_u32 s50, s51, 2\n"
    "v_mfma_f32_16x16x32_bf16 v[36:39], a[72:75], v[172:175], v[36:39]\n"
    "v_cmp_gt_u32 s[52:53], s50, v10\n"
    "v_mfma_f32_16x16x32_bf16 v[56:59], v[172:175], a[76:79], v[56:59]\n"
    "buffer_load_dwordx4 v97, s[8:11], s69 offen lds\n"
    "s_add_u32 m0, s3, 81920\n"
    "s_waitcnt lgkmcnt(0)\n"
    "v_mfma_f32_16x16x32_bf16 v[20:23], a[16:19], a[0:3], v[20:23]\n"
    "ds_read_b128 a[48:51], v111 offset:40960\n"
    "v_cndmask_b32 v168, v168, v228, s[96:97]\n"
    "v_mfma_f32_16x16x32_bf16 v[40:43], a[20:23], a[0:3], v[40:43]\n"
    "ds_read_b128 a[32:35], v104 offset:18432\n"
    "v_cndmask_b32 v169, v169, v228, s[96:97]\n"
    "v_mfma_f32_16x16x32_bf16 v[60:63], a[0:3], a[24:27], v[60:63]\n"
    "ds_read_b128 a[52:55], v111 offset:41984\n"
    "v_cndmask_b32 v170, v170, v228, s[96:97]\n"
    "v_mfma_f32_16x16x32_bf16 v[80:83], a[28:31], a[0:3], v[80:83]\n"
    "ds_read_b128 a[56:59], v111 offset:43008\n"
    "buffer_load_dwordx4 v98, s[8:11], s69 offen lds\n"
    "v_mfma_f32_16x16x32_bf16 v[24:27], a[16:19], a[4:7], v[24:27]\n"
    "ds_read_b128 a[60:63], v111 offset:44032\n"
    "s_add_u32 m0, s3, 86016\n"
    "v_mfma_f32_16x16x32_bf16 v[44:47], a[20:23], a[4:7], v[44:47]\n"
    "buffer_store_dwordx4 v[236:239], v1, s[4:7], s58 offen offset:0 sc1\n"
    "v_mfma_f32_16x16x32_bf16 v[64:67], a[4:7], a[24:27], v[64:67]\n"
    "ds_read_b128 a[36:39], v104 offset:20480\n"
    "v_cndmask_b32 v171, v171, v228, s[96:97]\n"
    "v_mfma_f32_16x16x32_bf16 v[84:87], a[28:31], a[4:7], v[84:87]\n"
    "ds_read_b128 a[40:43], v104 offset:22528\n"
    "v_mfma_f32_16x16x32_bf16 v[28:31], a[16:19], a[8:11], v[28:31]\n"
    "ds_read_b128 a[44:47], v104 offset:24576\n"
    "v_mfma_f32_16x16x32_bf16 v[48:51], a[20:23], a[8:11], v[48:51]\n"
    "ds_read_b128 v[172:175], v108 offset:18432\n"
    "buffer_load_dwordx4 v99, s[8:11], s69 offen lds\n"
    "v_mfma_f32_16x16x32_bf16 v[68:71], a[8:11], a[24:27], v[68:71]\n"
    "ds_read_b128 a[72:75], v176 offset:41984\n"
    "s_add_u32 m0, s3, 90112\n"
    "s_cmp_lt_u32 s36, 2\n"
    "s_cselect_b32 m0, m0, 162816\n"
    "v_mfma_f32_16x16x32_bf16 v[88:91], a[28:31], a[8:11], v[88:91]\n"
    "ds_read_b128 a[76:79], v177 offset:43008\n"
    "v_mfma_f32_16x16x32_bf16 v[32:35], a[16:19], a[12:15], v[32:35]\n"
    "s_cmp_eq_u32 s37, 0\n"
    "s_cselect_b64 s[52:53], 0, s[52:53]\n"
    "v_mfma_f32_16x16x32_bf16 v[52:55], a[20:23], a[12:15], v[52:55]\n"
    "buffer_load_dwordx4 v100, s[8:11], s69 offen lds\n"
    "s_add_u32 s69, s69, 128\n"
    "v_mfma_f32_16x16x32_bf16 v[72:75], a[12:15], a[24:27], v[72:75]\n"
    "s_cmp_lg_u32 s69, 1280\n"
    "s_cselect_b32 s69, s69, 0\n"
    "v_mfma_f32_16x16x32_bf16 v[92:95], a[28:31], a[12:15], v[92:95]\n"
    "s_add_u32 m0, s14, 16384\n"
    "v_subrev_u32 v9, 0, v11\n"
    "v_mfma_f32_16x16x32_bf16 v[36:39], a[64:67], v[168:171], v[36:39]\n"
    "v_cmp_lt_u32 s[54:55], 7, v9\n"
    "v_mov_b32 v9, 65535\n"
    "v_mfma_f32_16x16x32_bf16 v[56:59], v[168:171], a[68:71], v[56:59]\n"
    "buffer_load_dwordx4 v101, s[4:7], s12 offen offset:0 sc1 lds\n"
    "v_cndmask_b32 v220, 0, v7, s[54:55]\n"
    "s_waitcnt lgkmcnt(0)\n"
    "v_mfma_f32_16x16x32_bf16 v[20:23], a[48:51], a[32:35], v[20:23]\n"
    "s_waitcnt vmcnt(21)\n"
    "s_barrier\n"
    "v_mfma_f32_16x16x32_bf16 v[40:43], a[52:55], a[32:35], v[40:43]\n"
    "global_load_dwordx2 v[12:13], v112, s[4:5] sc1\n"
    "ds_read_b128 a[16:19], v111 offset:49152\n"
    "v_mfma_f32_16x16x32_bf16 v[60:63], a[32:35], a[56:59], v[60:63]\n"
    "ds_read_b128 a[0:3], v103 offset:36864\n"
    "v_cndmask_b32 v172, v172, v228, s[96:97]\n"
    "v_mfma_f32_16x16x32_bf16 v[80:83], a[60:63], a[32:35], v[80:83]\n"
    "ds_read_b128 a[20:23], v111 offset:50176\n"
    "buffer_load_dwordx4 v101, s[4:7], s12 offen offset:1024 sc1 lds\n"
    "v_mfma_f32_16x16x32_bf16 v[24:27], a[48:51], a[36:39], v[24:27]\n"
    "ds_read_b128 a[24:27], v111 offset:51200\n"
    "v_cndmask_b32 v173, v173, v228, s[96:97]\n"
    "v_mfma_f32_16x16x32_bf16 v[44:47], a[52:55], a[36:39], v[44:47]\n"
    "buffer_store_dwordx4 v[236:239], v1, s[4:7], s58 offen offset:1024 sc1\n"
    "v_mfma_f32_16x16x32_bf16 v[64:67], a[36:39], a[56:59], v[64:67]\n"
    "ds_read_b128 a[28:31], v111 offset:52224\n"
    "v_cndmask_b32 v174, v174, v228, s[96:97]\n"
    "v_mfma_f32_16x16x32_bf16 v[84:87], a[60:63], a[36:39], v[84:87]\n"
    "ds_read_b128 a[4:7], v103 offset:38912\n"
    "v_cndmask_b32 v175, v175, v228, s[96:97]\n"
    "v_mfma_f32_16x16x32_bf16 v[28:31], a[48:51], a[40:43], v[28:31]\n"
    "ds_read_b128 a[8:11], v103 offset:40960\n"
    "v_cmp_ge_i32 s[54:55], -9, v11\n"
    "v_mfma_f32_16x16x32_bf16 v[48:51], a[52:55], a[40:43], v[48:51]\n"
    "ds_read_b128 a[12:15], v103 offset:43008\n"
    "buffer_load_dwordx4 v101, s[4:7], s12 offen offset:2048 sc1 lds\n"
    "v_mfma_f32_16x16x32_bf16 v[68:71], a[40:43], a[56:59], v[68:71]\n"
    "ds_read_b128 v[168:171], v107 offset:36864\n"
    "ds_read_b128 a[64:67], v176 offset:50176\n"
    "v_mfma_f32_16x16x32_bf16 v[88:91], a[60:63], a[40:43], v[88:91]\n"
    "ds_read_b128 a[68:71], v177 offset:51200\n"
    "v_cmp_ge_i32 s[50:51], -1, v252\n"
    "v_mfma_f32_16x16x32_bf16 v[32:35], a[48:51], a[44:47], v[32:35]\n"
    "v_cndmask_b32 v216, v7, 0, s[54:55]\n"
    "v_cndmask_b32 v9, 0, v9, s[54:55]\n"
    "v_mfma_f32_16x16x32_bf16 v[52:55], a[52:55], a[44:47], v[52:55]\n"
    "buffer_load_dwordx4 v101, s[4:7], s12 offen offset:3072 sc1 lds\n"
    "s_add_u32 s12, s12, 16384\n"
    "v_mfma_f32_16x16x32_bf16 v[72:75], a[44:47], a[56:59], v[72:75]\n"
    "s_cmp_lg_u32 s12, 163840\n"
    "s_cselect_b32 s12, s12, 0\n"
    "v_mfma_f32_16x16x32_bf16 v[92:95], a[60:63], a[44:47], v[92:95]\n"
    "s_and_b64 s[50:51], s[50:51], s[52:53]\n"
    "v_or_b32_e64 v232, v232, v9\n"
    "v_mfma_f32_16x16x32_bf16 v[36:39], a[72:75], v[172:175], v[36:39]\n"
    "v_mov_b32 v9, 65535\n"
    "v_cndmask_b32 v224, v7, 0, s[50:51]\n"
    "v_mfma_f32_16x16x32_bf16 v[56:59], v[172:175], a[76:79], v[56:59]\n"
    "v_cndmask_b32 v9, 0, v9, s[50:51]\n"
    "v_or_b32 v178, v178, v9\n"
    "s_waitcnt lgkmcnt(0)\n"
    "v_mfma_f32_16x16x32_bf16 v[20:23], a[16:19], a[0:3], v[20:23]\n"
    "ds_read_b128 a[48:51], v111 offset:57344\n"
    "v_cndmask_b32 v168, v168, v228, s[96:97]\n"
    "v_mfma_f32_16x16x32_bf16 v[40:43], a[20:23], a[0:3], v[40:43]\n"
    "ds_read_b128 a[32:35], v104 offset:36864\n"
    "v_cndmask_b32 v169, v169, v228, s[96:97]\n"
    "v_mfma_f32_16x16x32_bf16 v[60:63], a[0:3], a[24:27], v[60:63]\n"
    "ds_read_b128 a[52:55], v111 offset:58368\n"
    "v_cndmask_b32 v170, v170, v228, s[96:97]\n"
    "v_mfma_f32_16x16x32_bf16 v[80:83], a[28:31], a[0:3], v[80:83]\n"
    "ds_read_b128 a[56:59], v111 offset:59392\n"
    "v_cndmask_b32 v171, v171, v228, s[96:97]\n"
    "v_mfma_f32_16x16x32_bf16 v[24:27], a[16:19], a[4:7], v[24:27]\n"
    "ds_read_b128 a[60:63], v111 offset:60416\n"
    "v_subrev_u32 v9, 1, v11\n"
    "v_mfma_f32_16x16x32_bf16 v[44:47], a[20:23], a[4:7], v[44:47]\n"
    "buffer_store_dwordx4 v[236:239], v1, s[4:7], s58 offen offset:2048 sc1\n"
    "v_mfma_f32_16x16x32_bf16 v[64:67], a[4:7], a[24:27], v[64:67]\n"
    "ds_read_b128 a[36:39], v104 offset:38912\n"
    "v_cmp_lt_u32 s[54:55], 7, v9\n"
    "v_mfma_f32_16x16x32_bf16 v[84:87], a[28:31], a[4:7], v[84:87]\n"
    "ds_read_b128 a[40:43], v104 offset:40960\n"
    "v_mov_b32 v9, 4294901760\n"
    "v_mfma_f32_16x16x32_bf16 v[28:31], a[16:19], a[8:11], v[28:31]\n"
    "ds_read_b128 a[44:47], v104 offset:43008\n"
    "v_cndmask_b32 v221, 0, v7, s[54:55]\n"
    "v_mfma_f32_16x16x32_bf16 v[48:51], a[20:23], a[8:11], v[48:51]\n"
    "ds_read_b128 v[172:175], v108 offset:36864\n"
    "ds_read_b128 a[72:75], v176 offset:58368\n"
    "v_mfma_f32_16x16x32_bf16 v[68:71], a[8:11], a[24:27], v[68:71]\n"
    "ds_read_b128 a[76:79], v177 offset:59392\n"
    "v_cmp_ge_i32 s[54:55], -8, v11\n"
    "v_mfma_f32_16x16x32_bf16 v[88:91], a[28:31], a[8:11], v[88:91]\n"
    "v_cmp_ge_i32 s[50:51], 0, v252\n"
    "v_cndmask_b32 v217, v7, 0, s[54:55]\n"
    "v_mfma_f32_16x16x32_bf16 v[32:35], a[16:19], a[12:15], v[32:35]\n"
    "v_cndmask_b32 v9, 0, v9, s[54:55]\n"
    "s_and_b64 s[50:51], s[50:51], s[52:53]\n"
    "v_mfma_f32_16x16x32_bf16 v[52:55], a[20:23], a[12:15], v[52:55]\n"
    "v_or_b32_e64 v232, v232, v9\n"
    "v_mov_b32 v9, 4294901760\n"
    "v_mfma_f32_16x16x32_bf16 v[72:75], a[12:15], a[24:27], v[72:75]\n"
    "v_cndmask_b32 v225, v7, 0, s[50:51]\n"
    "v_cndmask_b32 v9, 0, v9, s[50:51]\n"
    "v_mfma_f32_16x16x32_bf16 v[92:95], a[28:31], a[12:15], v[92:95]\n"
    "v_or_b32 v178, v178, v9\n"
    "v_subrev_u32 v9, 2, v11\n"
    "v_mfma_f32_16x16x32_bf16 v[36:39], a[64:67], v[168:171], v[36:39]\n"
    "v_cmp_lt_u32 s[54:55], 7, v9\n"
    "v_mov_b32 v9, 65535\n"
    "v_mfma_f32_16x16x32_bf16 v[56:59], v[168:171], a[68:71], v[56:59]\n"
    "v_cndmask_b32 v222, 0, v7, s[54:55]\n"
    "v_cmp_ge_i32 s[54:55], -7, v11\n"
    "s_waitcnt lgkmcnt(0)\n"
    "v_mfma_f32_16x16x32_bf16 v[20:23], a[48:51], a[32:35], v[20:23]\n"
    "s_waitcnt vmcnt(13)\n"
    "s_waitcnt vmcnt(5)\n"
    "v_cmp_ne_u64 vcc, s[42:43], v[12:13]\n"
    "s_nop 0\n"
    "s_cbranch_vccnz Lf2r8_%=\n"
    "Lf2b9_%=:\n"
    "s_barrier\n"
    "s_lshl2_add_u32 s49, s2, s36\n"
    "s_cmp_lg_u32 s49, 0\n"
    "s_cbranch_scc1 Lol11_%=\n"
    "v_mov_b32 v4, s42\n"
    "v_mov_b32 v5, s43\n"
    "s_mov_b32 s49, 0\n"
    "v_mov_b32 v6, s49\n"
    "s_mov_b64 exec, 1\n"
    "global_store_dwordx2 v6, v[4:5], s[4:5] sc1\n"
    "s_mov_b64 exec, -1\n"
    "Lol11_%=:\n"
    "v_mfma_f32_16x16x32_bf16 v[40:43], a[52:55], a[32:35], v[40:43]\n"
    "ds_read_b128 a[16:19], v111 offset:0\n"
    "s_add_u32 m0, s3, 157696\n"
    "v_mfma_f32_16x16x32_bf16 v[60:63], a[32:35], a[56:59], v[60:63]\n"
    "ds_read_b128 a[0:3], v105 offset:0\n"
    "buffer_load_dwordx4 v121, s[4:7], 0 offen sc1 lds\n"
    "v_mfma_f32_16x16x32_bf16 v[80:83], a[60:63], a[32:35], v[80:83]\n"
    "ds_read_b128 a[20:23], v111 offset:1024\n"
    "s_add_u32 s24, s22, 0\n"
    "v_mfma_f32_16x16x32_bf16 v[24:27], a[48:51], a[36:39], v[24:27]\n"
    "ds_read_b128 a[24:27], v111 offset:2048\n"
    "v_cndmask_b32 v172, v172, v228, s[96:97]\n"
    "v_mfma_f32_16x16x32_bf16 v[44:47], a[52:55], a[36:39], v[44:47]\n"
    "buffer_store_dwordx4 v[236:239], v1, s[4:7], s58 offen offset:3072 sc1\n"
    "v_mfma_f32_16x16x32_bf16 v[64:67], a[36:39], a[56:59], v[64:67]\n"
    "ds_read_b128 a[28:31], v111 offset:3072\n"
    "v_cndmask_b32 v173, v173, v228, s[96:97]\n"
    "v_mfma_f32_16x16x32_bf16 v[84:87], a[60:63], a[36:39], v[84:87]\n"
    "ds_read_b128 a[4:7], v105 offset:2048\n"
    "v_cndmask_b32 v174, v174, v228, s[96:97]\n"
    "v_mfma_f32_16x16x32_bf16 v[28:31], a[48:51], a[40:43], v[28:31]\n"
    "ds_read_b128 a[8:11], v105 offset:4096\n"
    "v_cndmask_b32 v175, v175, v228, s[96:97]\n"
    "v_mfma_f32_16x16x32_bf16 v[48:51], a[52:55], a[40:43], v[48:51]\n"
    "ds_read_b128 a[12:15], v105 offset:6144\n"
    "buffer_load_dwordx4 a[128:131], v1, s[4:7], s24 offen offset:0 sc1\n"
    "v_mfma_f32_16x16x32_bf16 v[68:71], a[40:43], a[56:59], v[68:71]\n"
    "ds_read_b128 v[168:171], v109 offset:0\n"
    "ds_read_b128 a[64:67], v176 offset:1024\n"
    "v_mfma_f32_16x16x32_bf16 v[88:91], a[60:63], a[40:43], v[88:91]\n"
    "ds_read_b128 a[68:71], v177 offset:2048\n"
    "v_cmp_ge_i32 s[50:51], 1, v252\n"
    "v_mfma_f32_16x16x32_bf16 v[32:35], a[48:51], a[44:47], v[32:35]\n"
    "v_cndmask_b32 v218, v7, 0, s[54:55]\n"
    "v_cndmask_b32 v9, 0, v9, s[54:55]\n"
    "v_mfma_f32_16x16x32_bf16 v[52:55], a[52:55], a[44:47], v[52:55]\n"
    "buffer_load_dwordx4 a[132:135], v1, s[4:7], s24 offen offset:1024 sc1\n"
    "s_and_b64 s[50:51], s[50:51], s[52:53]\n"
    "v_mfma_f32_16x16x32_bf16 v[72:75], a[44:47], a[56:59], v[72:75]\n"
    "v_or_b32_e64 v233, v233, v9\n"
    "v_mov_b32 v9, 65535\n"
    "v_mfma_f32_16x16x32_bf16 v[92:95], a[60:63], a[44:47], v[92:95]\n"
    "v_cndmask_b32 v226, v7, 0, s[50:51]\n"
    "v_cndmask_b32 v9, 0, v9, s[50:51]\n"
    "v_mfma_f32_16x16x32_bf16 v[36:39], a[72:75], v[172:175], v[36:39]\n"
    "v_or_b32 v179, v179, v9\n"
    "v_subrev_u32 v9, 3, v11\n"
    "v_mfma_f32_16x16x32_bf16 v[56:59], v[172:175], a[76:79], v[56:59]\n"
    "buffer_load_dwordx4 a[136:139], v1, s[4:7], s24 offen offset:2048 sc1\n"
    "v_cmp_lt_u32 s[54:55], 7, v9\n"
    "s_waitcnt lgkmcnt(0)\n"
    "v_mfma_f32_16x16x32_bf16 v[20:23], a[16:19], a[0:3], v[20:23]\n"
    "ds_read_b128 a[48:51], v111 offset:8192\n"
    "v_cndmask_b32 v168, v168, v228, s[96:97]\n"
    "v_mfma_f32_16x16x32_bf16 v[40:43], a[20:23], a[0:3], v[40:43]\n"
    "ds_read_b128 a[32:35], v106 offset:0\n"
    "v_cndmask_b32 v169, v169, v228, s[96:97]\n"
    "v_mfma_f32_16x16x32_bf16 v[60:63], a[0:3], a[24:27], v[60:63]\n"
    "ds_read_b128 a[52:55], v111 offset:9216\n"
    "v_cndmask_b32 v170, v170, v228, s[96:97]\n"
    "v_mfma_f32_16x16x32_bf16 v[80:83], a[28:31], a[0:3], v[80:83]\n"
    "ds_read_b128 a[56:59], v111 offset:10240\n"
    "buffer_load_dwordx4 a[140:143], v1, s[4:7], s24 offen offset:3072 sc1\n"
    "v_mfma_f32_16x16x32_bf16 v[24:27], a[16:19], a[4:7], v[24:27]\n"
    "ds_read_b128 a[60:63], v111 offset:11264\n"
    "s_add_u32 s24, s22, 4096\n"
    "v_mfma_f32_16x16x32_bf16 v[44:47], a[20:23], a[4:7], v[44:47]\n"
    "s_add_u32 s58, s58, 4096\n"
    "buffer_store_dwordx4 v[236:239], v1, s[4:7], s58 offen offset:0 sc1\n"
    "v_mfma_f32_16x16x32_bf16 v[64:67], a[4:7], a[24:27], v[64:67]\n"
    "ds_read_b128 a[36:39], v106 offset:2048\n"
    "v_cndmask_b32 v171, v171, v228, s[96:97]\n"
    "v_mfma_f32_16x16x32_bf16 v[84:87], a[28:31], a[4:7], v[84:87]\n"
    "ds_read_b128 a[40:43], v106 offset:4096\n"
    "v_mov_b32 v9, 4294901760\n"
    "v_mfma_f32_16x16x32_bf16 v[28:31], a[16:19], a[8:11], v[28:31]\n"
    "ds_read_b128 a[44:47], v106 offset:6144\n"
    "v_cndmask_b32 v223, 0, v7, s[54:55]\n"
    "v_mfma_f32_16x16x32_bf16 v[48:51], a[20:23], a[8:11], v[48:51]\n"
    "ds_read_b128 v[172:175], v110 offset:0\n"
    "buffer_load_dwordx4 a[144:147], v1, s[4:7], s24 offen offset:0 sc1\n"
    "v_mfma_f32_16x16x32_bf16 v[68:71], a[8:11], a[24:27], v[68:71]\n"
    "ds_read_b128 a[72:75], v176 offset:9216\n"
    "ds_read_b128 a[76:79], v177 offset:10240\n"
    "v_mfma_f32_16x16x32_bf16 v[88:91], a[28:31], a[8:11], v[88:91]\n"
    "v_cmp_ge_i32 s[54:55], -6, v11\n"
    "v_cmp_ge_i32 s[50:51], 2, v252\n"
    "v_mfma_f32_16x16x32_bf16 v[32:35], a[16:19], a[12:15], v[32:35]\n"
    "v_cndmask_b32 v219, v7, 0, s[54:55]\n"
    "v_cndmask_b32 v9, 0, v9, s[54:55]\n"
    "v_mfma_f32_16x16x32_bf16 v[52:55], a[20:23], a[12:15], v[52:55]\n"
    "buffer_load_dwordx4 a[148:151], v1, s[4:7], s24 offen offset:1024 sc1\n"
    "s_and_b64 s[50:51], s[50:51], s[52:53]\n"
    "v_mfma_f32_16x16x32_bf16 v[72:75], a[12:15], a[24:27], v[72:75]\n"
    "v_or_b32_e64 v233, v233, v9\n"
    "v_mov_b32 v9, 4294901760\n"
    "v_mfma_f32_16x16x32_bf16 v[92:95], a[28:31], a[12:15], v[92:95]\n"
    "v_cndmask_b32 v227, v7, 0, s[50:51]\n"
    "v_cndmask_b32 v9, 0, v9, s[50:51]\n"
    "v_mfma_f32_16x16x32_bf16 v[36:39], a[64:67], v[168:171], v[36:39]\n"
    "v_or_b32_e64 v179, v179, v9\n"
    "v_mfma_f32_16x16x32_bf16 v[56:59], v[168:171], a[68:71], v[56:59]\n"
    "buffer_load_dwordx4 a[152:155], v1, s[4:7], s24 offen offset:2048 sc1\n"
    "s_waitcnt lgkmcnt(0)\n"
    "v_mfma_f32_16x16x32_bf16 v[20:23], a[48:51], a[32:35], v[20:23]\n"
    "s_waitcnt vmcnt(11)\n"
    "s_barrier\n"
    "v_mfma_f32_16x16x32_bf16 v[40:43], a[52:55], a[32:35], v[40:43]\n"
    "ds_read_b128 a[16:19], v111 offset:16384\n"
    "v_cndmask_b32 v172, v172, v228, s[96:97]\n"
    "v_mfma_f32_16x16x32_bf16 v[60:63], a[32:35], a[56:59], v[60:63]\n"
    "ds_read_b128 a[0:3], v105 offset:18432\n"
    "v_cndmask_b32 v173, v173, v228, s[96:97]\n"
    "v_mfma_f32_16x16x32_bf16 v[80:83], a[60:63], a[32:35], v[80:83]\n"
    "ds_read_b128 a[20:23], v111 offset:17408\n"
    "buffer_load_dwordx4 a[156:159], v1, s[4:7], s24 offen offset:3072 sc1\n"
    "v_mfma_f32_16x16x32_bf16 v[24:27], a[48:51], a[36:39], v[24:27]\n"
    "ds_read_b128 a[24:27], v111 offset:18432\n"
    "s_add_u32 s24, s22, 8192\n"
    "v_mfma_f32_16x16x32_bf16 v[44:47], a[52:55], a[36:39], v[44:47]\n"
    "buffer_store_dwordx4 v[236:239], v1, s[4:7], s58 offen offset:1024 sc1\n"
    "v_mfma_f32_16x16x32_bf16 v[64:67], a[36:39], a[56:59], v[64:67]\n"
    "ds_read_b128 a[28:31], v111 offset:19456\n"
    "v_cndmask_b32 v174, v174, v228, s[96:97]\n"
    "v_mfma_f32_16x16x32_bf16 v[84:87], a[60:63], a[36:39], v[84:87]\n"
    "ds_read_b128 a[4:7], v105 offset:20480\n"
    "v_cndmask_b32 v175, v175, v228, s[96:97]\n"
    "v_mfma_f32_16x16x32_bf16 v[28:31], a[48:51], a[40:43], v[28:31]\n"
    "ds_read_b128 a[8:11], v105 offset:22528\n"
    "v_mfma_f32_16x16x32_bf16 v[48:51], a[52:55], a[40:43], v[48:51]\n"
    "ds_read_b128 a[12:15], v105 offset:24576\n"
    "buffer_load_dwordx4 a[160:163], v1, s[4:7], s24 offen offset:0 sc1\n"
    "v_mfma_f32_16x16x32_bf16 v[68:71], a[40:43], a[56:59], v[68:71]\n"
    "ds_read_b128 v[168:171], v109 offset:18432\n"
    "ds_read_b128 a[64:67], v176 offset:17408\n"
    "v_mfma_f32_16x16x32_bf16 v[88:91], a[60:63], a[40:43], v[88:91]\n"
    "ds_read_b128 a[68:71], v177 offset:18432\n"
    "v_mfma_f32_16x16x32_bf16 v[32:35], a[48:51], a[44:47], v[32:35]\n"
    "v_mfma_f32_16x16x32_bf16 v[52:55], a[52:55], a[44:47], v[52:55]\n"
    "buffer_load_dwordx4 a[164:167], v1, s[4:7], s24 offen offset:1024 sc1\n"
    "v_mfma_f32_16x16x32_bf16 v[72:75], a[44:47], a[56:59], v[72:75]\n"
    "v_mfma_f32_16x16x32_bf16 v[92:95], a[60:63], a[44:47], v[92:95]\n"
    "v_mfma_f32_16x16x32_bf16 v[36:39], a[72:75], v[172:175], v[36:39]\n"
    "v_mfma_f32_16x16x32_bf16 v[56:59], v[172:175], a[76:79], v[56:59]\n"
    "buffer_load_dwordx4 a[168:171], v1, s[4:7], s24 offen offset:2048 sc1\n"
    "s_waitcnt lgkmcnt(0)\n"
    "v_mfma_f32_16x16x32_bf16 v[20:23], a[16:19], a[0:3], v[20:23]\n"
    "ds_read_b128 a[48:51], v111 offset:24576\n"
    "v_cndmask_b32 v168, v168, v228, s[96:97]\n"
    "v_mfma_f32_16x16x32_bf16 v[40:43], a[20:23], a[0:3], v[40:43]\n"
    "ds_read_b128 a[32:35], v106 offset:18432\n"
    "v_cndmask_b32 v169, v169, v228, s[96:97]\n"
    "v_mfma_f32_16x16x32_bf16 v[60:63], a[0:3], a[24:27], v[60:63]\n"
    "ds_read_b128 a[52:55], v111 offset:25600\n"
    "v_cndmask_b32 v170, v170, v228, s[96:97]\n"
    "v_mfma_f32_16x16x32_bf16 v[80:83], a[28:31], a[0:3], v[80:83]\n"
    "ds_read_b128 a[56:59], v111 offset:26624\n"
    "buffer_load_dwordx4 a[172:175], v1, s[4:7], s24 offen offset:3072 sc1\n"
    "v_mfma_f32_16x16x32_bf16 v[24:27], a[16:19], a[4:7], v[24:27]\n"
    "ds_read_b128 a[60:63], v111 offset:27648\n"
    "s_add_u32 s24, s22, 12288\n"
    "v_mfma_f32_16x16x32_bf16 v[44:47], a[20:23], a[4:7], v[44:47]\n"
    "buffer_store_dwordx4 v[236:239], v235, s[4:7], s58 offen offset:2048 sc1\n"
    "v_mfma_f32_16x16x32_bf16 v[64:67], a[4:7], a[24:27], v[64:67]\n"
    "ds_read_b128 a[36:39], v106 offset:20480\n"
    "v_cndmask_b32 v171, v171, v228, s[96:97]\n"
    "v_mfma_f32_16x16x32_bf16 v[84:87], a[28:31], a[4:7], v[84:87]\n"
    "ds_read_b128 a[40:43], v106 offset:22528\n"
    "v_mfma_f32_16x16x32_bf16 v[28:31], a[16:19], a[8:11], v[28:31]\n"
    "ds_read_b128 a[44:47], v106 offset:24576\n"
    "v_mfma_f32_16x16x32_bf16 v[48:51], a[20:23], a[8:11], v[48:51]\n"
    "ds_read_b128 v[172:175], v110 offset:18432\n"
    "buffer_load_dwordx4 a[176:179], v1, s[4:7], s24 offen offset:0 sc1\n"
    "v_mfma_f32_16x16x32_bf16 v[68:71], a[8:11], a[24:27], v[68:71]\n"
    "ds_read_b128 a[72:75], v176 offset:25600\n"
    "ds_read_b128 a[76:79], v177 offset:26624\n"
    "v_mfma_f32_16x16x32_bf16 v[88:91], a[28:31], a[8:11], v[88:91]\n"
    "v_mfma_f32_16x16x32_bf16 v[32:35], a[16:19], a[12:15], v[32:35]\n"
    "v_mfma_f32_16x16x32_bf16 v[52:55], a[20:23], a[12:15], v[52:55]\n"
    "buffer_load_dwordx4 a[180:183], v1, s[4:7], s24 offen offset:1024 sc1\n"
    "v_mfma_f32_16x16x32_bf16 v[72:75], a[12:15], a[24:27], v[72:75]\n"
    "v_mfma_f32_16x16x32_bf16 v[92:95], a[28:31], a[12:15], v[92:95]\n"
    "v_mfma_f32_16x16x32_bf16 v[36:39], a[64:67], v[168:171], v[36:39]\n"
    "v_mfma_f32_16x16x32_bf16 v[56:59], v[168:171], a[68:71], v[56:59]\n"
    "buffer_load_dwordx4 a[184:187], v1, s[4:7], s24 offen offset:2048 sc1\n"
    "s_waitcnt lgkmcnt(0)\n"
    "v_mfma_f32_16x16x32_bf16 v[20:23], a[48:51], a[32:35], v[20:23]\n"
    "v_cndmask_b32 v172, v172, v228, s[96:97]\n"
    "v_cndmask_b32 v173, v173, v228, s[96:97]\n"
    "v_cndmask_b32 v174, v174, v228, s[96:97]\n"
    "v_cndmask_b32 v175, v175, v228, s[96:97]\n"
    "v_mfma_f32_16x16x32_bf16 v[40:43], a[52:55], a[32:35], v[40:43]\n"
    "v_mfma_f32_16x16x32_bf16 v[60:63], a[32:35], a[56:59], v[60:63]\n"
    "v_mfma_f32_16x16x32_bf16 v[36:39], a[72:75], v[172:175], v[36:39]\n"
    "buffer_load_dwordx4 a[188:191], v1, s[4:7], s24 offen offset:3072 sc1\n"
    "s_add_u32 s24, s22, 16384\n"
    "v_mfma_f32_16x16x32_bf16 v[56:59], v[172:175], a[76:79], v[56:59]\n"
    "v_mfma_f32_16x16x32_bf16 v[80:83], a[60:63], a[32:35], v[80:83]\n"
    "v_mfma_f32_16x16x32_bf16 v[24:27], a[48:51], a[36:39], v[24:27]\n"
    "v_mfma_f32_16x16x32_bf16 v[44:47], a[52:55], a[36:39], v[44:47]\n"
    "buffer_load_dwordx4 a[192:195], v1, s[4:7], s24 offen offset:0 sc1\n"
    "v_mfma_f32_16x16x32_bf16 v[64:67], a[36:39], a[56:59], v[64:67]\n"
    "ds_write_b128 v180, v[36:39]\n"
    "s_nop 1\n"
    "ds_write_b128 v180, v[56:59] offset:1024\n"
    "v_mfma_f32_16x16x32_bf16 v[84:87], a[60:63], a[36:39], v[84:87]\n"
    "ds_write_b32 v182, v56 offset:2048\n"
    "s_waitcnt lgkmcnt(0)\n"
    "v_mfma_f32_16x16x32_bf16 v[28:31], a[48:51], a[40:43], v[28:31]\n"
    "s_waitcnt vmcnt(21)\n"
    "s_barrier\n"
    "v_mfma_f32_16x16x32_bf16 v[48:51], a[52:55], a[40:43], v[48:51]\n"
    "ds_read_b128 v[182:185], v183 offset:6656\n"
    "buffer_load_dwordx4 a[196:199], v1, s[4:7], s24 offen offset:1024 sc1\n"
    "v_mfma_f32_16x16x32_bf16 v[68:71], a[40:43], a[56:59], v[68:71]\n"
    "ds_read_b128 v[36:39], v181\n"
    "ds_read_b128 v[76:79], v181 offset:4608\n"
    "v_mfma_f32_16x16x32_bf16 v[88:91], a[60:63], a[40:43], v[88:91]\n"
    "ds_read_b128 v[56:59], v181 offset:1024\n"
    "ds_read_b64 v[130:131], v117 offset:0\n"
    "v_mfma_f32_16x16x32_bf16 v[32:35], a[48:51], a[44:47], v[32:35]\n"
    "ds_read_b64 v[128:129], v118 offset:0\n"
    "ds_read_b64 v[132:133], v117 offset:256\n"
    "v_mfma_f32_16x16x32_bf16 v[52:55], a[52:55], a[44:47], v[52:55]\n"
    "ds_read_b64 v[134:135], v117 offset:512\n"
    "buffer_load_dwordx4 a[200:203], v1, s[4:7], s24 offen offset:2048 sc1\n"
    "v_mfma_f32_16x16x32_bf16 v[72:75], a[44:47], a[56:59], v[72:75]\n"
    "ds_read_b64 v[136:137], v117 offset:768\n"
    "ds_read_b128 v[138:141], v120 offset:0\n"
    "v_mfma_f32_16x16x32_bf16 v[92:95], a[60:63], a[44:47], v[92:95]\n"
    "ds_read_b128 v[158:161], v120 offset:64\n"
    "ds_read_b128 v[142:145], v119 offset:0\n"
    "ds_read_b128 v[162:165], v119 offset:64\n"
    "ds_read_b128 v[146:149], v119 offset:256\n"
    "ds_read_b128 v[166:169], v119 offset:320\n"
    "ds_read_b128 v[150:153], v119 offset:512\n"
    "ds_read_b128 v[170:173], v119 offset:576\n"
    "ds_read_b128 v[154:157], v119 offset:768\n"
    "ds_read_b128 v[174:177], v119 offset:832\n"
    "Lcatt_%=:\n"
    "v_cmp_eq_u32 s[60:61], 1, v10\n"
    "v_cmp_eq_u32 s[62:63], 2, v10\n"
    "v_cmp_eq_u32 s[64:65], 3, v10\n"
    "s_mov_b32 s48, 0x3ea52b5c\n"
    "s_mov_b32 s49, 0x3ea52b5c\n"
    "s_lshr_b32 s50, s36, 1\n"
    "s_lshl1_add_u32 s50, s37, s50\n"
    "s_mul_i32 s50, s50, 81920\n"
    "s_and_b32 s51, s36, 1\n"
    "s_lshl_b32 s51, s51, 9\n"
    "s_add_u32 s50, s50, s51\n"
    "s_add_u32 s50, s50, 8208384\n"
    "s_lshl_b32 s51, s38, 10\n"
    "s_add_u32 s51, s51, s50\n"
    "s_add_u32 s52, s51, 40960\n"
    "s_lshr_b32 s53, s38, 2\n"
    "s_lshl_b32 s53, s53, 10\n"
    "s_add_u32 s50, s50, s53\n"
    "s_and_b32 s53, s38, 3\n"
    "s_lshl_b32 s53, s53, 7\n"
    "s_add_u32 s50, s50, s53\n"
    "s_add_u32 s50, s50, 16384\n"
    "s_waitcnt lgkmcnt(15)\n"
    "v_mov_b32 v190, v56\n"
    "buffer_load_dwordx4 a[204:207], v1, s[4:7], s24 offen offset:3072 sc1\n"
    "v_mov_b32_e64 v191, v56\n"
    "v_mov_b32_dpp v186, v36 row_newbcast:0 row_mask:0xf bank_mask:0xf\n"
    "v_mov_b32_dpp v187, v37 row_newbcast:0 row_mask:0xf bank_mask:0xf\n"
    "v_permlane16_swap_b32 v190, v191\n"
    "v_mov_b32 v191, v190\n"
    "v_mov_b32_dpp v188, v38 row_newbcast:0 row_mask:0xf bank_mask:0xf\n"
    "v_mov_b32_dpp v189, v39 row_newbcast:0 row_mask:0xf bank_mask:0xf\n"
    "s_waitcnt lgkmcnt(13)\n"
    "v_pk_mul_f32 v[6:7], v[186:187], v[128:129] op_sel:[0,1] op_sel_hi:[1,1]\n"
    "v_pk_mul_f32 v[12:13], v[188:189], v[128:129] op_sel:[0,1] op_sel_hi:[1,1]\n"
    "v_mov_b32_dpp v192, v76 row_newbcast:0 row_mask:0xf bank_mask:0xf\n"
    "v_mov_b32_dpp v193, v77 row_newbcast:0 row_mask:0xf bank_mask:0xf\n"
    "v_mov_b32_dpp v194, v78 row_newbcast:0 row_mask:0xf bank_mask:0xf\n"
    "v_mov_b32_dpp v195, v79 row_newbcast:0 row_mask:0xf bank_mask:0xf\n"
    "v_permlane32_swap_b32 v190, v191\n"
    "s_add_u32 s24, s23, 0\n"
    "buffer_load_dwordx4 a[208:211], v1, s[4:7], s24 offen offset:0 sc1\n"
    "v_pk_fma_f32 v[36:37], v[36:37], v[128:129], v[6:7] op_sel_hi:[1,0,1]\n"
    "v_pk_fma_f32 v[38:39], v[38:39], v[128:129], v[12:13] op_sel_hi:[1,0,1]\n"
    "v_pk_mul_f32 v[14:15], v[192:193], v[128:129] op_sel:[0,1] op_sel_hi:[1,1]\n"
    "v_pk_mul_f32 v[4:5], v[194:195], v[128:129] op_sel:[0,1] op_sel_hi:[1,1]\n"
    "s_waitcnt lgkmcnt(8)\n"
    "v_pk_mul_f32 v[6:7], v[158:159], v[190:191] op_sel_hi:[1,0]\n"
    "v_pk_mul_f32 v[12:13], v[160:161], v[190:191] op_sel_hi:[1,0]\n"
    "v_pk_fma_f32 v[76:77], v[76:77], v[128:129], v[14:15] op_sel_hi:[1,0,1]\n"
    "v_pk_fma_f32 v[78:79], v[78:79], v[128:129], v[4:5] op_sel_hi:[1,0,1]\n"
    "v_pk_fma_f32 v[56:57], v[56:57], v[138:139], v[6:7]\n"
    "v_pk_fma_f32 v[58:59], v[58:59], v[140:141], v[12:13]\n"
    "v_pk_mul_f32 v[14:15], v[182:183], v[130:131] op_sel:[0,1] op_sel_hi:[1,1]\n"
    "v_pk_mul_f32 v[4:5], v[184:185], v[130:131] op_sel:[0,1] op_sel_hi:[1,1]\n"
    "v_pk_mul_f32 v[6:7], v[186:187], v[130:131] op_sel:[0,1] op_sel_hi:[1,1]\n"
    "v_pk_mul_f32 v[12:13], v[188:189], v[130:131] op_sel:[0,1] op_sel_hi:[1,1]\n"
    "v_pk_fma_f32 v[20:21], v[20:21], v[130:131], v[14:15] op_sel_hi:[1,0,1]\n"
    "v_pk_fma_f32 v[22:23], v[22:23], v[130:131], v[4:5] op_sel_hi:[1,0,1]\n"
    "buffer_load_dwordx4 a[212:215], v1, s[4:7], s24 offen offset:1024 sc1\n"
    "v_pk_fma_f32 v[40:41], v[40:41], v[130:131], v[6:7] op_sel_hi:[1,0,1]\n"
    "v_pk_fma_f32 v[42:43], v[42:43], v[130:131], v[12:13] op_sel_hi:[1,0,1]\n"
    "v_pk_mul_f32 v[14:15], v[192:193], v[130:131] op_sel:[0,1] op_sel_hi:[1,1]\n"
    "v_pk_mul_f32 v[4:5], v[194:195], v[130:131] op_sel:[0,1] op_sel_hi:[1,1]\n"
    "s_waitcnt lgkmcnt(6)\n"
    "v_pk_mul_f32 v[6:7], v[162:163], v[190:191] op_sel_hi:[1,0]\n"
    "v_pk_mul_f32 v[12:13], v[164:165], v[190:191] op_sel_hi:[1,0]\n"
    "v_pk_fma_f32 v[80:81], v[80:81], v[130:131], v[14:15] op_sel_hi:[1,0,1]\n"
    "v_pk_fma_f32 v[82:83], v[82:83], v[130:131], v[4:5] op_sel_hi:[1,0,1]\n"
    "v_pk_fma_f32 v[60:61], v[60:61], v[142:143], v[6:7]\n"
    "v_pk_fma_f32 v[62:63], v[62:63], v[144:145], v[12:13]\n"
    "v_pk_mul_f32 v[14:15], v[182:183], v[132:133] op_sel:[0,1] op_sel_hi:[1,1]\n"
    "v_pk_mul_f32 v[4:5], v[184:185], v[132:133] op_sel:[0,1] op_sel_hi:[1,1]\n"
    "v_pk_mul_f32 v[6:7], v[186:187], v[132:133] op_sel:[0,1] op_sel_hi:[1,1]\n"
    "v_pk_mul_f32 v[12:13], v[188:189], v[132:133] op_sel:[0,1] op_sel_hi:[1,1]\n"
    "v_pk_fma_f32 v[24:25], v[24:25], v[132:133], v[14:15] op_sel_hi:[1,0,1]\n"
    "v_pk_fma_f32 v[26:27], v[26:27], v[132:133], v[4:5] op_sel_hi:[1,0,1]\n"
    "buffer_load_dwordx4 a[216:219], v1, s[4:7], s24 offen offset:2048 sc1\n"
    "v_pk_fma_f32 v[44:45], v[44:45], v[132:133], v[6:7] op_sel_hi:[1,0,1]\n"
    "v_pk_fma_f32 v[46:47], v[46:47], v[132:133], v[12:13] op_sel_hi:[1,0,1]\n"
    "v_pk_mul_f32 v[14:15], v[192:193], v[132:133] op_sel:[0,1] op_sel_hi:[1,1]\n"
    "v_pk_mul_f32 v[4:5], v[194:195], v[132:133] op_sel:[0,1] op_sel_hi:[1,1]\n"
    "s_waitcnt lgkmcnt(4)\n"
    "v_pk_mul_f32 v[6:7], v[166:167], v[190:191] op_sel_hi:[1,0]\n"
    "v_pk_mul_f32 v[12:13], v[168:169], v[190:191] op_sel_hi:[1,0]\n"
    "v_pk_fma_f32 v[84:85], v[84:85], v[132:133], v[14:15] op_sel_hi:[1,0,1]\n"
    "v_pk_fma_f32 v[86:87], v[86:87], v[132:133], v[4:5] op_sel_hi:[1,0,1]\n"
    "v_pk_fma_f32 v[64:65], v[64:65], v[146:147], v[6:7]\n"
    "v_pk_fma_f32 v[66:67], v[66:67], v[148:149], v[12:13]\n"
    "v_pk_mul_f32 v[14:15], v[182:183], v[134:135] op_sel:[0,1] op_sel_hi:[1,1]\n"
    "v_pk_mul_f32 v[4:5], v[184:185], v[134:135] op_sel:[0,1] op_sel_hi:[1,1]\n"
    "v_pk_mul_f32 v[6:7], v[186:187], v[134:135] op_sel:[0,1] op_sel_hi:[1,1]\n"
    "v_pk_mul_f32 v[12:13], v[188:189], v[134:135] op_sel:[0,1] op_sel_hi:[1,1]\n"
    "v_pk_fma_f32 v[28:29], v[28:29], v[134:135], v[14:15] op_sel_hi:[1,0,1]\n"
    "v_pk_fma_f32 v[30:31], v[30:31], v[134:135], v[4:5] op_sel_hi:[1,0,1]\n"
    "buffer_load_dwordx4 a[220:223], v1, s[4:7], s24 offen offset:3072 sc1\n"
    "v_pk_fma_f32 v[48:49], v[48:49], v[134:135], v[6:7] op_sel_hi:[1,0,1]\n"
    "v_pk_fma_f32 v[50:51], v[50:51], v[134:135], v[12:13] op_sel_hi:[1,0,1]\n"
    "v_pk_mul_f32 v[14:15], v[192:193], v[134:135] op_sel:[0,1] op_sel_hi:[1,1]\n"
    "v_pk_mul_f32 v[4:5], v[194:195], v[134:135] op_sel:[0,1] op_sel_hi:[1,1]\n"
    "s_waitcnt lgkmcnt(2)\n"
    "v_pk_mul_f32 v[6:7], v[170:171], v[190:191] op_sel_hi:[1,0]\n"
    "v_pk_mul_f32 v[12:13], v[172:173], v[190:191] op_sel_hi:[1,0]\n"
    "v_pk_fma_f32 v[88:89], v[88:89], v[134:135], v[14:15] op_sel_hi:[1,0,1]\n"
    "v_pk_fma_f32 v[90:91], v[90:91], v[134:135], v[4:5] op_sel_hi:[1,0,1]\n"
    "v_pk_fma_f32 v[68:69], v[68:69], v[150:151], v[6:7]\n"
    "v_pk_fma_f32 v[70:71], v[70:71], v[152:153], v[12:13]\n"
    "v_pk_mul_f32 v[14:15], v[182:183], v[136:137] op_sel:[0,1] op_sel_hi:[1,1]\n"
    "v_pk_mul_f32 v[4:5], v[184:185], v[136:137] op_sel:[0,1] op_sel_hi:[1,1]\n"
    "v_pk_mul_f32 v[6:7], v[186:187], v[136:137] op_sel:[0,1] op_sel_hi:[1,1]\n"
    "v_pk_mul_f32 v[12:13], v[188:189], v[136:137] op_sel:[0,1] op_sel_hi:[1,1]\n"
    "v_pk_fma_f32 v[32:33], v[32:33], v[136:137], v[14:15] op_sel_hi:[1,0,1]\n"
    "v_pk_fma_f32 v[34:35], v[34:35], v[136:137], v[4:5] op_sel_hi:[1,0,1]\n"
    "s_add_u32 s24, s23, 4096\n"
    "buffer_load_dwordx4 a[224:227], v1, s[4:7], s24 offen offset:0 sc1\n"
    "v_pk_fma_f32 v[52:53], v[52:53], v[136:137], v[6:7] op_sel_hi:[1,0,1]\n"
    "v_pk_fma_f32 v[54:55], v[54:55], v[136:137], v[12:13] op_sel_hi:[1,0,1]\n"
    "v_cvt_pk_bf16_f32 v148, v76, v77\n"
    "v_cvt_pk_bf16_f32 v149, v78, v79\n"
    "v_pk_mul_f32 v[14:15], v[192:193], v[136:137] op_sel:[0,1] op_sel_hi:[1,1]\n"
    "v_pk_mul_f32 v[4:5], v[194:195], v[136:137] op_sel:[0,1] op_sel_hi:[1,1]\n"
    "s_waitcnt lgkmcnt(0)\n"
    "v_pk_mul_f32 v[6:7], v[174:175], v[190:191] op_sel_hi:[1,0]\n"
    "v_pk_mul_f32 v[12:13], v[176:177], v[190:191] op_sel_hi:[1,0]\n"
    "ds_write_b64 v115, v[148:149] offset:0\n"
    "v_cvt_pk_bf16_f32 v150, v80, v81\n"
    "v_cvt_pk_bf16_f32 v151, v82, v83\n"
    "v_pk_fma_f32 v[92:93], v[92:93], v[136:137], v[14:15] op_sel_hi:[1,0,1]\n"
    "v_pk_fma_f32 v[94:95], v[94:95], v[136:137], v[4:5] op_sel_hi:[1,0,1]\n"
    "v_pk_fma_f32 v[72:73], v[72:73], v[154:155], v[6:7]\n"
    "v_pk_fma_f32 v[74:75], v[74:75], v[156:157], v[12:13]\n"
    "ds_write_b64 v115, v[150:151] offset:128\n"
    "s_add_u32 m0, s20, 0\n"
    "s_add_u32 s21, s19, 0\n"
    "buffer_load_dwordx4 v1, s[4:7], s21 offen offset:0 sc1 lds\n"
    "v_cvt_pk_bf16_f32 v152, v84, v85\n"
    "v_cvt_pk_bf16_f32 v153, v86, v87\n"
    "ds_write_b64 v115, v[152:153] offset:256\n"
    "v_cvt_pk_bf16_f32 v154, v88, v89\n"
    "v_cvt_pk_bf16_f32 v155, v90, v91\n"
    "v_cvt_pk_bf16_f32 v156, v92, v93\n"
    "v_cvt_pk_bf16_f32 v157, v94, v95\n"
    "v_cvt_pk_bf16_f32 v98, v80, v81\n"
    "v_cvt_pk_bf16_f32 v99, v82, v83\n"
    "v_cvt_pk_bf16_f32 v102, v84, v85\n"
    "v_cvt_pk_bf16_f32 v103, v86, v87\n"
    "v_cvt_pk_bf16_f32 v106, v88, v89\n"
    "v_cvt_pk_bf16_f32 v107, v90, v91\n"
    "v_cvt_pk_bf16_f32 v110, v92, v93\n"
    "v_cvt_pk_bf16_f32 v111, v94, v95\n"
    "ds_write_b64 v115, v[154:155] offset:384\n"
    "buffer_load_dwordx4 v1, s[4:7], s21 offen offset:1024 sc1 lds\n"
    "ds_write_b64 v115, v[156:157] offset:512\n"
    "v_cndmask_b32 v202, 0, v150, s[60:61]\n"
    "v_cndmask_b32 v203, 0, v151, s[60:61]\n"
    "v_cndmask_b32 v206, 0, v152, s[60:61]\n"
    "v_cndmask_b32 v207, 0, v153, s[60:61]\n"
    "v_cndmask_b32 v210, 0, v154, s[60:61]\n"
    "v_cndmask_b32 v211, 0, v155, s[60:61]\n"
    "v_cndmask_b32 v214, 0, v156, s[60:61]\n"
    "v_cndmask_b32 v215, 0, v157, s[60:61]\n"
    "v_cvt_pk_bf16_f32 v196, v36, v37\n"
    "v_cvt_pk_bf16_f32 v197, v38, v39\n"
    "v_cndmask_b32 v198, 0, v148, s[60:61]\n"
    "v_cndmask_b32 v199, 0, v149, s[60:61]\n"
    "v_cvt_pk_bf16_f32 v200, v40, v41\n"
    "v_cvt_pk_bf16_f32 v201, v42, v43\n"
    "v_cvt_pk_bf16_f32 v96, v20, v21\n"
    "buffer_load_dwordx4 v1, s[4:7], s21 offen offset:2048 sc1 lds\n"
    "v_cvt_pk_bf16_f32 v97, v22, v23\n"
    "v_permlane16_swap_b32 v98, v150\n"
    "v_permlane16_swap_b32 v99, v151\n"
    "v_cvt_pk_bf16_f32 v204, v44, v45\n"
    "v_cvt_pk_bf16_f32 v205, v46, v47\n"
    "v_cvt_pk_bf16_f32 v100, v24, v25\n"
    "v_cvt_pk_bf16_f32 v101, v26, v27\n"
    "v_permlane16_swap_b32 v102, v152\n"
    "v_permlane16_swap_b32 v103, v153\n"
    "v_cvt_pk_bf16_f32 v208, v48, v49\n"
    "v_cvt_pk_bf16_f32 v209, v50, v51\n"
    "v_cvt_pk_bf16_f32 v104, v28, v29\n"
    "buffer_load_dwordx4 v1, s[4:7], s21 offen offset:3072 sc1 lds\n"
    "v_cvt_pk_bf16_f32 v105, v30, v31\n"
    "v_permlane16_swap_b32 v106, v154\n"
    "v_permlane16_swap_b32 v107, v155\n"
    "v_cvt_pk_bf16_f32 v212, v52, v53\n"
    "v_cvt_pk_bf16_f32 v213, v54, v55\n"
    "v_cvt_pk_bf16_f32 v108, v32, v33\n"
    "v_cvt_pk_bf16_f32 v109, v34, v35\n"
    "v_permlane16_swap_b32 v110, v156\n"
    "v_permlane16_swap_b32 v111, v157\n"
    "v_mfma_f32_16x16x32_bf16 v[16:19], v[196:199], v[96:99], v[224:227]\n"
    "s_add_u32 m0, s20, 4096\n"
    "s_add_u32 s21, s19, 4096\n"
    "buffer_load_dwordx4 v1, s[4:7], s21 offen offset:0 sc1 lds\n"
    "ds_read_b64_tr_b16 v[138:139], v116 offset:0\n"
    "ds_read_b64_tr_b16 v[140:141], v116 offset:128\n"
    "v_mfma_f32_16x16x32_bf16 v[20:23], v[200:203], v[96:99], v[220:223]\n"
    "ds_read_b64_tr_b16 v[142:143], v116 offset:256\n"
    "ds_read_b64_tr_b16 v[144:145], v116 offset:384\n"
    "v_mfma_f32_16x16x32_bf16 v[24:27], v[200:203], v[100:103], v[216:219]\n"
    "ds_read_b64_tr_b16 v[146:147], v116 offset:512\n"
    "v_cvt_pk_bf16_f32 v128, v56, v57\n"
    "v_mfma_f32_16x16x32_bf16 v[28:31], v[204:207], v[100:103], v[220:223]\n"
    "buffer_load_dwordx4 v1, s[4:7], s21 offen offset:1024 sc1 lds\n"
    "s_nop 0\n"
    "v_max_f32 v16, v16, v20\n"
    "v_max_f32_e64 v17, v17, v21\n"
    "v_mfma_f32_16x16x32_bf16 v[32:35], v[204:207], v[104:107], v[216:219]\n"
    "v_max_f32 v18, v18, v22\n"
    "v_max_f32 v19, v19, v23\n"
    "v_mfma_f32_16x16x32_bf16 v[36:39], v[208:211], v[104:107], v[220:223]\n"
    "v_max_f32 v24, v24, v28\n"
    "v_max_f32 v25, v25, v29\n"
    "v_mfma_f32_16x16x32_bf16 v[40:43], v[208:211], v[108:111], v[216:219]\n"
    "s_add_u32 m0, s20, 6144\n"
    "s_add_u32 s21, s26, 6144\n"
    "buffer_load_dwordx4 v1, s[4:7], s21 offen offset:0 sc1 lds\n"
    "v_max_f32_e64 v26, v26, v30\n"
    "v_max3_f32 v244, v16, v17, v18\n"
    "v_mfma_f32_16x16x32_bf16 v[44:47], v[212:215], v[108:111], v[220:223]\n"
    "v_max_f32 v32, v32, v36\n"
    "v_max_f32 v33, v33, v37\n"
    "v_max_f32 v34, v34, v38\n"
    "v_max_f32 v27, v27, v31\n"
    "v_max3_f32 v246, v24, v25, v26\n"
    "v_max_f32_e64 v35, v35, v39\n"
    "v_max3_f32 v248, v32, v33, v34\n"
    "s_nop 0\n"
    "v_max_f32 v40, v40, v44\n"
    "v_max_f32 v41, v41, v45\n"
    "v_max_f32 v42, v42, v46\n"
    "s_add_u32 m0, s20, 28672\n"
    "s_add_u32 s21, s19, 28672\n"
    "buffer_load_dwordx4 v1, s[4:7], s21 offen offset:0 sc1 lds\n"
    "v_max_f32_e64 v43, v43, v47\n"
    "v_max3_f32 v250, v40, v41, v42\n"
    "v_max_f32 v244, v244, v19\n"
    "v_max_f32 v246, v246, v27\n"
    "v_max_f32 v248, v248, v35\n"
    "v_max_f32 v250, v250, v43\n"
    "v_permlane32_swap_b32 v244, v246\n"
    "s_nop 0\n"
    "v_permlane32_swap_b32 v248, v250\n"
    "v_max_f32 v244, v244, v246\n"
    "v_max_f32_e64 v248, v248, v250\n"
    "v_cvt_pk_bf16_f32 v129, v58, v59\n"
    "v_cvt_pk_bf16_f32 v130, v60, v61\n"
    "v_permlane16_swap_b32_e64 v244, v248\n"
    "buffer_load_dwordx4 v1, s[4:7], s21 offen offset:1024 sc1 lds\n"
    "v_max_f32_e64 v244, v244, v248\n"
    "global_load_dwordx2 v[252:253], v122, s[4:5] sc1\n"
    "v_mul_f32 v244, s48, v244\n"
    "v_mov_b32 v248, v244\n"
    "v_cvt_pk_bf16_f32 v131, v62, v63\n"
    "v_cvt_pk_bf16_f32 v132, v64, v65\n"
    "v_permlane16_swap_b32 v244, v248\n"
    "v_mov_b32 v246, v244\n"
    "v_mov_b32_e64 v250, v248\n"
    "v_cvt_pk_bf16_f32 v133, v66, v67\n"
    "v_permlane32_swap_b32 v244, v246\n"
    "v_permlane32_swap_b32 v248, v250\n"
    "v_pk_fma_f32 v[16:17], v[16:17], s[48:49], v[244:245] op_sel_hi:[1,1,0] neg_lo:[0,0,1] neg_hi:[0,0,1]\n"
    "buffer_load_dwordx4 v1, s[4:7], s21 offen offset:2048 sc1 lds\n"
    "v_pk_fma_f32 v[18:19], v[18:19], s[48:49], v[244:245] op_sel_hi:[1,1,0] neg_lo:[0,0,1] neg_hi:[0,0,1]\n"
    "v_pk_fma_f32 v[24:25], v[24:25], s[48:49], v[246:247] op_sel_hi:[1,1,0] neg_lo:[0,0,1] neg_hi:[0,0,1]\n"
    "v_pk_fma_f32 v[26:27], v[26:27], s[48:49], v[246:247] op_sel_hi:[1,1,0] neg_lo:[0,0,1] neg_hi:[0,0,1]\n"
    "v_pk_fma_f32 v[32:33], v[32:33], s[48:49], v[248:249] op_sel_hi:[1,1,0] neg_lo:[0,0,1] neg_hi:[0,0,1]\n"
    "v_pk_fma_f32 v[34:35], v[34:35], s[48:49], v[248:249] op_sel_hi:[1,1,0] neg_lo:[0,0,1] neg_hi:[0,0,1]\n"
    "v_pk_fma_f32 v[40:41], v[40:41], s[48:49], v[250:251] op_sel_hi:[1,1,0] neg_lo:[0,0,1] neg_hi:[0,0,1]\n"
    "v_pk_fma_f32 v[42:43], v[42:43], s[48:49], v[250:251] op_sel_hi:[1,1,0] neg_lo:[0,0,1] neg_hi:[0,0,1]\n"
    "v_exp_f32 v16, v16\n"
    "v_exp_f32 v17, v17\n"
    "v_exp_f32 v18, v18\n"
    "v_exp_f32 v19, v19\n"
    "v_exp_f32_e64 v24, v24\n"
    "buffer_load_dwordx4 v1, s[4:7], s21 offen offset:3072 sc1 lds\n"
    "v_exp_f32 v25, v25\n"
    "v_exp_f32 v26, v26\n"
    "v_exp_f32 v27, v27\n"
    "v_exp_f32 v32, v32\n"
    "v_exp_f32 v33, v33\n"
    "v_exp_f32 v34, v34\n"
    "v_exp_f32 v35, v35\n"
    "v_exp_f32 v40, v40\n"
    "s_add_u32 m0, s20, 32768\n"
    "s_add_u32 s21, s19, 32768\n"
    "buffer_load_dwordx4 v1, s[4:7], s21 offen offset:0 sc1 lds\n"
    "v_exp_f32 v41, v41\n"
    "v_exp_f32 v42, v42\n"
    "v_exp_f32_e64 v43, v43\n"
    "v_cvt_pk_bf16_f32 v20, v16, v17\n"
    "v_cvt_pk_bf16_f32 v21, v18, v19\n"
    "v_cvt_pk_bf16_f32 v28, v24, v25\n"
    "v_cvt_pk_bf16_f32 v29, v26, v27\n"
    "v_cvt_pk_bf16_f32 v36, v32, v33\n"
    "v_cvt_pk_bf16_f32 v37, v34, v35\n"
    "v_cvt_pk_bf16_f32 v44, v40, v41\n"
    "v_cvt_pk_bf16_f32 v45, v42, v43\n"
    "v_and_b32_e64 v158, v20, v178\n"
    "v_bfi_b32 v160, v178, 0, v20\n"
    "buffer_load_dwordx4 v1, s[4:7], s21 offen offset:1024 sc1 lds\n"
    "v_and_b32_e64 v159, v21, v179\n"
    "v_bfi_b32 v161, v179, 0, v21\n"
    "v_and_b32_e64 v162, v28, v232\n"
    "v_bfi_b32 v164, v232, 0, v28\n"
    "v_and_b32_e64 v163, v29, v233\n"
    "v_bfi_b32 v165, v233, 0, v29\n"
    "v_and_b32_e64 v166, v36, v232\n"
    "v_bfi_b32 v168, v232, 0, v36\n"
    "v_and_b32_e64 v167, v37, v233\n"
    "v_bfi_b32 v169, v233, 0, v37\n"
    "v_and_b32_e64 v170, v44, v232\n"
    "v_bfi_b32 v172, v232, 0, v44\n"
    "v_and_b32_e64 v171, v45, v233\n"
    "v_bfi_b32 v173, v233, 0, v45\n"
    "v_cvt_pk_bf16_f32 v134, v68, v69\n"
    "v_cvt_pk_bf16_f32 v135, v70, v71\n"
    "s_add_u32 m0, s20, 34816\n"
    "s_add_u32 s21, s26, 34816\n"
    "buffer_load_dwordx4 v1, s[4:7], s21 offen offset:0 sc1 lds\n"
    "v_cvt_pk_bf16_f32 v136, v72, v73\n"
    "v_cvt_pk_bf16_f32 v137, v74, v75\n"
    "v_mfma_f32_16x16x32_bf16 v[48:51], v[228:231], v[158:161], 0\n"
    "v_mfma_f32_16x16x32_bf16 v[52:55], v[228:231], v[162:165], 0\n"
    "v_mfma_f32_16x16x32_bf16 v[56:59], v[228:231], v[166:169], 0\n"
    "s_nop 5\n"
    "v_rcp_f32 v244, v48\n"
    "v_mfma_f32_16x16x32_bf16 v[60:63], v[228:231], v[170:173], 0\n"
    "v_rcp_f32_e64 v246, v52\n"
    "v_mfma_f32_16x16x32_bf16 v[16:19], v[128:131], v[158:161], 0\n"
    "v_rcp_f32_e64 v248, v56\n"
    "v_mfma_f32_16x16x32_bf16 v[24:27], v[130:133], v[162:165], 0\n"
    "s_nop 3\n"
    "v_rcp_f32 v250, v60\n"
    "v_mfma_f32_16x16x32_bf16 v[32:35], v[132:135], v[166:169], 0\n"
    "v_mfma_f32_16x16x32_bf16 v[40:43], v[134:137], v[170:173], 0\n"
    "s_waitcnt lgkmcnt(3)\n"
    "v_mfma_f32_16x16x32_bf16 v[20:23], v[138:141], v[158:161], 0\n"
    "s_waitcnt lgkmcnt(2)\n"
    "v_mfma_f32_16x16x32_bf16 v[28:31], v[140:143], v[162:165], 0\n"
    "s_waitcnt lgkmcnt(1)\n"
    "v_mfma_f32_16x16x32_bf16 v[36:39], v[142:145], v[166:169], 0\n"
    "s_waitcnt lgkmcnt(0)\n"
    "v_mfma_f32_16x16x32_bf16 v[44:47], v[144:147], v[170:173], 0\n"
    "v_pk_mul_f32 v[16:17], v[16:17], v[244:245] op_sel_hi:[1,0]\n"
    "v_pk_mul_f32 v[18:19], v[18:19], v[244:245] op_sel_hi:[1,0]\n"
    "v_pk_mul_f32 v[24:25], v[24:25], v[246:247] op_sel_hi:[1,0]\n"
    "v_pk_mul_f32 v[26:27], v[26:27], v[246:247] op_sel_hi:[1,0]\n"
    "v_pk_mul_f32 v[32:33], v[32:33], v[248:249] op_sel_hi:[1,0]\n"
    "v_pk_mul_f32 v[34:35], v[34:35], v[248:249] op_sel_hi:[1,0]\n"
    "v_pk_mul_f32 v[40:41], v[40:41], v[250:251] op_sel_hi:[1,0]\n"
    "v_pk_mul_f32 v[42:43], v[42:43], v[250:251] op_sel_hi:[1,0]\n"
    "v_pk_mul_f32 v[20:21], v[20:21], v[244:245] op_sel_hi:[1,0]\n"
    "v_pk_mul_f32 v[22:23], v[22:23], v[244:245] op_sel_hi:[1,0]\n"
    "v_cvt_pk_bf16_f32 v64, v16, v17\n"
    "v_cvt_pk_bf16_f32 v65, v18, v19\n"
    "v_pk_mul_f32 v[28:29], v[28:29], v[246:247] op_sel_hi:[1,0]\n"
    "v_pk_mul_f32 v[30:31], v[30:31], v[246:247] op_sel_hi:[1,0]\n"
    "v_cvt_pk_bf16_f32 v66, v24, v25\n"
    "v_cvt_pk_bf16_f32 v67, v26, v27\n"
    "v_cvt_pk_bf16_f32 v68, v32, v33\n"
    "v_cvt_pk_bf16_f32 v69, v34, v35\n"
    "v_cvt_pk_bf16_f32 v70, v40, v41\n"
    "v_cvt_pk_bf16_f32 v71, v42, v43\n"
    "v_cvt_pk_bf16_f32 v72, v20, v21\n"
    "v_cvt_pk_bf16_f32 v73, v22, v23\n"
    "v_min_u32 v64, -2, v64\n"
    "v_min_u32 v65, -2, v65\n"
    "v_cvt_pk_bf16_f32 v74, v28, v29\n"
    "v_cvt_pk_bf16_f32 v75, v30, v31\n"
    "v_min_u32 v66, -2, v66\n"
    "v_min_u32 v67, -2, v67\n"
    "v_pk_mul_f32 v[36:37], v[36:37], v[248:249] op_sel_hi:[1,0]\n"
    "v_pk_mul_f32 v[38:39], v[38:39], v[248:249] op_sel_hi:[1,0]\n"
    "v_min_u32 v68, -2, v68\n"
    "v_min_u32 v69, -2, v69\n"
    "v_min_u32 v70, -2, v70\n"
    "v_min_u32 v71, -2, v71\n"
    "v_cvt_pk_bf16_f32 v76, v36, v37\n"
    "v_cvt_pk_bf16_f32 v77, v38, v39\n"
    "v_pk_mul_f32 v[44:45], v[44:45], v[250:251] op_sel_hi:[1,0]\n"
    "v_pk_mul_f32 v[46:47], v[46:47], v[250:251] op_sel_hi:[1,0]\n"
    "v_cndmask_b32 v80, v72, v74, s[60:61]\n"
    "v_cndmask_b32 v81, v73, v75, s[60:61]\n"
    "v_cvt_pk_bf16_f32 v78, v44, v45\n"
    "v_cvt_pk_bf16_f32 v79, v46, v47\n"
    "v_cndmask_b32 v80, v80, v76, s[62:63]\n"
    "v_cndmask_b32 v81, v81, v77, s[62:63]\n"
    "v_permlane16_swap_b32 v64, v66\n"
    "v_permlane16_swap_b32 v65, v67\n"
    "v_permlane16_swap_b32 v68, v70\n"
    "v_permlane16_swap_b32 v69, v71\n"
    "v_cndmask_b32 v80, v80, v78, s[64:65]\n"
    "v_cndmask_b32 v81, v81, v79, s[64:65]\n"
    "v_min_u32 v80, -2, v80\n"
    "v_min_u32 v81, -2, v81\n"
    "s_waitcnt vmcnt(5)\n"
    "v_cmp_ne_u64 vcc, s[42:43], v[252:253]\n"
    "s_nop 0\n"
    "s_cbranch_vccnz Lf2r12_%=\n"
    "Lf2b13_%=:\n"
    "s_barrier\n"
    "buffer_store_dwordx4 v[64:67], v113, s[4:7], s51 offen sc1\n"
    "buffer_store_dwordx4 v[68:71], v113, s[4:7], s52 offen sc1\n"
    "buffer_store_dwordx2 v[80:81], v114, s[4:7], s50 offen sc1\n"
    "v_accvgpr_read_b32 v176, a246\n"
    "v_accvgpr_read_b32 v177, a247\n"
    "v_accvgpr_read_b32 v178, a248\n"
    "v_accvgpr_read_b32 v179, a249\n"
    "v_accvgpr_read_b32 v180, a250\n"
    "v_accvgpr_read_b32 v181, a251\n"
    "v_accvgpr_read_b32 v182, a252\n"
    "v_accvgpr_read_b32 v183, a253\n"
    "v_accvgpr_read_b32 v184, a254\n"
    "v_accvgpr_read_b32 v185, a255\n"
    "s_lshl1_add_u32 s44, s39, s40\n"
    "s_mul_i32 s45, s44, 81920\n"
    "s_add_u32 s45, s45, 8208384\n"
    "s_lshl_b32 s46, s36, 12\n"
    "s_lshl_b32 s47, s36, 10\n"
    "s_add_u32 s47, s47, 16384\n"
    "s_add_u32 s49, s36, 1\n"
    "s_and_b32 s49, s49, 3\n"
    "s_mul_i32 s49, s49, 20480\n"
    "s_add_u32 s49, s49, s45\n"
    "s_add_u32 s60, s49, s46\n"
    "s_add_u32 s52, s49, s47\n"
    "s_add_u32 s49, s36, 2\n"
    "s_and_b32 s49, s49, 3\n"
    "s_mul_i32 s49, s49, 20480\n"
    "s_add_u32 s49, s49, s45\n"
    "s_add_u32 s61, s49, s46\n"
    "s_add_u32 s53, s49, s47\n"
    "s_add_u32 s49, s36, 3\n"
    "s_and_b32 s49, s49, 3\n"
    "s_mul_i32 s49, s49, 20480\n"
    "s_add_u32 s49, s49, s45\n"
    "s_add_u32 s62, s49, s46\n"
    "s_add_u32 s54, s49, s47\n"
    "s_add_u32 s49, s36, 4\n"
    "s_and_b32 s49, s49, 3\n"
    "s_mul_i32 s49, s49, 20480\n"
    "s_add_u32 s49, s49, s45\n"
    "s_add_u32 s63, s49, s46\n"
    "s_add_u32 s55, s49, s47\n"
    "s_lshl2_add_u32 s49, s44, s36\n"
    "s_mul_i32 s58, s49, 20480\n"
    "s_add_u32 s58, s58, 10829824\n"
    "s_lshl_b32 s59, s41, 9\n"
    "s_add_u32 s59, s59, s58\n"
    "s_add_u32 s59, s59, 16384\n"
    "s_lshl_b32 s50, s41, 11\n"
    "s_add_u32 s58, s58, s50\n"
    "s_lshl_b32 s27, s49, 10\n"
    "s_lshl_b32 s50, s41, 7\n"
    "s_add_u32 s27, s27, s50\n"
    "s_add_u32 s27, s27, 13451264\n"
    "v_lshrrev_b32 v5, 5, v0\n"
    "v_lshlrev_b32 v6, 4, v8\n"
    "v_lshl_add_u32 v6, v5, 8, v6\n"
    "v_lshlrev_b32_e64 v11, 3, v8\n"
    "v_lshl_add_u32 v11, v5, 2, v11\n"
    "v_and_b32 v5, 16, v0\n"
    "v_cmp_eq_u32 vcc, 0, v5\n"
    "v_mov_b32_e64 v5, 1073741824\n"
    "s_mul_i32 s49, s36, 2560\n"
    "s_add_u32 s49, s49, 144384\n"
    "v_cndmask_b32 v12, v5, v6, vcc\n"
    "v_cndmask_b32 v13, v5, v11, vcc\n"
    "v_add_u32 v14, s49, v2\n"
    "s_add_u32 s49, s36, 1\n"
    "s_and_b32 s49, s49, 3\n"
    "s_mul_i32 s49, s49, 15360\n"
    "s_add_u32 s49, s49, 98304\n"
    "v_add_u32 v4, s49, v1\n"
    "s_add_u32 s49, s36, 2\n"
    "s_and_b32 s49, s49, 3\n"
    "s_mul_i32 s49, s49, 15360\n"
    "s_add_u32 s49, s49, 103424\n"
    "v_add_u32 v5, s49, v1\n"
    "s_add_u32 s49, s36, 3\n"
    "s_and_b32 s49, s49, 3\n"
    "s_mul_i32 s49, s49, 15360\n"
    "s_add_u32 s49, s49, 108544\n"
    "v_add_u32 v6, s49, v1\n"
    "s_mul_i32 s49, s36, 15360\n"
    "s_add_u32 s49, s49, 98304\n"
    "v_add_u32 v7, s49, v1\n"
    "v_lshlrev_b32 v236, 16, v176\n"
    "v_and_b32 v237, 0xffff0000, v176\n"
    "v_lshlrev_b32_e64 v238, 16, v177\n"
    "v_and_b32 v239, 0xffff0000, v177\n"
    "v_lshlrev_b32_e64 v240, 16, v178\n"
    "v_and_b32 v241, 0xffff0000, v178\n"
    "v_lshlrev_b32_e64 v242, 16, v179\n"
    "v_and_b32 v243, 0xffff0000, v179\n"
    "v_lshlrev_b32_e64 v244, 16, v180\n"
    "v_and_b32 v245, 0xffff0000, v180\n"
    "v_lshlrev_b32_e64 v246, 16, v181\n"
    "v_and_b32 v247, 0xffff0000, v181\n"
    "v_lshlrev_b32_e64 v248, 16, v182\n"
    "v_and_b32 v249, 0xffff0000, v182\n"
    "v_lshlrev_b32_e64 v250, 16, v183\n"
    "v_and_b32 v251, 0xffff0000, v183\n"
    "v_lshlrev_b32_e64 v252, 16, v184\n"
    "v_and_b32 v253, 0xffff0000, v184\n"
    "v_lshlrev_b32_e64 v254, 16, v185\n"
    "v_and_b32 v255, 0xffff0000, v185\n"
    "s_waitcnt vmcnt(0)\n"
    "s_sleep 8\n"
    "Lg15_%=:\n"
    "s_mov_b32 s66, 65536\n"
    "Lrp17_%=:\n"
    "s_add_u32 s67, s52, 0\n"
    "buffer_load_dwordx2 v[32:33], v2, s[4:7], s67 offen offset:0 sc1\n"
    "s_add_u32 s67, s52, 0\n"
    "buffer_load_dwordx2 v[34:35], v2, s[4:7], s67 offen offset:512 sc1\n"
    "s_add_u32 s67, s60, 0\n"
    "buffer_load_dwordx4 v[16:19], v1, s[4:7], s67 offen offset:0 sc1\n"
    "s_add_u32 s67, s60, 0\n"
    "buffer_load_dwordx4 v[20:23], v1, s[4:7], s67 offen offset:1024 sc1\n"
    "s_add_u32 s67, s60, 0\n"
    "buffer_load_dwordx4 v[24:27], v1, s[4:7], s67 offen offset:2048 sc1\n"
    "s_add_u32 s67, s60, 0\n"
    "buffer_load_dwordx4 v[28:31], v1, s[4:7], s67 offen offset:3072 sc1\n"
    "s_waitcnt vmcnt(0)\n"
    "v_max3_u32 v15, v32, v33, v34\n"
    "v_max3_u32 v15, v15, v35, v16\n"
    "v_max3_u32 v15, v15, v17, v18\n"
    "v_max3_u32 v15, v15, v19, v20\n"
    "v_max3_u32 v15, v15, v21, v22\n"
    "v_max3_u32 v15, v15, v23, v24\n"
    "v_max3_u32 v15, v15, v25, v26\n"
    "v_max3_u32 v15, v15, v27, v28\n"
    "v_max3_u32 v15, v15, v29, v30\n"
    "v_max_u32 v15, v15, v31\n"
    "v_cmp_eq_u32 vcc, -1, v15\n"
    "s_nop 0\n"
    "s_cbranch_vccz Lgb16_%=\n"
    "s_sub_u32 s66, s66, 1\n"
    "s_cbranch_scc0 Lrp17_%=\n"
    "Lgb16_%=:\n"
    "buffer_load_dwordx2 v[52:53], v2, s[4:7], s53 offen offset:0 sc1\n"
    "buffer_load_dwordx2 v[54:55], v2, s[4:7], s53 offen offset:512 sc1\n"
    "buffer_load_dwordx4 v[36:39], v1, s[4:7], s61 offen offset:0 sc1\n"
    "buffer_load_dwordx4 v[40:43], v1, s[4:7], s61 offen offset:1024 sc1\n"
    "buffer_load_dwordx4 v[44:47], v1, s[4:7], s61 offen offset:2048 sc1\n"
    "buffer_load_dwordx4 v[48:51], v1, s[4:7], s61 offen offset:3072 sc1\n"
    "buffer_load_dwordx2 v[72:73], v2, s[4:7], s54 offen offset:0 sc1\n"
    "buffer_load_dwordx2 v[74:75], v2, s[4:7], s54 offen offset:512 sc1\n"
    "buffer_load_dwordx4 v[56:59], v1, s[4:7], s62 offen offset:0 sc1\n"
    "buffer_load_dwordx4 v[60:63], v1, s[4:7], s62 offen offset:1024 sc1\n"
    "buffer_load_dwordx4 v[64:67], v1, s[4:7], s62 offen offset:2048 sc1\n"
    "buffer_load_dwordx4 v[68:71], v1, s[4:7], s62 offen offset:3072 sc1\n"
    "v_mfma_f32_16x16x32_bf16 v[96:99], a[128:131], v[16:19], 0\n"
    "buffer_load_dwordx2 v[92:93], v2, s[4:7], s55 offen offset:0 sc1\n"
    "v_mfma_f32_16x16x32_bf16 v[96:99], a[148:151], v[20:23], v[96:99]\n"
    "v_mfma_f32_16x16x32_bf16 v[96:99], a[168:171], v[24:27], v[96:99]\n"
    "v_mfma_f32_16x16x32_bf16 v[96:99], a[188:191], v[28:31], v[96:99]\n"
    "buffer_load_dwordx2 v[94:95], v2, s[4:7], s55 offen offset:512 sc1\n"
    "v_mfma_f32_16x16x32_bf16 v[96:99], a[208:211], v[32:35], v[96:99]\n"
    "v_mfma_f32_16x16x32_bf16 v[100:103], a[132:135], v[16:19], 0\n"
    "v_mfma_f32_16x16x32_bf16 v[100:103], a[152:155], v[20:23], v[100:103]\n"
    "buffer_load_dwordx4 v[76:79], v1, s[4:7], s63 offen offset:0 sc1\n"
    "v_mfma_f32_16x16x32_bf16 v[100:103], a[172:175], v[24:27], v[100:103]\n"
    "v_mfma_f32_16x16x32_bf16 v[100:103], a[192:195], v[28:31], v[100:103]\n"
    "v_mfma_f32_16x16x32_bf16 v[100:103], a[212:215], v[32:35], v[100:103]\n"
    "buffer_load_dwordx4 v[80:83], v1, s[4:7], s63 offen offset:1024 sc1\n"
    "v_mfma_f32_16x16x32_bf16 v[104:107], a[136:139], v[16:19], 0\n"
    "v_mfma_f32_16x16x32_bf16 v[104:107], a[156:159], v[20:23], v[104:107]\n"
    "v_mfma_f32_16x16x32_bf16 v[104:107], a[176:179], v[24:27], v[104:107]\n"
    "buffer_load_dwordx4 v[84:87], v1, s[4:7], s63 offen offset:2048 sc1\n"
    "s_waitcnt vmcnt(15)\n"
    "v_max3_u32 v15, v52, v53, v54\n"
    "v_mfma_f32_16x16x32_bf16 v[104:107], a[196:199], v[28:31], v[104:107]\n"
    "s_waitcnt vmcnt(14)\n"
    "v_max3_u32 v15, v15, v55, v36\n"
    "v_max3_u32 v15, v15, v37, v38\n"
    "v_mfma_f32_16x16x32_bf16 v[104:107], a[216:219], v[32:35], v[104:107]\n"
    "s_waitcnt vmcnt(13)\n"
    "v_max3_u32 v15, v15, v39, v40\n"
    "v_max3_u32 v15, v15, v41, v42\n"
    "v_mfma_f32_16x16x32_bf16 v[108:111], a[140:143], v[16:19], 0\n"
    "buffer_load_dwordx4 v[88:91], v1, s[4:7], s63 offen offset:3072 sc1\n"
    "s_waitcnt vmcnt(13)\n"
    "v_max3_u32 v15, v15, v43, v44\n"
    "v_mfma_f32_16x16x32_bf16 v[108:111], a[160:163], v[20:23], v[108:111]\n"
    "v_max3_u32 v15, v15, v45, v46\n"
    "s_waitcnt vmcnt(12)\n"
    "v_max3_u32 v15, v15, v47, v48\n"
    "v_mfma_f32_16x16x32_bf16 v[108:111], a[180:183], v[24:27], v[108:111]\n"
    "v_max3_u32 v15, v15, v49, v50\n"
    "v_max_u32_e64 v15, v15, v51\n"
    "v_mfma_f32_16x16x32_bf16 v[108:111], a[200:203], v[28:31], v[108:111]\n"
    "s_add_u32 s67, s95, Lcp3_%=-Lpc_%=+0\n"
    "s_min_u32 s67, s67, Lend_%=-Lpc_%=-4\n"
    "v_mov_b32_e64 v3, s67\n"
    "global_load_dword v3, v3, s[98:99]\n"
    "v_mfma_f32_16x16x32_bf16 v[108:111], a[220:223], v[32:35], v[108:111]\n"
    "v_cmp_eq_u32_e64 vcc, -1, v15\n"
    "v_mfma_f32_16x16x32_bf16 v[112:115], a[144:147], v[16:19], 0\n"
    "s_cbranch_vccnz Lrt18_%=\n"
    "Lbk19_%=:\n"
    "v_mfma_f32_16x16x32_bf16 v[112:115], a[164:167], v[20:23], v[112:115]\n"
    "v_mfma_f32_16x16x32_bf16 v[112:115], a[184:187], v[24:27], v[112:115]\n"
    "v_mfma_f32_16x16x32_bf16 v[112:115], a[204:207], v[28:31], v[112:115]\n"
    "v_mfma_f32_16x16x32_bf16 v[112:115], a[224:227], v[32:35], v[112:115]\n"
    "s_add_u32 m0, s20, 57344\n"
    "s_add_u32 s21, s19, 57344\n"
    "buffer_load_dwordx4 v1, s[4:7], s21 offen offset:0 sc1 lds\n"
    "v_mfma_f32_16x16x32_bf16 v[116:119], a[128:131], v[36:39], 0\n"
    "v_mfma_f32_16x16x32_bf16 v[116:119], a[148:151], v[40:43], v[116:119]\n"
    "v_mfma_f32_16x16x32_bf16 v[116:119], a[168:171], v[44:47], v[116:119]\n"
    "ds_write_b128 v4, v[96:99] offset:0\n"
    "v_mfma_f32_16x16x32_bf16 v[116:119], a[188:191], v[48:51], v[116:119]\n"
    "v_mfma_f32_16x16x32_bf16 v[116:119], a[208:211], v[52:55], v[116:119]\n"
    "ds_write_b128 v4, v[100:103] offset:1024\n"
    "v_mfma_f32_16x16x32_bf16 v[120:123], a[132:135], v[36:39], 0\n"
    "buffer_load_dwordx4 v1, s[4:7], s21 offen offset:1024 sc1 lds\n"
    "v_mfma_f32_16x16x32_bf16 v[120:123], a[152:155], v[40:43], v[120:123]\n"
    "ds_write_b128 v4, v[104:107] offset:2048\n"
    "v_mfma_f32_16x16x32_bf16 v[120:123], a[172:175], v[44:47], v[120:123]\n"
    "v_mfma_f32_16x16x32_bf16 v[120:123], a[192:195], v[48:51], v[120:123]\n"
    "ds_write_b128 v4, v[108:111] offset:3072\n"
    "v_mfma_f32_16x16x32_bf16 v[120:123], a[212:215], v[52:55], v[120:123]\n"
    "v_mfma_f32_16x16x32_bf16 v[124:127], a[136:139], v[36:39], 0\n"
    "ds_write_b128 v4, v[112:115] offset:4096\n"
    "v_mfma_f32_16x16x32_bf16 v[124:127], a[156:159], v[40:43], v[124:127]\n"
    "buffer_load_dwordx4 v1, s[4:7], s21 offen offset:2048 sc1 lds\n"
    "v_mfma_f32_16x16x32_bf16 v[124:127], a[176:179], v[44:47], v[124:127]\n"
    "s_waitcnt vmcnt(14)\n"
    "v_max3_u32 v15, v72, v73, v74\n"
    "s_waitcnt vmcnt(13)\n"
    "v_max3_u32 v15, v15, v75, v56\n"
    "v_mfma_f32_16x16x32_bf16 v[124:127], a[196:199], v[48:51], v[124:127]\n"
    "v_max3_u32 v15, v15, v57, v58\n"
    "s_waitcnt vmcnt(12)\n"
    "v_max3_u32 v15, v15, v59, v60\n"
    "v_mfma_f32_16x16x32_bf16 v[124:127], a[216:219], v[52:55], v[124:127]\n"
    "v_max3_u32 v15, v15, v61, v62\n"
    "s_waitcnt vmcnt(11)\n"
    "v_max3_u32 v15, v15, v63, v64\n"
    "v_mfma_f32_16x16x32_bf16 v[128:131], a[140:143], v[36:39], 0\n"
    "v_max3_u32 v15, v15, v65, v66\n"
    "s_waitcnt vmcnt(10)\n"
    "v_max3_u32 v15, v15, v67, v68\n"
    "v_mfma_f32_16x16x32_bf16 v[128:131], a[160:163], v[40:43], v[128:131]\n"
    "v_max3_u32 v15, v15, v69, v70\n"
    "v_max_u32_e64 v15, v15, v71\n"
    "v_mfma_f32_16x16x32_bf16 v[128:131], a[180:183], v[44:47], v[128:131]\n"
    "buffer_load_dwordx4 v1, s[4:7], s21 offen offset:3072 sc1 lds\n"
    "v_cmp_eq_u32_e64 vcc, -1, v15\n"
    "v_mfma_f32_16x16x32_bf16 v[128:131], a[200:203], v[48:51], v[128:131]\n"
    "s_cbranch_vccnz Lrt21_%=\n"
    "Lbk22_%=:\n"
    "v_mfma_f32_16x16x32_bf16 v[128:131], a[220:223], v[52:55], v[128:131]\n"
    "v_mfma_f32_16x16x32_bf16 v[132:135], a[144:147], v[36:39], 0\n"
    "v_mfma_f32_16x16x32_bf16 v[132:135], a[164:167], v[40:43], v[132:135]\n"
    "v_mfma_f32_16x16x32_bf16 v[132:135], a[184:187], v[44:47], v[132:135]\n"
    "v_mfma_f32_16x16x32_bf16 v[132:135], a[204:207], v[48:51], v[132:135]\n"
    "s_add_u32 m0, s20, 61440\n"
    "s_add_u32 s21, s19, 61440\n"
    "buffer_load_dwordx4 v1, s[4:7], s21 offen offset:0 sc1 lds\n"
    "v_mfma_f32_16x16x32_bf16 v[132:135], a[224:227], v[52:55], v[132:135]\n"
    "v_mfma_f32_16x16x32_bf16 v[136:139], a[128:131], v[56:59], 0\n"
    "v_mfma_f32_16x16x32_bf16 v[136:139], a[148:151], v[60:63], v[136:139]\n"
    "v_mfma_f32_16x16x32_bf16 v[136:139], a[168:171], v[64:67], v[136:139]\n"
    "ds_write_b128 v5, v[116:119] offset:0\n"
    "v_mfma_f32_16x16x32_bf16 v[136:139], a[188:191], v[68:71], v[136:139]\n"
    "v_mfma_f32_16x16x32_bf16 v[136:139], a[208:211], v[72:75], v[136:139]\n"
    "buffer_load_dwordx4 v1, s[4:7], s21 offen offset:1024 sc1 lds\n"
    "ds_write_b128 v5, v[120:123] offset:1024\n"
    "v_mfma_f32_16x16x32_bf16 v[140:143], a[132:135], v[56:59], 0\n"
    "v_mfma_f32_16x16x32_bf16 v[140:143], a[152:155], v[60:63], v[140:143]\n"
    "ds_write_b128 v5, v[124:127] offset:2048\n"
    "v_mfma_f32_16x16x32_bf16 v[140:143], a[172:175], v[64:67], v[140:143]\n"
    "v_mfma_f32_16x16x32_bf16 v[140:143], a[192:195], v[68:71], v[140:143]\n"
    "ds_write_b128 v5, v[128:131] offset:3072\n"
    "v_mfma_f32_16x16x32_bf16 v[140:143], a[212:215], v[72:75], v[140:143]\n"
    "v_mfma_f32_16x16x32_bf16 v[144:147], a[136:139], v[56:59], 0\n"
    "s_add_u32 m0, s20, 63488\n"
    "s_add_u32 s21, s26, 63488\n"
    "buffer_load_dwordx4 v1, s[4:7], s21 offen offset:0 sc1 lds\n"
    "v_mfma_f32_16x16x32_bf16 v[144:147], a[156:159], v[60:63], v[144:147]\n"
    "ds_write_b128 v5, v[132:135] offset:4096\n"
    "v_mfma_f32_16x16x32_bf16 v[144:147], a[176:179], v[64:67], v[144:147]\n"
    "s_waitcnt vmcnt(12)\n"
    "v_max3_u32 v15, v92, v93, v94\n"
    "s_waitcnt vmcnt(11)\n"
    "v_max3_u32 v15, v15, v95, v76\n"
    "v_mfma_f32_16x16x32_bf16 v[144:147], a[196:199], v[68:71], v[144:147]\n"
    "v_max3_u32 v15, v15, v77, v78\n"
    "s_waitcnt vmcnt(10)\n"
    "v_max3_u32 v15, v15, v79, v80\n"
    "v_mfma_f32_16x16x32_bf16 v[144:147], a[216:219], v[72:75], v[144:147]\n"
    "v_max3_u32 v15, v15, v81, v82\n"
    "s_waitcnt vmcnt(9)\n"
    "v_max3_u32 v15, v15, v83, v84\n"
    "v_mfma_f32_16x16x32_bf16 v[148:151], a[140:143], v[56:59], 0\n"
    "v_max3_u32 v15, v15, v85, v86\n"
    "s_waitcnt vmcnt(8)\n"
    "v_max3_u32 v15, v15, v87, v88\n"
    "v_mfma_f32_16x16x32_bf16 v[148:151], a[160:163], v[60:63], v[148:151]\n"
    "s_add_u32 s18, s16, 0\n"
    "buffer_load_dwordx4 a[56:59], v1, s[4:7], s18 offen offset:0 sc1\n"
    "v_mfma_f32_16x16x32_bf16 v[148:151], a[180:183], v[64:67], v[148:151]\n"
    "v_max3_u32 v15, v15, v89, v90\n"
    "v_max_u32 v15, v15, v91\n"
    "v_mfma_f32_16x16x32_bf16 v[148:151], a[200:203], v[68:71], v[148:151]\n"
    "v_cmp_eq_u32_e64 vcc, -1, v15\n"
    "v_mfma_f32_16x16x32_bf16 v[148:151], a[220:223], v[72:75], v[148:151]\n"
    "s_cbranch_vccnz Lrt24_%=\n"
    "Lbk25_%=:\n"
    "v_mfma_f32_16x16x32_bf16 v[152:155], a[144:147], v[56:59], 0\n"
    "v_mfma_f32_16x16x32_bf16 v[152:155], a[164:167], v[60:63], v[152:155]\n"
    "v_mfma_f32_16x16x32_bf16 v[152:155], a[184:187], v[64:67], v[152:155]\n"
    "buffer_load_dwordx4 a[60:63], v1, s[4:7], s18 offen offset:1024 sc1\n"
    "v_mfma_f32_16x16x32_bf16 v[152:155], a[204:207], v[68:71], v[152:155]\n"
    "v_mfma_f32_16x16x32_bf16 v[152:155], a[224:227], v[72:75], v[152:155]\n"
    "v_mfma_f32_16x16x32_bf16 v[156:159], a[128:131], v[76:79], v[236:239]\n"
    "v_mfma_f32_16x16x32_bf16 v[156:159], a[148:151], v[80:83], v[156:159]\n"
    "v_mfma_f32_16x16x32_bf16 v[156:159], a[168:171], v[84:87], v[156:159]\n"
    "ds_write_b128 v6, v[136:139] offset:0\n"
    "v_mfma_f32_16x16x32_bf16 v[156:159], a[188:191], v[88:91], v[156:159]\n"
    "buffer_load_dwordx4 a[64:67], v1, s[4:7], s18 offen offset:2048 sc1\n"
    "v_mfma_f32_16x16x32_bf16 v[156:159], a[208:211], v[92:95], v[156:159]\n"
    "ds_write_b128 v6, v[140:143] offset:1024\n"
    "v_mfma_f32_16x16x32_bf16 v[160:163], a[132:135], v[76:79], v[240:243]\n"
    "v_mfma_f32_16x16x32_bf16 v[160:163], a[152:155], v[80:83], v[160:163]\n"
    "ds_write_b128 v6, v[144:147] offset:2048\n"
    "v_mfma_f32_16x16x32_bf16 v[160:163], a[172:175], v[84:87], v[160:163]\n"
    "v_mfma_f32_16x16x32_bf16 v[160:163], a[192:195], v[88:91], v[160:163]\n"
    "ds_write_b128 v6, v[148:151] offset:3072\n"
    "v_mfma_f32_16x16x32_bf16 v[160:163], a[212:215], v[92:95], v[160:163]\n"
    "buffer_load_dwordx4 a[68:71], v1, s[4:7], s18 offen offset:3072 sc1\n"
    "v_mfma_f32_16x16x32_bf16 v[164:167], a[136:139], v[76:79], v[244:247]\n"
    "ds_write_b128 v6, v[152:155] offset:4096\n"
    "v_mfma_f32_16x16x32_bf16 v[164:167], a[156:159], v[80:83], v[164:167]\n"
    "s_waitcnt lgkmcnt(0)\n"
    "s_barrier\n"
    "v_mfma_f32_16x16x32_bf16 v[164:167], a[176:179], v[84:87], v[164:167]\n"
    "ds_read_b128 v[176:179], v7 offset:0\n"
    "ds_read_b128 v[196:199], v7 offset:5120\n"
    "v_mfma_f32_16x16x32_bf16 v[164:167], a[196:199], v[88:91], v[164:167]\n"
    "ds_read_b128 v[180:183], v7 offset:1024\n"
    "ds_read_b128 v[200:203], v7 offset:6144\n"
    "v_mfma_f32_16x16x32_bf16 v[164:167], a[216:219], v[92:95], v[164:167]\n"
    "ds_read_b128 v[184:187], v7 offset:2048\n"
    "ds_read_b128 v[204:207], v7 offset:7168\n"
    "v_mfma_f32_16x16x32_bf16 v[168:171], a[140:143], v[76:79], v[248:251]\n"
    "s_add_u32 s18, s16, 4096\n"
    "buffer_load_dwordx4 a[72:75], v1, s[4:7], s18 offen offset:0 sc1\n"
    "v_mfma_f32_16x16x32_bf16 v[168:171], a[160:163], v[80:83], v[168:171]\n"
    "ds_read_b128 v[188:191], v7 offset:3072\n"
    "ds_read_b128 v[208:211], v7 offset:8192\n"
    "v_mfma_f32_16x16x32_bf16 v[168:171], a[180:183], v[84:87], v[168:171]\n"
    "ds_read_b128 v[192:195], v7 offset:4096\n"
    "ds_read_b128 v[212:215], v7 offset:9216\n"
    "v_mfma_f32_16x16x32_bf16 v[168:171], a[200:203], v[88:91], v[168:171]\n"
    "ds_read_b128 v[216:219], v7 offset:10240\n"
    "ds_read_b128 v[220:223], v7 offset:11264\n"
    "v_mfma_f32_16x16x32_bf16 v[168:171], a[220:223], v[92:95], v[168:171]\n"
    "ds_read_b128 v[224:227], v7 offset:12288\n"
    "ds_read_b128 v[228:231], v7 offset:13312\n"
    "v_mfma_f32_16x16x32_bf16 v[172:175], a[144:147], v[76:79], v[252:255]\n"
    "ds_read_b128 v[232:235], v7 offset:14336\n"
    "v_mfma_f32_16x16x32_bf16 v[172:175], a[164:167], v[80:83], v[172:175]\n"
    "s_add_u32 s18, s16, 12288\n"
    "buffer_load_dwordx4 a[76:79], v1, s[4:7], s18 offen offset:3072 sc1\n"
    "v_mfma_f32_16x16x32_bf16 v[172:175], a[184:187], v[84:87], v[172:175]\n"
    "v_mfma_f32_16x16x32_bf16 v[172:175], a[204:207], v[88:91], v[172:175]\n"
    "v_mfma_f32_16x16x32_bf16 v[172:175], a[224:227], v[92:95], v[172:175]\n"
    "s_waitcnt vmcnt(6)\n"
    "s_waitcnt lgkmcnt(0)\n"
    "s_barrier\n"
    "v_pk_add_f32 v[176:177], v[176:177], v[196:197]\n"
    "s_add_u32 m0, s20, 86016\n"
    "s_add_u32 s21, s19, 86016\n"
    "buffer_load_dwordx4 v1, s[4:7], s21 offen offset:0 sc1 lds\n"
    "v_pk_add_f32 v[156:157], v[156:157], v[216:217]\n"
    "v_pk_add_f32 v[178:179], v[178:179], v[198:199]\n"
    "v_pk_add_f32 v[158:159], v[158:159], v[218:219]\n"
    "v_pk_add_f32 v[156:157], v[156:157], v[176:177]\n"
    "v_pk_add_f32 v[158:159], v[158:159], v[178:179]\n"
    "v_pk_add_f32 v[180:181], v[180:181], v[200:201]\n"
    "v_pk_add_f32 v[160:161], v[160:161], v[220:221]\n"
    "v_pk_add_f32 v[182:183], v[182:183], v[202:203]\n"
    "v_pk_add_f32 v[162:163], v[162:163], v[222:223]\n"
    "v_pk_add_f32 v[160:161], v[160:161], v[180:181]\n"
    "v_pk_add_f32 v[162:163], v[162:163], v[182:183]\n"
    "v_pk_add_f32 v[184:185], v[184:185], v[204:205]\n"
    "v_pk_add_f32 v[164:165], v[164:165], v[224:225]\n"
    "v_pk_add_f32 v[186:187], v[186:187], v[206:207]\n"
    "v_pk_add_f32 v[166:167], v[166:167], v[226:227]\n"
    "v_pk_mul_f32 v[36:37], v[156:157], v[156:157]\n"
    "buffer_load_dwordx4 v1, s[4:7], s21 offen offset:1024 sc1 lds\n"
    "v_pk_mul_f32 v[38:39], v[158:159], v[158:159]\n"
    "v_pk_add_f32 v[164:165], v[164:165], v[184:185]\n"
    "v_pk_add_f32 v[166:167], v[166:167], v[186:187]\n"
    "v_pk_add_f32 v[188:189], v[188:189], v[208:209]\n"
    "v_pk_add_f32 v[168:169], v[168:169], v[228:229]\n"
    "v_pk_add_f32 v[190:191], v[190:191], v[210:211]\n"
    "v_pk_add_f32 v[170:171], v[170:171], v[230:231]\n"
    "v_pk_add_f32 v[32:33], v[156:157], v[158:159]\n"
    "v_pk_add_f32 v[34:35], v[160:161], v[162:163]\n"
    "v_pk_fma_f32 v[36:37], v[160:161], v[160:161], v[36:37]\n"
    "v_pk_fma_f32 v[38:39], v[162:163], v[162:163], v[38:39]\n"
    "v_pk_add_f32 v[168:169], v[168:169], v[188:189]\n"
    "v_pk_add_f32 v[170:171], v[170:171], v[190:191]\n"
    "v_pk_add_f32 v[192:193], v[192:193], v[212:213]\n"
    "v_pk_add_f32 v[172:173], v[172:173], v[232:233]\n"
    "v_pk_add_f32 v[194:195], v[194:195], v[214:215]\n"
    "buffer_load_dwordx4 v1, s[4:7], s21 offen offset:2048 sc1 lds\n"
    "v_pk_add_f32 v[174:175], v[174:175], v[234:235]\n"
    "v_pk_add_f32 v[32:33], v[32:33], v[164:165]\n"
    "v_pk_fma_f32 v[36:37], v[164:165], v[164:165], v[36:37]\n"
    "v_pk_add_f32 v[34:35], v[34:35], v[166:167]\n"
    "v_pk_fma_f32 v[38:39], v[166:167], v[166:167], v[38:39]\n"
    "v_pk_add_f32 v[172:173], v[172:173], v[192:193]\n"
    "v_pk_add_f32 v[174:175], v[174:175], v[194:195]\n"
    "v_pk_add_f32 v[32:33], v[32:33], v[168:169]\n"
    "v_pk_fma_f32 v[36:37], v[168:169], v[168:169], v[36:37]\n"
    "v_pk_add_f32 v[34:35], v[34:35], v[170:171]\n"
    "v_pk_fma_f32 v[38:39], v[170:171], v[170:171], v[38:39]\n"
    "v_pk_add_f32 v[32:33], v[32:33], v[172:173]\n"
    "v_pk_fma_f32 v[36:37], v[172:173], v[172:173], v[36:37]\n"
    "v_pk_add_f32 v[34:35], v[34:35], v[174:175]\n"
    "v_pk_fma_f32 v[38:39], v[174:175], v[174:175], v[38:39]\n"
    "v_pk_add_f32 v[32:33], v[32:33], v[34:35]\n"
    "buffer_load_dwordx4 v1, s[4:7], s21 offen offset:3072 sc1 lds\n"
    "v_pk_add_f32 v[36:37], v[36:37], v[38:39]\n"
    "v_add_f32 v32, v32, v33\n"
    "v_add_f32 v36, v36, v37\n"
    "v_cvt_pk_bf16_f32 v24, v172, v173\n"
    "v_cvt_pk_bf16_f32 v25, v174, v175\n"
    "v_permlane32_swap_b32 v32, v36\n"
    "v_add_f32 v32, v32, v36\n"
    "v_cvt_pk_bf16_f32 v16, v156, v157\n"
    "v_cvt_pk_bf16_f32 v17, v158, v159\n"
    "v_min_u32 v24, -2, v24\n"
    "v_min_u32 v25, -2, v25\n"
    "v_mov_b32 v36, v32\n"
    "v_min_u32 v16, -2, v16\n"
    "v_min_u32_e64 v17, -2, v17\n"
    "v_cvt_pk_bf16_f32 v18, v160, v161\n"
    "s_add_u32 m0, s20, 90112\n"
    "s_add_u32 s21, s19, 90112\n"
    "buffer_load_dwordx4 v1, s[4:7], s21 offen offset:0 sc1 lds\n"
    "v_cvt_pk_bf16_f32 v19, v162, v163\n"
    "v_mov_b32 v28, v24\n"
    "v_mov_b32 v29, v25\n"
    "v_mov_b32 v30, v24\n"
    "v_mov_b32 v31, v25\n"
    "v_min_u32 v18, -2, v18\n"
    "v_min_u32 v19, -2, v19\n"
    "v_cvt_pk_bf16_f32 v20, v164, v165\n"
    "v_cvt_pk_bf16_f32 v21, v166, v167\n"
    "v_cvt_pk_bf16_f32 v22, v168, v169\n"
    "v_cvt_pk_bf16_f32 v23, v170, v171\n"
    "ds_write_b64 v14, v[16:17] offset:0\n"
    "v_min_u32 v20, -2, v20\n"
    "v_min_u32 v21, -2, v21\n"
    "v_min_u32 v22, -2, v22\n"
    "v_min_u32 v23, -2, v23\n"
    "buffer_store_dwordx4 v[16:19], v1, s[4:7], s58 offen sc1\n"
    "v_permlane16_swap_b32_e64 v32, v36\n"
    "ds_write_b64 v14, v[18:19] offset:512\n"
    "buffer_store_dwordx4 v[20:23], v1, s[4:7], s58 offen offset:1024 sc1\n"
    "v_permlane16_swap_b32 v28, v30\n"
    "v_permlane16_swap_b32 v29, v31\n"
    "v_add_f32_e64 v32, v32, v36\n"
    "ds_write_b64 v14, v[20:21] offset:1024\n"
    "buffer_store_dwordx4 v[28:31], v12, s[4:7], s59 offen sc1\n"
    "v_min_u32_e64 v32, -2, v32\n"
    "ds_write_b64 v14, v[22:23] offset:1536\n"
    "buffer_store_dword v32, v13, s[4:7], s27 offen sc1\n"
    "ds_write_b64 v14, v[24:25] offset:2048\n"
    "s_lshl_b32 s49, s36, 10\n"
    "v_add_u32 v6, s49, v1\n"
    "s_add_u32 s50, s36, 4\n"
    "s_min_u32 s50, s50, 6\n"
    "s_lshl_b32 s49, s50, 10\n"
    "v_add_u32 v7, s49, v1\n"
    "v_add_u32 v9, 71680, v6\n"
    "v_add_u32 v11, 71680, v7\n"
    "v_mov_b32 v248, 0x3f803f80\n"
    "v_mov_b32 v249, 0x3f803f80\n"
    "v_mov_b32 v250, 0x3f803f80\n"
    "v_mov_b32 v251, 0x3f803f80\n"
    "ds_read_b128 a[0:3], v6 offset:0\n"
    "ds_read_b128 a[4:7], v7 offset:0\n"
    "ds_read_b128 a[8:11], v6 offset:7168\n"
    "ds_read_b128 a[12:15], v7 offset:7168\n"
    "ds_read_b128 a[16:19], v6 offset:14336\n"
    "ds_read_b128 a[20:23], v7 offset:14336\n"
    "ds_read_b128 a[24:27], v6 offset:21504\n"
    "ds_read_b128 a[28:31], v7 offset:21504\n"
    "ds_read_b128 a[32:35], v6 offset:28672\n"
    "ds_read_b128 a[36:39], v7 offset:28672\n"
    "s_waitcnt lgkmcnt(9)\n"
    "v_mfma_f32_16x16x32_bf16 v[240:243], a[0:3], v[248:251], 0\n"
    "buffer_load_dwordx4 v1, s[4:7], s21 offen offset:1024 sc1 lds\n"
    "ds_read_b128 a[40:43], v6 offset:35840\n"
    "s_waitcnt lgkmcnt(9)\n"
    "v_mfma_f32_16x16x32_bf16 v[244:247], a[4:7], v[248:251], 0\n"
    "ds_read_b128 a[44:47], v7 offset:35840\n"
    "v_lshlrev_b32_e64 v255, 4, v10\n"
    "v_add_u32 v255, 143360, v255\n"
    "s_lshl_b32 s49, s36, 6\n"
    "s_waitcnt lgkmcnt(9)\n"
    "v_mfma_f32_16x16x32_bf16 v[240:243], a[8:11], v[248:251], v[240:243]\n"
    "ds_read_b128 a[48:51], v6 offset:43008\n"
    "s_waitcnt lgkmcnt(9)\n"
    "v_mfma_f32_16x16x32_bf16 v[244:247], a[12:15], v[248:251], v[244:247]\n"
    "ds_read_b128 a[52:55], v7 offset:43008\n"
    "v_add_u32 v12, s49, v255\n"
    "s_lshl_b32 s49, s50, 6\n"
    "v_add_u32 v13, s49, v255\n"
    "s_waitcnt lgkmcnt(9)\n"
    "v_mfma_f32_16x16x32_bf16 v[240:243], a[16:19], v[248:251], v[240:243]\n"
    "s_add_u32 m0, s20, 92160\n"
    "s_add_u32 s21, s26, 92160\n"
    "buffer_load_dwordx4 v1, s[4:7], s21 offen offset:0 sc1 lds\n"
    "s_waitcnt lgkmcnt(8)\n"
    "v_mfma_f32_16x16x32_bf16 v[244:247], a[20:23], v[248:251], v[244:247]\n"
    "ds_read_b128 a[0:3], v6 offset:50176\n"
    "ds_read_b128 a[4:7], v7 offset:50176\n"
    "s_min_u32 s49, s38, 12\n"
    "s_mul_i32 s50, s38, 6\n"
    "s_add_u32 s49, s49, s50\n"
    "s_waitcnt lgkmcnt(9)\n"
    "v_mfma_f32_16x16x32_bf16 v[240:243], a[24:27], v[248:251], v[240:243]\n"
    "ds_read_b128 a[8:11], v6 offset:57344\n"
    "s_waitcnt lgkmcnt(9)\n"
    "v_mfma_f32_16x16x32_bf16 v[244:247], a[28:31], v[248:251], v[244:247]\n"
    "ds_read_b128 a[12:15], v7 offset:57344\n"
    "s_lshl_b32 s49, s49, 4\n"
    "s_lshl_b32 s50, s36, 6\n"
    "v_add_u32 v254, s50, v0\n"
    "s_waitcnt lgkmcnt(9)\n"
    "v_mfma_f32_16x16x32_bf16 v[240:243], a[32:35], v[248:251], v[240:243]\n"
    "s_add_u32 m0, s20, 114688\n"
    "s_add_u32 s21, s19, 114688\n"
    "buffer_load_dwordx4 v1, s[4:7], s21 offen offset:0 sc1 lds\n"
    "s_waitcnt lgkmcnt(8)\n"
    "v_mfma_f32_16x16x32_bf16 v[244:247], a[36:39], v[248:251], v[244:247]\n"
    "ds_read_b128 a[16:19], v6 offset:64512\n"
    "ds_read_b128 a[20:23], v7 offset:64512\n"
    "v_add_u32_e64 v253, s49, v254\n"
    "v_min_u32 v253, 1727, v253\n"
    "v_lshlrev_b32 v253, 1, v253\n"
    "s_waitcnt lgkmcnt(9)\n"
    "v_mfma_f32_16x16x32_bf16 v[240:243], a[40:43], v[248:251], v[240:243]\n"
    "global_load_ushort v253, v253, s[70:71]\n"
    "v_min_u32 v254, 127, v254\n"
    "s_waitcnt lgkmcnt(8)\n"
    "v_mfma_f32_16x16x32_bf16 v[244:247], a[44:47], v[248:251], v[244:247]\n"
    "v_lshlrev_b32 v254, 2, v254\n"
    "s_waitcnt lgkmcnt(7)\n"
    "v_mfma_f32_16x16x32_bf16 v[240:243], a[48:51], v[248:251], v[240:243]\n"
    "buffer_load_dwordx4 v1, s[4:7], s21 offen offset:1024 sc1 lds\n"
    "v_add_u32 v254, 143872, v254\n"
    "s_waitcnt lgkmcnt(6)\n"
    "v_mfma_f32_16x16x32_bf16 v[244:247], a[52:55], v[248:251], v[244:247]\n"
    "v_add_u32 v3, 20480, v1\n"
    "v_add_u32 v4, 64512, v1\n"
    "s_waitcnt lgkmcnt(5)\n"
    "v_mfma_f32_16x16x32_bf16 v[240:243], a[0:3], v[248:251], v[240:243]\n"
    "v_add_u32 v5, 129024, v1\n"
    "s_lshl2_add_u32 s49, s37, s36\n"
    "s_waitcnt lgkmcnt(4)\n"
    "v_mfma_f32_16x16x32_bf16 v[244:247], a[4:7], v[248:251], v[244:247]\n"
    "s_mul_i32 s60, s49, 40960\n"
    "s_waitcnt lgkmcnt(3)\n"
    "v_mfma_f32_16x16x32_bf16 v[240:243], a[8:11], v[248:251], v[240:243]\n"
    "s_add_u32 s60, s60, 10829824\n"
    "s_lshl_b32 s61, s49, 11\n"
    "s_waitcnt lgkmcnt(2)\n"
    "v_mfma_f32_16x16x32_bf16 v[244:247], a[12:15], v[248:251], v[244:247]\n"
    "s_add_u32 s61, s61, 13451264\n"
    "s_waitcnt lgkmcnt(1)\n"
    "v_mfma_f32_16x16x32_bf16 v[240:243], a[16:19], v[248:251], v[240:243]\n"
    "s_mul_i32 s62, s49, 110592\n"
    "s_add_u32 s62, s62, 13582336\n"
    "s_waitcnt lgkmcnt(0)\n"
    "v_mfma_f32_16x16x32_bf16 v[244:247], a[20:23], v[248:251], v[244:247]\n"
    "s_lshl_b32 s50, s38, 9\n"
    "s_add_u32 s63, s62, s50\n"
    "s_add_u32 s63, s63, 49152\n"
    "s_cmp_lt_u32 s38, 12\n"
    "s_cselect_b32 s63, s63, 1073741824\n"
    "s_mul_i32 s50, s38, 3072\n"
    "s_add_u32 s62, s62, s50\n"
    "s_add_u32 s64, s62, 55296\n"
    "s_mov_b32 s65, 0x3acccccd\n"
    "s_waitcnt vmcnt(1)\n"
    "v_lshlrev_b32_e64 v253, 16, v253\n"
    "ds_write_b32 v254, v253\n"
    "Lg27_%=:\n"
    "s_mov_b32 s66, 65536\n"
    "Lrp29_%=:\n"
    "s_add_u32 s67, s61, 0\n"
    "buffer_load_dwordx2 v[232:233], v2, s[4:7], s67 offen offset:0 sc1\n"
    "s_add_u32 s67, s61, 0\n"
    "buffer_load_dwordx2 v[234:235], v2, s[4:7], s67 offen offset:512 sc1\n"
    "s_add_u32 s67, s61, 0\n"
    "buffer_load_dwordx2 v[236:237], v2, s[4:7], s67 offen offset:1024 sc1\n"
    "s_add_u32 s67, s61, 0\n"
    "buffer_load_dwordx2 v[238:239], v2, s[4:7], s67 offen offset:1536 sc1\n"
    "s_add_u32 s67, s60, 0\n"
    "buffer_load_dwordx4 v[16:19], v1, s[4:7], s67 offen offset:0 sc1\n"
    "s_add_u32 s67, s60, 0\n"
    "buffer_load_dwordx4 v[20:23], v3, s[4:7], s67 offen offset:0 sc1\n"
    "s_waitcnt vmcnt(0)\n"
    "v_max3_u32 v15, v232, v233, v234\n"
    "v_max3_u32 v15, v15, v235, v236\n"
    "v_max3_u32 v15, v15, v237, v238\n"
    "v_max3_u32 v15, v15, v239, v16\n"
    "v_max3_u32 v15, v15, v17, v18\n"
    "v_max3_u32 v15, v15, v19, v20\n"
    "v_max3_u32 v15, v15, v21, v22\n"
    "v_max_u32 v15, v15, v23\n"
    "v_cmp_eq_u32 vcc, -1, v15\n"
    "s_nop 0\n"
    "s_cbranch_vccz Lgb28_%=\n"
    "s_sub_u32 s66, s66, 1\n"
    "s_cbranch_scc0 Lrp29_%=\n"
    "Lgb28_%=:\n"
    "s_add_u32 s46, s60, 0\n"
    "buffer_load_dwordx4 v[24:27], v1, s[4:7], s46 offen offset:1024 sc1\n"
    "buffer_load_dwordx4 v[28:31], v3, s[4:7], s46 offen offset:1024 sc1\n"
    "buffer_load_dwordx4 v[32:35], v1, s[4:7], s46 offen offset:2048 sc1\n"
    "buffer_load_dwordx4 v[36:39], v3, s[4:7], s46 offen offset:2048 sc1\n"
    "buffer_load_dwordx4 v[40:43], v1, s[4:7], s46 offen offset:3072 sc1\n"
    "buffer_load_dwordx4 v[44:47], v3, s[4:7], s46 offen offset:3072 sc1\n"
    "s_add_u32 s46, s60, 4096\n"
    "buffer_load_dwordx4 v[48:51], v1, s[4:7], s46 offen offset:0 sc1\n"
    "buffer_load_dwordx4 v[52:55], v3, s[4:7], s46 offen offset:0 sc1\n"
    "Lcp3_%=:\n"
    "ds_read_b128 a[0:3], v1 offset:0\n"
    "ds_read_b128 a[4:7], v1 offset:1024\n"
    "ds_read_b128 a[8:11], v1 offset:2048\n"
    "ds_read_b128 a[12:15], v1 offset:3072\n"
    "ds_read_b128 a[16:19], v1 offset:4096\n"
    "ds_read_b128 a[20:23], v1 offset:5120\n"
    "ds_read_b128 a[24:27], v1 offset:6144\n"
    "s_waitcnt lgkmcnt(0)\n"
    "v_mfma_f32_16x16x32_bf16 v[176:179], a[0:3], v[16:19], 0\n"
    "buffer_load_dwordx4 v[56:59], v1, s[4:7], s46 offen offset:1024 sc1\n"
    "ds_read_b128 a[28:31], v1 offset:7168\n"
    "v_mfma_f32_16x16x32_bf16 v[204:207], a[0:3], v[20:23], 0\n"
    "ds_read_b128 a[32:35], v1 offset:8192\n"
    "ds_read_b128 a[36:39], v1 offset:9216\n"
    "v_mfma_f32_16x16x32_bf16 v[180:183], a[4:7], v[16:19], 0\n"
    "ds_read_b128 a[40:43], v1 offset:10240\n"
    "ds_read_b128 a[44:47], v1 offset:11264\n"
    "v_mfma_f32_16x16x32_bf16 v[208:211], a[4:7], v[20:23], 0\n"
    "buffer_load_dwordx4 v[60:63], v3, s[4:7], s46 offen offset:1024 sc1\n"
    "ds_read_b128 a[48:51], v1 offset:12288\n"
    "v_mfma_f32_16x16x32_bf16 v[184:187], a[8:11], v[16:19], 0\n"
    "ds_read_b128 a[52:55], v1 offset:13312\n"
    "v_mfma_f32_16x16x32_bf16 v[212:215], a[8:11], v[20:23], 0\n"
    "v_mfma_f32_16x16x32_bf16 v[188:191], a[12:15], v[16:19], 0\n"
    "buffer_load_dwordx4 v[64:67], v1, s[4:7], s46 offen offset:2048 sc1\n"
    "s_waitcnt vmcnt(10)\n"
    "v_max3_u32 v15, v24, v25, v26\n"
    "v_mfma_f32_16x16x32_bf16 v[216:219], a[12:15], v[20:23], 0\n"
    "s_waitcnt vmcnt(9)\n"
    "v_max3_u32 v15, v15, v27, v28\n"
    "v_max3_u32 v15, v15, v29, v30\n"
    "v_mfma_f32_16x16x32_bf16 v[192:195], a[16:19], v[16:19], 0\n"
    "v_max_u32 v15, v15, v31\n"
    "v_cmp_eq_u32_e64 vcc, -1, v15\n"
    "v_mfma_f32_16x16x32_bf16 v[220:223], a[16:19], v[20:23], 0\n"
    "buffer_load_dwordx4 v[68:71], v3, s[4:7], s46 offen offset:2048 sc1\n"
    "v_mfma_f32_16x16x32_bf16 v[196:199], a[20:23], v[16:19], 0\n"
    "s_cbranch_vccnz Lrt30_%=\n"
    "Lbk31_%=:\n"
    "v_mfma_f32_16x16x32_bf16 v[224:227], a[20:23], v[20:23], 0\n"
    "v_mfma_f32_16x16x32_bf16 v[200:203], a[24:27], v[16:19], 0\n"
    "buffer_load_dwordx4 v[72:75], v1, s[4:7], s46 offen offset:3072 sc1\n"
    "v_mfma_f32_16x16x32_bf16 v[228:231], a[24:27], v[20:23], 0\n"
    "s_waitcnt lgkmcnt(0)\n"
    "v_mfma_f32_16x16x32_bf16 v[176:179], a[28:31], v[24:27], v[176:179]\n"
    "ds_read_b128 a[0:3], v1 offset:14336\n"
    "ds_read_b128 a[4:7], v1 offset:15360\n"
    "v_mfma_f32_16x16x32_bf16 v[204:207], a[28:31], v[28:31], v[204:207]\n"
    "buffer_load_dwordx4 v[76:79], v3, s[4:7], s46 offen offset:3072 sc1\n"
    "ds_read_b128 a[8:11], v1 offset:16384\n"
    "v_mfma_f32_16x16x32_bf16 v[180:183], a[32:35], v[24:27], v[180:183]\n"
    "ds_read_b128 a[12:15], v1 offset:17408\n"
    "ds_read_b128 a[16:19], v1 offset:18432\n"
    "v_mfma_f32_16x16x32_bf16 v[208:211], a[32:35], v[28:31], v[208:211]\n"
    "ds_read_b128 a[20:23], v1 offset:19456\n"
    "ds_read_b128 a[24:27], v1 offset:20480\n"
    "v_mfma_f32_16x16x32_bf16 v[184:187], a[36:39], v[24:27], v[184:187]\n"
    "s_add_u32 s46, s60, 8192\n"
    "buffer_load_dwordx4 v[80:83], v1, s[4:7], s46 offen offset:0 sc1\n"
    "v_mfma_f32_16x16x32_bf16 v[212:215], a[36:39], v[28:31], v[212:215]\n"
    "v_mfma_f32_16x16x32_bf16 v[188:191], a[40:43], v[24:27], v[188:191]\n"
    "s_waitcnt vmcnt(12)\n"
    "v_max3_u32 v15, v32, v33, v34\n"
    "s_waitcnt vmcnt(11)\n"
    "v_max3_u32 v15, v15, v35, v36\n"
    "v_mfma_f32_16x16x32_bf16 v[216:219], a[40:43], v[28:31], v[216:219]\n"
    "buffer_load_dwordx4 v[84:87], v3, s[4:7], s46 offen offset:0 sc1\n"
    "v_max3_u32 v15, v15, v37, v38\n"
    "v_mfma_f32_16x16x32_bf16 v[192:195], a[44:47], v[24:27], v[192:195]\n"
    "v_max_u32 v15, v15, v39\n"
    "v_cmp_eq_u32 vcc, -1, v15\n"
    "v_mfma_f32_16x16x32_bf16 v[220:223], a[44:47], v[28:31], v[220:223]\n"
    "s_cbranch_vccnz Lrt33_%=\n"
    "Lbk34_%=:\n"
    "v_mfma_f32_16x16x32_bf16 v[196:199], a[48:51], v[24:27], v[196:199]\n"
    "buffer_load_dwordx4 v[88:91], v1, s[4:7], s46 offen offset:1024 sc1\n"
    "v_mfma_f32_16x16x32_bf16 v[224:227], a[48:51], v[28:31], v[224:227]\n"
    "v_mfma_f32_16x16x32_bf16 v[200:203], a[52:55], v[24:27], v[200:203]\n"
    "v_mfma_f32_16x16x32_bf16 v[228:231], a[52:55], v[28:31], v[228:231]\n"
    "buffer_load_dwordx4 v[92:95], v3, s[4:7], s46 offen offset:1024 sc1\n"
    "s_waitcnt lgkmcnt(0)\n"
    "v_mfma_f32_16x16x32_bf16 v[176:179], a[0:3], v[32:35], v[176:179]\n"
    "ds_read_b128 a[28:31], v1 offset:21504\n"
    "ds_read_b128 a[32:35], v1 offset:22528\n"
    "v_mfma_f32_16x16x32_bf16 v[204:207], a[0:3], v[36:39], v[204:207]\n"
    "ds_read_b128 a[36:39], v1 offset:23552\n"
    "ds_read_b128 a[40:43], v1 offset:24576\n"
    "v_mfma_f32_16x16x32_bf16 v[180:183], a[4:7], v[32:35], v[180:183]\n"
    "buffer_load_dwordx4 v[96:99], v1, s[4:7], s46 offen offset:2048 sc1\n"
    "ds_read_b128 a[44:47], v1 offset:25600\n"
    "v_mfma_f32_16x16x32_bf16 v[208:211], a[4:7], v[36:39], v[208:211]\n"
    "ds_read_b128 a[48:51], v1 offset:26624\n"
    "ds_read_b128 a[52:55], v1 offset:27648\n"
    "v_mfma_f32_16x16x32_bf16 v[184:187], a[8:11], v[32:35], v[184:187]\n"
    "v_mfma_f32_16x16x32_bf16 v[212:215], a[8:11], v[36:39], v[212:215]\n"
    "buffer_load_dwordx4 v[100:103], v3, s[4:7], s46 offen offset:2048 sc1\n"
    "v_mfma_f32_16x16x32_bf16 v[188:191], a[12:15], v[32:35], v[188:191]\n"
    "s_waitcnt vmcnt(15)\n"
    "v_max3_u32 v15, v40, v41, v42\n"
    "s_waitcnt vmcnt(14)\n"
    "v_max3_u32 v15, v15, v43, v44\n"
    "v_mfma_f32_16x16x32_bf16 v[216:219], a[12:15], v[36:39], v[216:219]\n"
    "v_max3_u32 v15, v15, v45, v46\n"
    "v_max_u32_e64 v15, v15, v47\n"
    "v_mfma_f32_16x16x32_bf16 v[192:195], a[16:19], v[32:35], v[192:195]\n"
    "buffer_load_dwordx4 v[104:107], v1, s[4:7], s46 offen offset:3072 sc1\n"
    "v_cmp_eq_u32_e64 vcc, -1, v15\n"
    "v_mfma_f32_16x16x32_bf16 v[220:223], a[16:19], v[36:39], v[220:223]\n"
    "s_cbranch_vccnz Lrt36_%=\n"
    "Lbk37_%=:\n"
    "v_mfma_f32_16x16x32_bf16 v[196:199], a[20:23], v[32:35], v[196:199]\n"
    "v_add_f32 v232, v232, v234\n"
    "v_add_f32_e64 v233, v233, v235\n"
    "v_mfma_f32_16x16x32_bf16 v[224:227], a[20:23], v[36:39], v[224:227]\n"
    "buffer_load_dwordx4 v[108:111], v3, s[4:7], s46 offen offset:3072 sc1\n"
    "v_add_f32_e64 v236, v236, v238\n"
    "v_mfma_f32_16x16x32_bf16 v[200:203], a[24:27], v[32:35], v[200:203]\n"
    "v_add_f32 v237, v237, v239\n"
    "v_permlane32_swap_b32 v232, v233\n"
    "v_mfma_f32_16x16x32_bf16 v[228:231], a[24:27], v[36:39], v[228:231]\n"
    "v_permlane32_swap_b32 v236, v237\n"
    "s_waitcnt lgkmcnt(0)\n"
    "v_mfma_f32_16x16x32_bf16 v[176:179], a[28:31], v[40:43], v[176:179]\n"
    "s_add_u32 s46, s60, 12288\n"
    "buffer_load_dwordx4 v[112:115], v1, s[4:7], s46 offen offset:0 sc1\n"
    "v_mfma_f32_16x16x32_bf16 v[204:207], a[28:31], v[44:47], v[204:207]\n"
    "ds_read_b128 a[0:3], v1 offset:28672\n"
    "ds_read_b128 a[4:7], v1 offset:29696\n"
    "v_mfma_f32_16x16x32_bf16 v[180:183], a[32:35], v[40:43], v[180:183]\n"
    "ds_read_b128 a[8:11], v1 offset:30720\n"
    "ds_read_b128 a[12:15], v1 offset:31744\n"
    "v_mfma_f32_16x16x32_bf16 v[208:211], a[32:35], v[44:47], v[208:211]\n"
    "buffer_load_dwordx4 v[116:119], v3, s[4:7], s46 offen offset:0 sc1\n"
    "ds_read_b128 a[16:19], v1 offset:32768\n"
    "v_mfma_f32_16x16x32_bf16 v[184:187], a[36:39], v[40:43], v[184:187]\n"
    "ds_read_b128 a[20:23], v1 offset:33792\n"
    "ds_read_b128 a[24:27], v1 offset:34816\n"
    "v_mfma_f32_16x16x32_bf16 v[212:215], a[36:39], v[44:47], v[212:215]\n"
    "v_mfma_f32_16x16x32_bf16 v[188:191], a[40:43], v[40:43], v[188:191]\n"
    "buffer_load_dwordx4 v[120:123], v1, s[4:7], s46 offen offset:1024 sc1\n"
    "s_waitcnt vmcnt(18)\n"
    "v_max3_u32 v15, v48, v49, v50\n"
    "v_mfma_f32_16x16x32_bf16 v[216:219], a[40:43], v[44:47], v[216:219]\n"
    "s_waitcnt vmcnt(17)\n"
    "v_max3_u32 v15, v15, v51, v52\n"
    "v_max3_u32 v15, v15, v53, v54\n"
    "v_mfma_f32_16x16x32_bf16 v[192:195], a[44:47], v[40:43], v[192:195]\n"
    "v_max_u32 v15, v15, v55\n"
    "v_cmp_eq_u32 vcc, -1, v15\n"
    "v_mfma_f32_16x16x32_bf16 v[220:223], a[44:47], v[44:47], v[220:223]\n"
    "buffer_load_dwordx4 v[124:127], v3, s[4:7], s46 offen offset:1024 sc1\n"
    "v_mfma_f32_16x16x32_bf16 v[196:199], a[48:51], v[40:43], v[196:199]\n"
    "s_cbranch_vccnz Lrt39_%=\n"
    "Lbk40_%=:\n"
    "v_mfma_f32_16x16x32_bf16 v[224:227], a[48:51], v[44:47], v[224:227]\n"
    "v_add_f32 v232, v232, v233\n"
    "v_add_f32_e64 v236, v236, v237\n"
    "v_mfma_f32_16x16x32_bf16 v[200:203], a[52:55], v[40:43], v[200:203]\n"
    "buffer_load_dwordx4 v[128:131], v1, s[4:7], s46 offen offset:2048 sc1\n"
    "v_mov_b32_e64 v233, v232\n"
    "v_mfma_f32_16x16x32_bf16 v[228:231], a[52:55], v[44:47], v[228:231]\n"
    "v_mov_b32 v237, v236\n"
    "v_permlane16_swap_b32 v232, v233\n"
    "s_nop 0\n"
    "v_permlane16_swap_b32 v236, v237\n"
    "s_waitcnt lgkmcnt(0)\n"
    "v_mfma_f32_16x16x32_bf16 v[176:179], a[0:3], v[48:51], v[176:179]\n"
    "ds_read_b128 a[28:31], v1 offset:35840\n"
    "ds_read_b128 a[32:35], v1 offset:36864\n"
    "v_mfma_f32_16x16x32_bf16 v[204:207], a[0:3], v[52:55], v[204:207]\n"
    "buffer_load_dwordx4 v[132:135], v3, s[4:7], s46 offen offset:2048 sc1\n"
    "ds_read_b128 a[36:39], v1 offset:37888\n"
    "v_mfma_f32_16x16x32_bf16 v[180:183], a[4:7], v[48:51], v[180:183]\n"
    "ds_read_b128 a[40:43], v1 offset:38912\n"
    "ds_read_b128 a[44:47], v1 offset:39936\n"
    "v_mfma_f32_16x16x32_bf16 v[208:211], a[4:7], v[52:55], v[208:211]\n"
    "ds_read_b128 a[48:51], v1 offset:40960\n"
    "ds_read_b128 a[52:55], v1 offset:41984\n"
    "v_mfma_f32_16x16x32_bf16 v[184:187], a[8:11], v[48:51], v[184:187]\n"
    "v_mfma_f32_16x16x32_bf16 v[212:215], a[8:11], v[52:55], v[212:215]\n"
    "buffer_load_dwordx4 v1, s[4:7], s21 offen offset:2048 sc1 lds\n"
    "v_mfma_f32_16x16x32_bf16 v[188:191], a[12:15], v[48:51], v[188:191]\n"
    "s_waitcnt vmcnt(20)\n"
    "v_max3_u32 v15, v56, v57, v58\n"
    "s_waitcnt vmcnt(19)\n"
    "v_max3_u32 v15, v15, v59, v60\n"
    "v_mfma_f32_16x16x32_bf16 v[216:219], a[12:15], v[52:55], v[216:219]\n"
    "v_max3_u32 v15, v15, v61, v62\n"
    "v_max_u32 v15, v15, v63\n"
    "v_mfma_f32_16x16x32_bf16 v[192:195], a[16:19], v[48:51], v[192:195]\n"
    "v_cmp_eq_u32_e64 vcc, -1, v15\n"
    "v_mfma_f32_16x16x32_bf16 v[220:223], a[16:19], v[52:55], v[220:223]\n"
    "buffer_load_dwordx4 v1, s[4:7], s21 offen offset:3072 sc1 lds\n"
    "v_mfma_f32_16x16x32_bf16 v[196:199], a[20:23], v[48:51], v[196:199]\n"
    "s_cbranch_vccnz Lrt42_%=\n"
    "Lbk43_%=:\n"
    "v_mfma_f32_16x16x32_bf16 v[224:227], a[20:23], v[52:55], v[224:227]\n"
    "v_add_f32 v232, v232, v233\n"
    "v_add_f32_e64 v236, v236, v237\n"
    "v_mfma_f32_16x16x32_bf16 v[200:203], a[24:27], v[48:51], v[200:203]\n"
    "v_mov_b32 v233, v232\n"
    "v_mov_b32 v237, v236\n"
    "v_mfma_f32_16x16x32_bf16 v[228:231], a[24:27], v[52:55], v[228:231]\n"
    "s_add_u32 m0, s20, 118784\n"
    "s_add_u32 s21, s19, 118784\n"
    "buffer_load_dwordx4 v1, s[4:7], s21 offen offset:0 sc1 lds\n"
    "v_permlane32_swap_b32 v232, v233\n"
    "v_permlane32_swap_b32 v236, v237\n"
    "s_waitcnt lgkmcnt(0)\n"
    "v_mfma_f32_16x16x32_bf16 v[176:179], a[28:31], v[56:59], v[176:179]\n"
    "ds_read_b128 a[0:3], v1 offset:43008\n"
    "ds_read_b128 a[4:7], v1 offset:44032\n"
    "v_mfma_f32_16x16x32_bf16 v[204:207], a[28:31], v[60:63], v[204:207]\n"
    "ds_read_b128 a[8:11], v1 offset:45056\n"
    "ds_read_b128 a[12:15], v1 offset:46080\n"
    "v_mfma_f32_16x16x32_bf16 v[180:183], a[32:35], v[56:59], v[180:183]\n"
    "ds_read_b128 a[16:19], v1 offset:47104\n"
    "ds_read_b128 a[20:23], v1 offset:48128\n"
    "v_mfma_f32_16x16x32_bf16 v[208:211], a[32:35], v[60:63], v[208:211]\n"
    "buffer_load_dwordx4 v1, s[4:7], s21 offen offset:1024 sc1 lds\n"
    "ds_read_b128 a[24:27], v1 offset:49152\n"
    "v_mfma_f32_16x16x32_bf16 v[184:187], a[36:39], v[56:59], v[184:187]\n"
    "v_mfma_f32_16x16x32_bf16 v[212:215], a[36:39], v[60:63], v[212:215]\n"
    "v_mfma_f32_16x16x32_bf16 v[188:191], a[40:43], v[56:59], v[188:191]\n"
    "s_waitcnt vmcnt(21)\n"
    "v_max3_u32 v15, v64, v65, v66\n"
    "s_waitcnt vmcnt(20)\n"
    "v_max3_u32 v15, v15, v67, v68\n"
    "v_mfma_f32_16x16x32_bf16 v[216:219], a[40:43], v[60:63], v[216:219]\n"
    "s_add_u32 m0, s20, 120832\n"
    "s_add_u32 s21, s26, 120832\n"
    "buffer_load_dwordx4 v1, s[4:7], s21 offen offset:0 sc1 lds\n"
    "v_mfma_f32_16x16x32_bf16 v[192:195], a[44:47], v[56:59], v[192:195]\n"
    "v_max3_u32 v15, v15, v69, v70\n"
    "v_max_u32 v15, v15, v71\n"
    "v_mfma_f32_16x16x32_bf16 v[220:223], a[44:47], v[60:63], v[220:223]\n"
    "v_cmp_eq_u32_e64 vcc, -1, v15\n"
    "v_mfma_f32_16x16x32_bf16 v[196:199], a[48:51], v[56:59], v[196:199]\n"
    "buffer_load_dwordx4 v[136:139], v1, s[4:7], s46 offen offset:3072 sc1\n"
    "v_mfma_f32_16x16x32_bf16 v[224:227], a[48:51], v[60:63], v[224:227]\n"
    "s_cbranch_vccnz Lrt45_%=\n"
    "Lbk46_%=:\n"
    "v_mfma_f32_16x16x32_bf16 v[200:203], a[52:55], v[56:59], v[200:203]\n"
    "v_mul_f32 v234, s65, v232\n"
    "v_mul_f32_e64 v238, s65, v236\n"
    "v_mfma_f32_16x16x32_bf16 v[228:231], a[52:55], v[60:63], v[228:231]\n"
    "buffer_load_dwordx4 v[140:143], v3, s[4:7], s46 offen offset:3072 sc1\n"
    "v_mul_f32 v232, v234, v234\n"
    "v_mul_f32 v236, v238, v238\n"
    "v_fma_f32 v232, v233, s65, -v232\n"
    "v_fma_f32 v236, v237, s65, -v236\n"
    "s_waitcnt lgkmcnt(0)\n"
    "v_mfma_f32_16x16x32_bf16 v[176:179], a[0:3], v[64:67], v[176:179]\n"
    "ds_read_b128 a[28:31], v1 offset:50176\n"
    "ds_read_b128 a[32:35], v1 offset:51200\n"
    "v_mfma_f32_16x16x32_bf16 v[204:207], a[0:3], v[68:71], v[204:207]\n"
    "ds_read_b128 a[36:39], v1 offset:52224\n"
    "ds_read_b128 a[40:43], v1 offset:53248\n"
    "v_mfma_f32_16x16x32_bf16 v[180:183], a[4:7], v[64:67], v[180:183]\n"
    "s_add_u32 s46, s60, 16384\n"
    "buffer_load_dwordx4 v[144:147], v1, s[4:7], s46 offen offset:0 sc1\n"
    "v_mfma_f32_16x16x32_bf16 v[208:211], a[4:7], v[68:71], v[208:211]\n"
    "ds_read_b128 a[44:47], v1 offset:54272\n"
    "ds_read_b128 a[48:51], v1 offset:55296\n"
    "v_mfma_f32_16x16x32_bf16 v[184:187], a[8:11], v[64:67], v[184:187]\n"
    "ds_read_b128 a[52:55], v1 offset:56320\n"
    "v_mfma_f32_16x16x32_bf16 v[212:215], a[8:11], v[68:71], v[212:215]\n"
    "buffer_load_dwordx4 v[148:151], v3, s[4:7], s46 offen offset:0 sc1\n"
    "v_mfma_f32_16x16x32_bf16 v[188:191], a[12:15], v[64:67], v[188:191]\n"
    "s_waitcnt vmcnt(24)\n"
    "v_max3_u32 v15, v72, v73, v74\n"
    "s_waitcnt vmcnt(23)\n"
    "v_max3_u32 v15, v15, v75, v76\n"
    "v_mfma_f32_16x16x32_bf16 v[216:219], a[12:15], v[68:71], v[216:219]\n"
    "v_max3_u32 v15, v15, v77, v78\n"
    "v_max_u32 v15, v15, v79\n"
    "v_mfma_f32_16x16x32_bf16 v[192:195], a[16:19], v[64:67], v[192:195]\n"
    "buffer_load_dwordx4 v[152:155], v1, s[4:7], s46 offen offset:1024 sc1\n"
    "v_cmp_eq_u32_e64 vcc, -1, v15\n"
    "v_mfma_f32_16x16x32_bf16 v[220:223], a[16:19], v[68:71], v[220:223]\n"
    "s_cbranch_vccnz Lrt48_%=\n"
    "Lbk49_%=:\n"
    "v_mfma_f32_16x16x32_bf16 v[196:199], a[20:23], v[64:67], v[196:199]\n"
    "v_max_f32 v232, 0, v232\n"
    "v_max_f32_e64 v236, 0, v236\n"
    "v_mfma_f32_16x16x32_bf16 v[224:227], a[20:23], v[68:71], v[224:227]\n"
    "buffer_load_dwordx4 v[156:159], v3, s[4:7], s46 offen offset:1024 sc1\n"
    "v_add_f32 v232, 0x3727c5ac, v232\n"
    "v_mfma_f32_16x16x32_bf16 v[200:203], a[24:27], v[64:67], v[200:203]\n"
    "v_add_f32 v236, 0x3727c5ac, v236\n"
    "v_rsq_f32_e64 v232, v232\n"
    "v_mfma_f32_16x16x32_bf16 v[228:231], a[24:27], v[68:71], v[228:231]\n"
    "v_rsq_f32 v236, v236\n"
    "s_waitcnt lgkmcnt(0)\n"
    "v_mfma_f32_16x16x32_bf16 v[176:179], a[28:31], v[72:75], v[176:179]\n"
    "buffer_load_dwordx4 v[160:163], v1, s[4:7], s46 offen offset:2048 sc1\n"
    "ds_read_b128 a[0:3], v1 offset:57344\n"
    "v_mfma_f32_16x16x32_bf16 v[204:207], a[28:31], v[76:79], v[204:207]\n"
    "ds_read_b128 a[4:7], v1 offset:58368\n"
    "ds_read_b128 a[8:11], v1 offset:59392\n"
    "v_mfma_f32_16x16x32_bf16 v[180:183], a[32:35], v[72:75], v[180:183]\n"
    "ds_read_b128 a[12:15], v1 offset:60416\n"
    "ds_read_b128 a[16:19], v1 offset:61440\n"
    "v_mfma_f32_16x16x32_bf16 v[208:211], a[32:35], v[76:79], v[208:211]\n"
    "buffer_load_dwordx4 v[164:167], v3, s[4:7], s46 offen offset:2048 sc1\n"
    "ds_read_b128 a[20:23], v1 offset:62464\n"
    "v_mfma_f32_16x16x32_bf16 v[184:187], a[36:39], v[72:75], v[184:187]\n"
    "ds_read_b128 a[24:27], v1 offset:63488\n"
    "v_mfma_f32_16x16x32_bf16 v[212:215], a[36:39], v[76:79], v[212:215]\n"
    "v_mfma_f32_16x16x32_bf16 v[188:191], a[40:43], v[72:75], v[188:191]\n"
    "buffer_load_dwordx4 v[168:171], v1, s[4:7], s46 offen offset:3072 sc1\n"
    "s_waitcnt vmcnt(27)\n"
    "v_max3_u32 v15, v80, v81, v82\n"
    "v_mfma_f32_16x16x32_bf16 v[216:219], a[40:43], v[76:79], v[216:219]\n"
    "s_waitcnt vmcnt(26)\n"
    "v_max3_u32 v15, v15, v83, v84\n"
    "v_max3_u32 v15, v15, v85, v86\n"
    "v_mfma_f32_16x16x32_bf16 v[192:195], a[44:47], v[72:75], v[192:195]\n"
    "v_max_u32 v15, v15, v87\n"
    "v_cmp_eq_u32 vcc, -1, v15\n"
    "v_mfma_f32_16x16x32_bf16 v[220:223], a[44:47], v[76:79], v[220:223]\n"
    "buffer_load_dwordx4 v[172:175], v3, s[4:7], s46 offen offset:3072 sc1\n"
    "v_mfma_f32_16x16x32_bf16 v[196:199], a[48:51], v[72:75], v[196:199]\n"
    "s_cbranch_vccnz Lrt51_%=\n"
    "Lbk52_%=:\n"
    "v_mfma_f32_16x16x32_bf16 v[224:227], a[48:51], v[76:79], v[224:227]\n"
    "v_mul_f32_e64 v233, -v234, v232\n"
    "v_mul_f32_e64 v237, -v238, v236\n"
    "v_mfma_f32_16x16x32_bf16 v[200:203], a[52:55], v[72:75], v[200:203]\n"
    "v_bfe_u32 v6, v0, 4, 1\n"
    "v_mul_u32_u24 v6, 55296, v6\n"
    "v_mfma_f32_16x16x32_bf16 v[228:231], a[52:55], v[76:79], v[228:231]\n"
    "s_add_u32 s67, s95, Lce3_%=-Lpc_%=+0\n"
    "s_min_u32 s67, s67, Lend_%=-Lpc_%=-4\n"
    "v_mov_b32 v253, s67\n"
    "global_load_dword v253, v253, s[98:99]\n"
    "v_lshrrev_b32_e64 v7, 5, v0\n"
    "v_lshl_add_u32 v6, v7, 8, v6\n"
    "s_waitcnt lgkmcnt(0)\n"
    "v_mfma_f32_16x16x32_bf16 v[176:179], a[0:3], v[80:83], v[176:179]\n"
    "ds_read_b128 a[28:31], v4 offset:0\n"
    "ds_read_b128 a[32:35], v4 offset:1024\n"
    "v_mfma_f32_16x16x32_bf16 v[204:207], a[0:3], v[84:87], v[204:207]\n"
    "ds_read_b128 a[36:39], v4 offset:2048\n"
    "ds_read_b128 a[40:43], v4 offset:3072\n"
    "v_mfma_f32_16x16x32_bf16 v[180:183], a[4:7], v[80:83], v[180:183]\n"
    "ds_read_b128 a[44:47], v4 offset:4096\n"
    "ds_read_b128 a[48:51], v4 offset:5120\n"
    "v_mfma_f32_16x16x32_bf16 v[208:211], a[4:7], v[84:87], v[208:211]\n"
    "s_add_u32 s18, s16, 16384\n"
    "buffer_load_dwordx4 a[80:83], v1, s[4:7], s18 offen offset:0 sc1\n"
    "v_mfma_f32_16x16x32_bf16 v[184:187], a[8:11], v[80:83], v[184:187]\n"
    "ds_read_b128 a[52:55], v4 offset:6144\n"
    "v_mfma_f32_16x16x32_bf16 v[212:215], a[8:11], v[84:87], v[212:215]\n"
    "v_mfma_f32_16x16x32_bf16 v[188:191], a[12:15], v[80:83], v[188:191]\n"
    "s_waitcnt vmcnt(28)\n"
    "v_max3_u32 v15, v88, v89, v90\n"
    "s_waitcnt vmcnt(27)\n"
    "v_max3_u32 v15, v15, v91, v92\n"
    "v_mfma_f32_16x16x32_bf16 v[216:219], a[12:15], v[84:87], v[216:219]\n"
    "buffer_load_dwordx4 a[84:87], v1, s[4:7], s18 offen offset:1024 sc1\n"
    "v_max3_u32 v15, v15, v93, v94\n"
    "v_mfma_f32_16x16x32_bf16 v[192:195], a[16:19], v[80:83], v[192:195]\n"
    "v_max_u32 v15, v15, v95\n"
    "v_cmp_eq_u32_e64 vcc, -1, v15\n"
    "v_mfma_f32_16x16x32_bf16 v[220:223], a[16:19], v[84:87], v[220:223]\n"
    "s_cbranch_vccnz Lrt54_%=\n"
    "Lbk55_%=:\n"
    "v_mfma_f32_16x16x32_bf16 v[196:199], a[20:23], v[80:83], v[196:199]\n"
    "ds_read_b128 v[32:35], v9 offset:0\n"
    "ds_read_b128 v[36:39], v11 offset:0\n"
    "v_mfma_f32_16x16x32_bf16 v[224:227], a[20:23], v[84:87], v[224:227]\n"
    "buffer_load_dwordx4 a[88:91], v1, s[4:7], s18 offen offset:2048 sc1\n"
    "v_lshl_add_u32 v14, v8, 4, v6\n"
    "v_mfma_f32_16x16x32_bf16 v[200:203], a[24:27], v[80:83], v[200:203]\n"
    "s_mov_b32 s48, 0xbdd2d3e8\n"
    "s_mov_b32 s49, 0xbdd2d3e8\n"
    "v_mfma_f32_16x16x32_bf16 v[228:231], a[24:27], v[84:87], v[228:231]\n"
    "s_lshl1_add_u32 s50, s39, s40\n"
    "s_mul_i32 s50, s50, 221184\n"
    "s_add_u32 s50, s50, 13582336\n"
    "s_waitcnt lgkmcnt(0)\n"
    "v_mfma_f32_16x16x32_bf16 v[176:179], a[28:31], v[88:91], v[176:179]\n"
    "ds_read_b128 a[0:3], v4 offset:7168\n"
    "ds_read_b128 a[4:7], v4 offset:8192\n"
    "v_mfma_f32_16x16x32_bf16 v[204:207], a[28:31], v[92:95], v[204:207]\n"
    "buffer_load_dwordx4 a[92:95], v1, s[4:7], s18 offen offset:3072 sc1\n"
    "ds_read_b128 a[8:11], v4 offset:9216\n"
    "v_mfma_f32_16x16x32_bf16 v[180:183], a[32:35], v[88:91], v[180:183]\n"
    "ds_read_b128 a[12:15], v4 offset:10240\n"
    "ds_read_b128 a[16:19], v4 offset:11264\n"
    "v_mfma_f32_16x16x32_bf16 v[208:211], a[32:35], v[92:95], v[208:211]\n"
    "ds_read_b128 a[20:23], v4 offset:12288\n"
    "ds_read_b128 a[24:27], v4 offset:13312\n"
    "v_mfma_f32_16x16x32_bf16 v[184:187], a[36:39], v[88:91], v[184:187]\n"
    "v_mfma_f32_16x16x32_bf16 v[212:215], a[36:39], v[92:95], v[212:215]\n"
    "s_add_u32 s18, s16, 28672\n"
    "buffer_load_dwordx4 a[96:99], v1, s[4:7], s18 offen offset:2048 sc1\n"
    "v_mfma_f32_16x16x32_bf16 v[188:191], a[40:43], v[88:91], v[188:191]\n"
    "s_waitcnt vmcnt(30)\n"
    "v_max3_u32 v15, v96, v97, v98\n"
    "s_waitcnt vmcnt(29)\n"
    "v_max3_u32 v15, v15, v99, v100\n"
    "v_mfma_f32_16x16x32_bf16 v[216:219], a[40:43], v[92:95], v[216:219]\n"
    "v_max3_u32 v15, v15, v101, v102\n"
    "v_max_u32 v15, v15, v103\n"
    "v_mfma_f32_16x16x32_bf16 v[192:195], a[44:47], v[88:91], v[192:195]\n"
    "v_cmp_eq_u32_e64 vcc, -1, v15\n"
    "v_mfma_f32_16x16x32_bf16 v[220:223], a[44:47], v[92:95], v[220:223]\n"
    "buffer_load_dwordx4 a[100:103], v1, s[4:7], s18 offen offset:3072 sc1\n"
    "v_mfma_f32_16x16x32_bf16 v[196:199], a[48:51], v[88:91], v[196:199]\n"
    "s_cbranch_vccnz Lrt57_%=\n"
    "Lbk58_%=:\n"
    "v_mfma_f32_16x16x32_bf16 v[224:227], a[48:51], v[92:95], v[224:227]\n"
    "ds_read_b128 v[40:43], v9 offset:7168\n"
    "ds_read_b128 v[44:47], v11 offset:7168\n"
    "v_mfma_f32_16x16x32_bf16 v[200:203], a[52:55], v[88:91], v[200:203]\n"
    "s_add_u32 s52, s36, 1\n"
    "s_and_b32 s52, s52, 3\n"
    "v_mfma_f32_16x16x32_bf16 v[228:231], a[52:55], v[92:95], v[228:231]\n"
    "s_add_u32 s18, s16, 32768\n"
    "buffer_load_dwordx4 a[104:107], v1, s[4:7], s18 offen offset:0 sc1\n"
    "v_mfma_f32_16x16x32_bf16 v[240:243], v[32:35], v[248:251], v[240:243]\n"
    "s_mul_i32 s52, s52, 55296\n"
    "s_add_u32 s53, s36, 2\n"
    "v_mfma_f32_16x16x32_bf16 v[244:247], v[36:39], v[248:251], v[244:247]\n"
    "s_and_b32 s53, s53, 3\n"
    "s_mul_i32 s53, s53, 55296\n"
    "s_waitcnt lgkmcnt(0)\n"
    "v_mfma_f32_16x16x32_bf16 v[176:179], a[0:3], v[96:99], v[176:179]\n"
    "s_waitcnt vmcnt(18)\n"
    "s_barrier\n"
    "v_mfma_f32_16x16x32_bf16 v[204:207], a[0:3], v[100:103], v[204:207]\n"
    "buffer_load_dwordx4 a[108:111], v1, s[4:7], s18 offen offset:1024 sc1\n"
    "ds_read_b128 a[28:31], v4 offset:14336\n"
    "v_mfma_f32_16x16x32_bf16 v[180:183], a[4:7], v[96:99], v[180:183]\n"
    "ds_read_b128 a[32:35], v4 offset:15360\n"
    "ds_read_b128 a[36:39], v4 offset:16384\n"
    "v_mfma_f32_16x16x32_bf16 v[208:211], a[4:7], v[100:103], v[208:211]\n"
    "ds_read_b128 a[40:43], v4 offset:17408\n"
    "ds_read_b128 a[44:47], v4 offset:18432\n"
    "v_mfma_f32_16x16x32_bf16 v[184:187], a[8:11], v[96:99], v[184:187]\n"
    "ds_read_b128 a[48:51], v4 offset:19456\n"
    "ds_read_b128 a[52:55], v4 offset:20480\n"
    "v_mfma_f32_16x16x32_bf16 v[212:215], a[8:11], v[100:103], v[212:215]\n"
    "buffer_load_dwordx4 a[112:115], v1, s[4:7], s18 offen offset:2048 sc1\n"
    "v_mfma_f32_16x16x32_bf16 v[188:191], a[12:15], v[96:99], v[188:191]\n"
    "v_max3_u32 v15, v104, v105, v106\n"
    "v_max3_u32 v15, v15, v107, v108\n"
    "v_mfma_f32_16x16x32_bf16 v[216:219], a[12:15], v[100:103], v[216:219]\n"
    "v_max3_u32 v15, v15, v109, v110\n"
    "v_max_u32_e64 v15, v15, v111\n"
    "v_mfma_f32_16x16x32_bf16 v[192:195], a[16:19], v[96:99], v[192:195]\n"
    "v_cmp_eq_u32_e64 vcc, -1, v15\n"
    "v_mfma_f32_16x16x32_bf16 v[220:223], a[16:19], v[100:103], v[220:223]\n"
    "s_add_u32 s18, s16, 45056\n"
    "buffer_load_dwordx4 a[116:119], v1, s[4:7], s18 offen offset:1024 sc1\n"
    "v_mfma_f32_16x16x32_bf16 v[196:199], a[20:23], v[96:99], v[196:199]\n"
    "s_cbranch_vccnz Lrt60_%=\n"
    "Lbk61_%=:\n"
    "v_mfma_f32_16x16x32_bf16 v[224:227], a[20:23], v[100:103], v[224:227]\n"
    "ds_read_b128 v[16:19], v9 offset:14336\n"
    "ds_read_b128 v[20:23], v11 offset:14336\n"
    "v_mfma_f32_16x16x32_bf16 v[200:203], a[24:27], v[96:99], v[200:203]\n"
    "s_add_u32 s54, s36, 3\n"
    "s_and_b32 s54, s54, 3\n"
    "v_mfma_f32_16x16x32_bf16 v[228:231], a[24:27], v[100:103], v[228:231]\n"
    "buffer_load_dwordx4 a[120:123], v1, s[4:7], s18 offen offset:2048 sc1\n"
    "s_mul_i32 s54, s54, 55296\n"
    "v_mfma_f32_16x16x32_bf16 v[240:243], v[40:43], v[248:251], v[240:243]\n"
    "s_add_u32 s55, s36, 4\n"
    "s_and_b32 s55, s55, 3\n"
    "v_mfma_f32_16x16x32_bf16 v[244:247], v[44:47], v[248:251], v[244:247]\n"
    "s_mul_i32 s55, s55, 55296\n"
    "s_waitcnt lgkmcnt(0)\n"
    "v_mfma_f32_16x16x32_bf16 v[176:179], a[28:31], v[104:107], v[176:179]\n"
    "ds_read_b128 a[0:3], v4 offset:21504\n"
    "ds_read_b128 a[4:7], v4 offset:22528\n"
    "v_mfma_f32_16x16x32_bf16 v[204:207], a[28:31], v[108:111], v[204:207]\n"
    "buffer_load_dwordx4 a[124:127], v1, s[4:7], s18 offen offset:3072 sc1\n"
    "ds_read_b128 a[8:11], v4 offset:23552\n"
    "v_mfma_f32_16x16x32_bf16 v[180:183], a[32:35], v[104:107], v[180:183]\n"
    "ds_read_b128 a[12:15], v4 offset:24576\n"
    "ds_read_b128 a[16:19], v4 offset:25600\n"
    "v_mfma_f32_16x16x32_bf16 v[208:211], a[32:35], v[108:111], v[208:211]\n"
    "ds_read_b128 a[20:23], v4 offset:26624\n"
    "ds_read_b128 a[24:27], v4 offset:27648\n"
    "v_mfma_f32_16x16x32_bf16 v[184:187], a[36:39], v[104:107], v[184:187]\n"
    "v_mfma_f32_16x16x32_bf16 v[212:215], a[36:39], v[108:111], v[212:215]\n"
    "s_add_u32 s18, s16, 49152\n"
    "buffer_load_dwordx4 a[228:231], v1, s[4:7], s18 offen offset:0 sc1\n"
    "v_mfma_f32_16x16x32_bf16 v[188:191], a[40:43], v[104:107], v[188:191]\n"
    "v_max3_u32 v15, v112, v113, v114\n"
    "v_max3_u32 v15, v15, v115, v116\n"
    "v_mfma_f32_16x16x32_bf16 v[216:219], a[40:43], v[108:111], v[216:219]\n"
    "v_max3_u32 v15, v15, v117, v118\n"
    "v_max_u32_e64 v15, v15, v119\n"
    "v_mfma_f32_16x16x32_bf16 v[192:195], a[44:47], v[104:107], v[192:195]\n"
    "v_cmp_eq_u32_e64 vcc, -1, v15\n"
    "v_mfma_f32_16x16x32_bf16 v[220:223], a[44:47], v[108:111], v[220:223]\n"
    "buffer_load_dwordx4 a[232:235], v1, s[4:7], s18 offen offset:1024 sc1\n"
    "v_mfma_f32_16x16x32_bf16 v[196:199], a[48:51], v[104:107], v[196:199]\n"
    "s_cbranch_vccnz Lrt63_%=\n"
    "Lbk64_%=:\n"
    "v_mfma_f32_16x16x32_bf16 v[224:227], a[48:51], v[108:111], v[224:227]\n"
    "ds_read_b128 v[24:27], v9 offset:21504\n"
    "ds_read_b128 v[28:31], v11 offset:21504\n"
    "v_mfma_f32_16x16x32_bf16 v[200:203], a[52:55], v[104:107], v[200:203]\n"
    "s_mul_i32 s44, s36, 12288\n"
    "s_add_u32 s44, s44, s50\n"
    "v_mfma_f32_16x16x32_bf16 v[228:231], a[52:55], v[108:111], v[228:231]\n"
    "s_add_u32 s18, s16, 4096\n"
    "buffer_load_dwordx4 a[236:239], v1, s[4:7], s18 offen offset:1024 sc1\n"
    "v_mfma_f32_16x16x32_bf16 v[240:243], v[16:19], v[248:251], v[240:243]\n"
    "s_lshl_b32 s45, s36, 11\n"
    "s_add_u32 s45, s45, s50\n"
    "v_mfma_f32_16x16x32_bf16 v[244:247], v[20:23], v[248:251], v[244:247]\n"
    "s_add_u32 s45, s45, 49152\n"
    "s_cmp_lt_u32 s36, 3\n"
    "s_cselect_b32 s45, s45, 1073741824\n"
    "s_waitcnt lgkmcnt(0)\n"
    "v_mfma_f32_16x16x32_bf16 v[176:179], a[0:3], v[112:115], v[176:179]\n"
    "ds_read_b128 a[28:31], v4 offset:28672\n"
    "ds_read_b128 a[32:35], v4 offset:29696\n"
    "v_mfma_f32_16x16x32_bf16 v[204:207], a[0:3], v[116:119], v[204:207]\n"
    "buffer_load_dwordx4 a[240:243], v1, s[4:7], s18 offen offset:2048 sc1\n"
    "ds_read_b128 a[36:39], v4 offset:30720\n"
    "v_mfma_f32_16x16x32_bf16 v[180:183], a[4:7], v[112:115], v[180:183]\n"
    "ds_read_b128 a[40:43], v4 offset:31744\n"
    "ds_read_b128 a[44:47], v4 offset:32768\n"
    "v_mfma_f32_16x16x32_bf16 v[208:211], a[4:7], v[116:119], v[208:211]\n"
    "ds_read_b128 a[48:51], v4 offset:33792\n"
    "ds_read_b128 a[52:55], v4 offset:34816\n"
    "v_mfma_f32_16x16x32_bf16 v[184:187], a[8:11], v[112:115], v[184:187]\n"
    "v_mfma_f32_16x16x32_bf16 v[212:215], a[8:11], v[116:119], v[212:215]\n"
    "buffer_load_dwordx4 a[244:247], v1, s[4:7], s18 offen offset:3072 sc1\n"
    "v_mfma_f32_16x16x32_bf16 v[188:191], a[12:15], v[112:115], v[188:191]\n"
    "v_max3_u32 v15, v120, v121, v122\n"
    "v_max3_u32 v15, v15, v123, v124\n"
    "v_mfma_f32_16x16x32_bf16 v[216:219], a[12:15], v[116:119], v[216:219]\n"
    "v_max3_u32 v15, v15, v125, v126\n"
    "v_max_u32 v15, v15, v127\n"
    "v_mfma_f32_16x16x32_bf16 v[192:195], a[16:19], v[112:115], v[192:195]\n"
    "v_cmp_eq_u32_e64 vcc, -1, v15\n"
    "v_mfma_f32_16x16x32_bf16 v[220:223], a[16:19], v[116:119], v[220:223]\n"
    "s_add_u32 s18, s16, 8192\n"
    "buffer_load_dwordx4 a[248:251], v1, s[4:7], s18 offen offset:0 sc1\n"
    "v_mfma_f32_16x16x32_bf16 v[196:199], a[20:23], v[112:115], v[196:199]\n"
    "s_cbranch_vccnz Lrt66_%=\n"
    "Lbk67_%=:\n"
    "v_mfma_f32_16x16x32_bf16 v[224:227], a[20:23], v[116:119], v[224:227]\n"
    "ds_read_b128 v[32:35], v9 offset:28672\n"
    "ds_read_b128 v[36:39], v11 offset:28672\n"
    "v_mfma_f32_16x16x32_bf16 v[200:203], a[24:27], v[112:115], v[200:203]\n"
    "s_mul_i32 s51, s41, 160\n"
    "v_lshlrev_b32 v6, 3, v10\n"
    "v_mfma_f32_16x16x32_bf16 v[228:231], a[24:27], v[116:119], v[228:231]\n"
    "buffer_load_dwordx4 a[252:255], v1, s[4:7], s18 offen offset:1024 sc1\n"
    "v_add_u32_e64 v6, s51, v6\n"
    "v_mfma_f32_16x16x32_bf16 v[240:243], v[24:27], v[248:251], v[240:243]\n"
    "s_mul_i32 s51, s36, 2560\n"
    "s_add_u32 s51, s51, 144384\n"
    "v_mfma_f32_16x16x32_bf16 v[244:247], v[28:31], v[248:251], v[244:247]\n"
    "v_add_u32 v7, s51, v2\n"
    "s_waitcnt lgkmcnt(0)\n"
    "v_mfma_f32_16x16x32_bf16 v[176:179], a[28:31], v[120:123], v[176:179]\n"
    "ds_read_b128 a[0:3], v4 offset:35840\n"
    "ds_read_b128 a[4:7], v4 offset:36864\n"
    "v_mfma_f32_16x16x32_bf16 v[204:207], a[28:31], v[124:127], v[204:207]\n"
    "s_add_u32 s18, s16, 20480\n"
    "buffer_load_dwordx4 a[128:131], v1, s[4:7], s18 offen offset:0 sc1\n"
    "v_mfma_f32_16x16x32_bf16 v[180:183], a[32:35], v[120:123], v[180:183]\n"
    "ds_read_b128 a[8:11], v4 offset:37888\n"
    "ds_read_b128 a[12:15], v4 offset:38912\n"
    "v_mfma_f32_16x16x32_bf16 v[208:211], a[32:35], v[124:127], v[208:211]\n"
    "ds_read_b128 a[16:19], v4 offset:39936\n"
    "ds_read_b128 a[20:23], v4 offset:40960\n"
    "v_mfma_f32_16x16x32_bf16 v[184:187], a[36:39], v[120:123], v[184:187]\n"
    "ds_read_b128 a[24:27], v4 offset:41984\n"
    "v_mfma_f32_16x16x32_bf16 v[212:215], a[36:39], v[124:127], v[212:215]\n"
    "buffer_load_dwordx4 a[132:135], v1, s[4:7], s18 offen offset:1024 sc1\n"
    "v_mfma_f32_16x16x32_bf16 v[188:191], a[40:43], v[120:123], v[188:191]\n"
    "v_max3_u32 v15, v128, v129, v130\n"
    "v_max3_u32 v15, v15, v131, v132\n"
    "v_mfma_f32_16x16x32_bf16 v[216:219], a[40:43], v[124:127], v[216:219]\n"
    "v_max3_u32 v15, v15, v133, v134\n"
    "v_max_u32_e64 v15, v15, v135\n"
    "v_mfma_f32_16x16x32_bf16 v[192:195], a[44:47], v[120:123], v[192:195]\n"
    "v_cmp_eq_u32_e64 vcc, -1, v15\n"
    "v_mfma_f32_16x16x32_bf16 v[220:223], a[44:47], v[124:127], v[220:223]\n"
    "buffer_load_dwordx4 a[136:139], v1, s[4:7], s18 offen offset:2048 sc1\n"
    "v_mfma_f32_16x16x32_bf16 v[196:199], a[48:51], v[120:123], v[196:199]\n"
    "s_cbranch_vccnz Lrt69_%=\n"
    "Lbk70_%=:\n"
    "v_mfma_f32_16x16x32_bf16 v[224:227], a[48:51], v[124:127], v[224:227]\n"
    "ds_read_b128 v[40:43], v9 offset:35840\n"
    "ds_read_b128 v[44:47], v11 offset:35840\n"
    "v_mfma_f32_16x16x32_bf16 v[200:203], a[52:55], v[120:123], v[200:203]\n"
    "v_mfma_f32_16x16x32_bf16 v[228:231], a[52:55], v[124:127], v[228:231]\n"
    "buffer_load_dwordx4 a[140:143], v1, s[4:7], s18 offen offset:3072 sc1\n"
    "v_mfma_f32_16x16x32_bf16 v[240:243], v[32:35], v[248:251], v[240:243]\n"
    "v_mfma_f32_16x16x32_bf16 v[244:247], v[36:39], v[248:251], v[244:247]\n"
    "s_waitcnt lgkmcnt(0)\n"
    "v_mfma_f32_16x16x32_bf16 v[176:179], a[0:3], v[128:131], v[176:179]\n"
    "ds_read_b128 a[28:31], v4 offset:43008\n"
    "ds_read_b128 a[32:35], v4 offset:44032\n"
    "v_mfma_f32_16x16x32_bf16 v[204:207], a[0:3], v[132:135], v[204:207]\n"
    "s_add_u32 s18, s16, 24576\n"
    "buffer_load_dwordx4 a[144:147], v1, s[4:7], s18 offen offset:0 sc1\n"
    "v_mfma_f32_16x16x32_bf16 v[180:183], a[4:7], v[128:131], v[180:183]\n"
    "ds_read_b128 a[36:39], v4 offset:45056\n"
    "ds_read_b128 a[40:43], v4 offset:46080\n"
    "v_mfma_f32_16x16x32_bf16 v[208:211], a[4:7], v[132:135], v[208:211]\n"
    "ds_read_b128 a[44:47], v4 offset:47104\n"
    "ds_read_b128 a[48:51], v4 offset:48128\n"
    "v_mfma_f32_16x16x32_bf16 v[184:187], a[8:11], v[128:131], v[184:187]\n"
    "ds_read_b128 a[52:55], v4 offset:49152\n"
    "v_mfma_f32_16x16x32_bf16 v[212:215], a[8:11], v[132:135], v[212:215]\n"
    "s_add_u32 s18, s16, 32768\n"
    "buffer_load_dwordx4 a[148:151], v1, s[4:7], s18 offen offset:3072 sc1\n"
    "v_mfma_f32_16x16x32_bf16 v[188:191], a[12:15], v[128:131], v[188:191]\n"
    "s_waitcnt vmcnt(35)\n"
    "v_max3_u32 v15, v136, v137, v138\n"
    "s_waitcnt vmcnt(34)\n"
    "v_max3_u32 v15, v15, v139, v140\n"
    "v_mfma_f32_16x16x32_bf16 v[216:219], a[12:15], v[132:135], v[216:219]\n"
    "v_max3_u32 v15, v15, v141, v142\n"
    "v_max_u32_e64 v15, v15, v143\n"
    "v_mfma_f32_16x16x32_bf16 v[192:195], a[16:19], v[128:131], v[192:195]\n"
    "v_cmp_eq_u32_e64 vcc, -1, v15\n"
    "v_mfma_f32_16x16x32_bf16 v[220:223], a[16:19], v[132:135], v[220:223]\n"
    "s_add_u32 s18, s16, 36864\n"
    "buffer_load_dwordx4 a[152:155], v1, s[4:7], s18 offen offset:0 sc1\n"
    "v_mfma_f32_16x16x32_bf16 v[196:199], a[20:23], v[128:131], v[196:199]\n"
    "s_cbranch_vccnz Lrt72_%=\n"
    "Lbk73_%=:\n"
    "v_mfma_f32_16x16x32_bf16 v[224:227], a[20:23], v[132:135], v[224:227]\n"
    "ds_read_b128 v[16:19], v9 offset:43008\n"
    "ds_read_b128 v[20:23], v11 offset:43008\n"
    "v_mfma_f32_16x16x32_bf16 v[200:203], a[24:27], v[128:131], v[200:203]\n"
    "ds_read_b128 v[24:27], v9 offset:50176\n"
    "ds_read_b128 v[28:31], v11 offset:50176\n"
    "v_mfma_f32_16x16x32_bf16 v[228:231], a[24:27], v[132:135], v[228:231]\n"
    "buffer_load_dwordx4 a[156:159], v1, s[4:7], s18 offen offset:1024 sc1\n"
    "v_mfma_f32_16x16x32_bf16 v[240:243], v[40:43], v[248:251], v[240:243]\n"
    "v_mfma_f32_16x16x32_bf16 v[244:247], v[44:47], v[248:251], v[244:247]\n"
    "s_waitcnt lgkmcnt(0)\n"
    "v_mfma_f32_16x16x32_bf16 v[176:179], a[28:31], v[136:139], v[176:179]\n"
    "ds_read_b128 a[0:3], v4 offset:50176\n"
    "ds_read_b128 a[4:7], v4 offset:51200\n"
    "v_mfma_f32_16x16x32_bf16 v[204:207], a[28:31], v[140:143], v[204:207]\n"
    "global_load_dwordx2 v[96:97], v6, s[34:35] offset:0\n"
    "ds_read_b128 a[8:11], v4 offset:52224\n"
    "v_mfma_f32_16x16x32_bf16 v[180:183], a[32:35], v[136:139], v[180:183]\n"
    "ds_read_b128 a[12:15], v4 offset:53248\n"
    "ds_read_b128 a[16:19], v4 offset:54272\n"
    "v_mfma_f32_16x16x32_bf16 v[208:211], a[32:35], v[140:143], v[208:211]\n"
    "ds_read_b128 a[20:23], v4 offset:55296\n"
    "ds_read_b128 a[24:27], v4 offset:56320\n"
    "v_mfma_f32_16x16x32_bf16 v[184:187], a[36:39], v[136:139], v[184:187]\n"
    "v_mfma_f32_16x16x32_bf16 v[212:215], a[36:39], v[140:143], v[212:215]\n"
    "global_load_dwordx2 v[98:99], v6, s[34:35] offset:32\n"
    "v_mfma_f32_16x16x32_bf16 v[188:191], a[40:43], v[136:139], v[188:191]\n"
    "s_waitcnt vmcnt(37)\n"
    "v_max3_u32 v15, v144, v145, v146\n"
    "s_waitcnt vmcnt(36)\n"
    "v_max3_u32 v15, v15, v147, v148\n"
    "v_mfma_f32_16x16x32_bf16 v[216:219], a[40:43], v[140:143], v[216:219]\n"
    "v_max3_u32 v15, v15, v149, v150\n"
    "v_max_u32_e64 v15, v15, v151\n"
    "v_mfma_f32_16x16x32_bf16 v[192:195], a[44:47], v[136:139], v[192:195]\n"
    "v_cmp_eq_u32_e64 vcc, -1, v15\n"
    "v_mfma_f32_16x16x32_bf16 v[220:223], a[44:47], v[140:143], v[220:223]\n"
    "global_load_dwordx2 v[100:101], v6, s[34:35] offset:64\n"
    "v_mfma_f32_16x16x32_bf16 v[196:199], a[48:51], v[136:139], v[196:199]\n"
    "s_cbranch_vccnz Lrt75_%=\n"
    "Lbk76_%=:\n"
    "v_mfma_f32_16x16x32_bf16 v[224:227], a[48:51], v[140:143], v[224:227]\n"
    "ds_read_b128 v[32:35], v9 offset:57344\n"
    "ds_read_b128 v[36:39], v11 offset:57344\n"
    "v_mfma_f32_16x16x32_bf16 v[200:203], a[52:55], v[136:139], v[200:203]\n"
    "ds_read_b128 v[40:43], v9 offset:64512\n"
    "ds_read_b128 v[44:47], v11 offset:64512\n"
    "v_mfma_f32_16x16x32_bf16 v[228:231], a[52:55], v[140:143], v[228:231]\n"
    "global_load_dwordx2 v[102:103], v6, s[34:35] offset:96\n"
    "v_mfma_f32_16x16x32_bf16 v[240:243], v[16:19], v[248:251], v[240:243]\n"
    "v_mfma_f32_16x16x32_bf16 v[244:247], v[20:23], v[248:251], v[244:247]\n"
    "v_mfma_f32_16x16x32_bf16 v[240:243], v[24:27], v[248:251], v[240:243]\n"
    "v_mfma_f32_16x16x32_bf16 v[244:247], v[28:31], v[248:251], v[244:247]\n"
    "global_load_dwordx2 v[104:105], v6, s[34:35] offset:128\n"
    "s_waitcnt lgkmcnt(0)\n"
    "v_mfma_f32_16x16x32_bf16 v[176:179], a[0:3], v[144:147], v[176:179]\n"
    "ds_read_b128 a[28:31], v4 offset:57344\n"
    "ds_read_b128 a[32:35], v4 offset:58368\n"
    "v_mfma_f32_16x16x32_bf16 v[204:207], a[0:3], v[148:151], v[204:207]\n"
    "ds_read_b128 a[36:39], v4 offset:59392\n"
    "ds_read_b128 a[40:43], v4 offset:60416\n"
    "v_mfma_f32_16x16x32_bf16 v[180:183], a[4:7], v[144:147], v[180:183]\n"
    "ds_read_b128 a[44:47], v4 offset:61440\n"
    "ds_read_b128 a[48:51], v4 offset:62464\n"
    "v_mfma_f32_16x16x32_bf16 v[208:211], a[4:7], v[148:151], v[208:211]\n"
    "buffer_load_dwordx4 a[160:163], v1, s[4:7], s18 offen offset:2048 sc1\n"
    "ds_read_b128 a[52:55], v4 offset:63488\n"
    "v_mfma_f32_16x16x32_bf16 v[184:187], a[8:11], v[144:147], v[184:187]\n"
    "v_mfma_f32_16x16x32_bf16 v[212:215], a[8:11], v[148:151], v[212:215]\n"
    "v_mfma_f32_16x16x32_bf16 v[188:191], a[12:15], v[144:147], v[188:191]\n"
    "s_waitcnt vmcnt(39)\n"
    "v_max3_u32 v15, v152, v153, v154\n"
    "s_waitcnt vmcnt(38)\n"
    "v_max3_u32 v15, v15, v155, v156\n"
    "v_mfma_f32_16x16x32_bf16 v[216:219], a[12:15], v[148:151], v[216:219]\n"
    "buffer_load_dwordx4 a[164:167], v1, s[4:7], s18 offen offset:3072 sc1\n"
    "v_max3_u32 v15, v15, v157, v158\n"
    "v_mfma_f32_16x16x32_bf16 v[192:195], a[16:19], v[144:147], v[192:195]\n"
    "v_max_u32 v15, v15, v159\n"
    "v_cmp_eq_u32 vcc, -1, v15\n"
    "v_mfma_f32_16x16x32_bf16 v[220:223], a[16:19], v[148:151], v[220:223]\n"
    "s_cbranch_vccnz Lrt78_%=\n"
    "Lbk79_%=:\n"
    "v_mfma_f32_16x16x32_bf16 v[196:199], a[20:23], v[144:147], v[196:199]\n"
    "ds_read_b64 v[106:107], v7 offset:0\n"
    "ds_read_b64 v[108:109], v7 offset:512\n"
    "v_mfma_f32_16x16x32_bf16 v[224:227], a[20:23], v[148:151], v[224:227]\n"
    "s_add_u32 s18, s16, 49152\n"
    "buffer_load_dwordx4 a[168:171], v1, s[4:7], s18 offen offset:2048 sc1\n"
    "v_mfma_f32_16x16x32_bf16 v[200:203], a[24:27], v[144:147], v[200:203]\n"
    "ds_read_b64 v[110:111], v7 offset:1024\n"
    "ds_read_b64 v[112:113], v7 offset:1536\n"
    "v_mfma_f32_16x16x32_bf16 v[228:231], a[24:27], v[148:151], v[228:231]\n"
    "ds_read_b64 v[114:115], v7 offset:2048\n"
    "v_mfma_f32_16x16x32_bf16 v[240:243], v[32:35], v[248:251], v[240:243]\n"
    "v_mfma_f32_16x16x32_bf16 v[244:247], v[36:39], v[248:251], v[244:247]\n"
    "buffer_load_dwordx4 a[172:175], v1, s[4:7], s18 offen offset:3072 sc1\n"
    "v_mfma_f32_16x16x32_bf16 v[240:243], v[40:43], v[248:251], v[240:243]\n"
    "v_mfma_f32_16x16x32_bf16 v[244:247], v[44:47], v[248:251], v[244:247]\n"
    "s_waitcnt lgkmcnt(5)\n"
    "v_mfma_f32_16x16x32_bf16 v[176:179], a[28:31], v[152:155], v[176:179]\n"
    "ds_read_b128 a[0:3], v5 offset:0\n"
    "ds_read_b128 a[4:7], v5 offset:1024\n"
    "v_mfma_f32_16x16x32_bf16 v[204:207], a[28:31], v[156:159], v[204:207]\n"
    "s_add_u32 s18, s16, 53248\n"
    "buffer_load_dwordx4 a[176:179], v1, s[4:7], s18 offen offset:0 sc1\n"
    "v_mfma_f32_16x16x32_bf16 v[180:183], a[32:35], v[152:155], v[180:183]\n"
    "ds_read_b128 a[8:11], v5 offset:2048\n"
    "ds_read_b128 a[12:15], v5 offset:3072\n"
    "v_mfma_f32_16x16x32_bf16 v[208:211], a[32:35], v[156:159], v[208:211]\n"
    "ds_read_b128 a[16:19], v5 offset:4096\n"
    "ds_read_b128 a[20:23], v5 offset:5120\n"
    "v_mfma_f32_16x16x32_bf16 v[184:187], a[36:39], v[152:155], v[184:187]\n"
    "ds_read_b128 a[24:27], v5 offset:6144\n"
    "v_mfma_f32_16x16x32_bf16 v[212:215], a[36:39], v[156:159], v[212:215]\n"
    "buffer_load_dwordx4 a[180:183], v1, s[4:7], s18 offen offset:1024 sc1\n"
    "v_mfma_f32_16x16x32_bf16 v[188:191], a[40:43], v[152:155], v[188:191]\n"
    "s_waitcnt vmcnt(42)\n"
    "v_max3_u32 v15, v160, v161, v162\n"
    "s_waitcnt vmcnt(41)\n"
    "v_max3_u32 v15, v15, v163, v164\n"
    "v_mfma_f32_16x16x32_bf16 v[216:219], a[40:43], v[156:159], v[216:219]\n"
    "v_max3_u32 v15, v15, v165, v166\n"
    "v_max_u32_e64 v15, v15, v167\n"
    "v_mfma_f32_16x16x32_bf16 v[192:195], a[44:47], v[152:155], v[192:195]\n"
    "v_cmp_eq_u32_e64 vcc, -1, v15\n"
    "v_mfma_f32_16x16x32_bf16 v[220:223], a[44:47], v[156:159], v[220:223]\n"
    "buffer_load_dwordx4 a[184:187], v1, s[4:7], s18 offen offset:2048 sc1\n"
    "v_mfma_f32_16x16x32_bf16 v[196:199], a[48:51], v[152:155], v[196:199]\n"
    "s_cbranch_vccnz Lrt81_%=\n"
    "Lbk82_%=:\n"
    "v_mfma_f32_16x16x32_bf16 v[224:227], a[48:51], v[156:159], v[224:227]\n"
    "ds_read_b128 v[120:123], v255 offset:512\n"
    "ds_read_b128 v[124:127], v255 offset:576\n"
    "v_mfma_f32_16x16x32_bf16 v[200:203], a[52:55], v[152:155], v[200:203]\n"
    "ds_read_b128 v[128:131], v255 offset:640\n"
    "ds_read_b128 v[132:135], v255 offset:704\n"
    "v_mfma_f32_16x16x32_bf16 v[228:231], a[52:55], v[156:159], v[228:231]\n"
    "s_add_u32 s18, s16, 8192\n"
    "buffer_load_dwordx4 a[188:191], v1, s[4:7], s18 offen offset:2048 sc1\n"
    "ds_read_b128 v[136:139], v255 offset:768\n"
    "ds_read_b128 v[140:143], v255 offset:832\n"
    "ds_read_b128 v[144:147], v255 offset:896\n"
    "ds_write_b128 v12, v[240:243]\n"
    "ds_write_b128 v13, v[244:247]\n"
    "s_waitcnt lgkmcnt(9)\n"
    "v_mfma_f32_16x16x32_bf16 v[176:179], a[0:3], v[160:163], v[176:179]\n"
    "s_waitcnt lgkmcnt(0)\n"
    "s_barrier\n"
    "v_mfma_f32_16x16x32_bf16 v[204:207], a[0:3], v[164:167], v[204:207]\n"
    "ds_read_b128 a[28:31], v5 offset:7168\n"
    "ds_read_b128 a[32:35], v5 offset:8192\n"
    "v_mfma_f32_16x16x32_bf16 v[180:183], a[4:7], v[160:163], v[180:183]\n"
    "ds_read_b128 a[36:39], v5 offset:9216\n"
    "ds_read_b128 a[40:43], v5 offset:10240\n"
    "v_mfma_f32_16x16x32_bf16 v[208:211], a[4:7], v[164:167], v[208:211]\n"
    "buffer_load_dwordx4 a[192:195], v1, s[4:7], s18 offen offset:3072 sc1\n"
    "ds_read_b128 a[44:47], v5 offset:11264\n"
    "v_mfma_f32_16x16x32_bf16 v[184:187], a[8:11], v[160:163], v[184:187]\n"
    "ds_read_b128 a[48:51], v5 offset:12288\n"
    "ds_read_b128 a[52:55], v5 offset:13312\n"
    "v_mfma_f32_16x16x32_bf16 v[212:215], a[8:11], v[164:167], v[212:215]\n"
    "v_mfma_f32_16x16x32_bf16 v[188:191], a[12:15], v[160:163], v[188:191]\n"
    "s_waitcnt vmcnt(43)\n"
    "v_max3_u32 v15, v168, v169, v170\n"
    "s_waitcnt vmcnt(42)\n"
    "v_max3_u32 v15, v15, v171, v172\n"
    "v_mfma_f32_16x16x32_bf16 v[216:219], a[12:15], v[164:167], v[216:219]\n"
    "s_add_u32 s18, s16, 12288\n"
    "buffer_load_dwordx4 a[196:199], v1, s[4:7], s18 offen offset:0 sc1\n"
    "v_mfma_f32_16x16x32_bf16 v[192:195], a[16:19], v[160:163], v[192:195]\n"
    "v_max3_u32 v15, v15, v173, v174\n"
    "v_max_u32_e64 v15, v15, v175\n"
    "v_mfma_f32_16x16x32_bf16 v[220:223], a[16:19], v[164:167], v[220:223]\n"
    "v_cmp_eq_u32_e64 vcc, -1, v15\n"
    "v_mfma_f32_16x16x32_bf16 v[196:199], a[20:23], v[160:163], v[196:199]\n"
    "s_cbranch_vccnz Lrt84_%=\n"
    "Lbk85_%=:\n"
    "v_mfma_f32_16x16x32_bf16 v[224:227], a[20:23], v[164:167], v[224:227]\n"
    "buffer_load_dwordx4 a[200:203], v1, s[4:7], s18 offen offset:1024 sc1\n"
    "ds_read_b128 v[64:67], v255 offset:0\n"
    "v_mfma_f32_16x16x32_bf16 v[200:203], a[24:27], v[160:163], v[200:203]\n"
    "ds_read_b128 v[68:71], v255 offset:64\n"
    "ds_read_b128 v[72:75], v255 offset:128\n"
    "v_mfma_f32_16x16x32_bf16 v[228:231], a[24:27], v[164:167], v[228:231]\n"
    "ds_read_b128 v[76:79], v255 offset:192\n"
    "ds_read_b128 v[80:83], v255 offset:256\n"
    "ds_read_b128 v[84:87], v255 offset:320\n"
    "ds_read_b128 v[88:91], v255 offset:384\n"
    "s_waitcnt lgkmcnt(7)\n"
    "v_mfma_f32_16x16x32_bf16 v[176:179], a[28:31], v[168:171], v[176:179]\n"
    "v_mov_b32 v240, 0xc0135761\n"
    "v_add_u32_e64 v252, s52, v1\n"
    "v_mfma_f32_16x16x32_bf16 v[204:207], a[28:31], v[172:175], v[204:207]\n"
    "buffer_load_dwordx4 a[204:207], v1, s[4:7], s18 offen offset:2048 sc1\n"
    "s_waitcnt vmcnt(44)\n"
    "v_add_u32 v253, s53, v1\n"
    "v_mfma_f32_16x16x32_bf16 v[180:183], a[32:35], v[168:171], v[180:183]\n"
    "v_add_u32 v254, s54, v1\n"
    "v_add_u32 v255, s55, v1\n"
    "v_mfma_f32_16x16x32_bf16 v[208:211], a[32:35], v[172:175], v[208:211]\n"
    "v_mfma_f32_16x16x32_bf16 v[184:187], a[36:39], v[168:171], v[184:187]\n"
    "v_mfma_f32_16x16x32_bf16 v[212:215], a[36:39], v[172:175], v[212:215]\n"
    "s_add_u32 s18, s16, 24576\n"
    "buffer_load_dwordx4 a[208:211], v1, s[4:7], s18 offen offset:1024 sc1\n"
    "v_mfma_f32_16x16x32_bf16 v[188:191], a[40:43], v[168:171], v[188:191]\n"
    "v_mfma_f32_16x16x32_bf16 v[216:219], a[40:43], v[172:175], v[216:219]\n"
    "v_mfma_f32_16x16x32_bf16 v[192:195], a[44:47], v[168:171], v[192:195]\n"
    "v_mfma_f32_16x16x32_bf16 v[220:223], a[44:47], v[172:175], v[220:223]\n"
    "buffer_load_dwordx4 a[212:215], v1, s[4:7], s18 offen offset:2048 sc1\n"
    "v_mfma_f32_16x16x32_bf16 v[196:199], a[48:51], v[168:171], v[196:199]\n"
    "v_mfma_f32_16x16x32_bf16 v[224:227], a[48:51], v[172:175], v[224:227]\n"
    "v_mfma_f32_16x16x32_bf16 v[200:203], a[52:55], v[168:171], v[200:203]\n"
    "v_mfma_f32_16x16x32_bf16 v[228:231], a[52:55], v[172:175], v[228:231]\n"
    "buffer_load_dwordx4 a[216:219], v1, s[4:7], s18 offen offset:3072 sc1\n"
    "Lce3_%=:\n"
    "s_waitcnt lgkmcnt(6)\n"
    "v_pk_fma_f32 v[48:49], v[64:65], v[232:233], v[120:121] op_sel:[0,1,0] op_sel_hi:[1,1,1]\n"
    "s_add_u32 s18, s16, 28672\n"
    "buffer_load_dwordx4 a[220:223], v1, s[4:7], s18 offen offset:0 sc1\n"
    "v_pk_fma_f32 v[50:51], v[66:67], v[232:233], v[122:123] op_sel:[0,1,0] op_sel_hi:[1,1,1]\n"
    "s_waitcnt lgkmcnt(5)\n"
    "v_pk_fma_f32 v[56:57], v[68:69], v[232:233], v[124:125] op_sel:[0,1,0] op_sel_hi:[1,1,1]\n"
    "v_pk_fma_f32 v[58:59], v[70:71], v[232:233], v[126:127] op_sel:[0,1,0] op_sel_hi:[1,1,1]\n"
    "v_pk_fma_f32 v[176:177], v[176:177], v[232:233], v[48:49] op_sel_hi:[1,0,1]\n"
    "v_pk_fma_f32 v[178:179], v[178:179], v[232:233], v[50:51] op_sel_hi:[1,0,1]\n"
    "v_pk_fma_f32 v[52:53], v[64:65], v[236:237], v[120:121] op_sel:[0,1,0] op_sel_hi:[1,1,1]\n"
    "v_pk_fma_f32 v[54:55], v[66:67], v[236:237], v[122:123] op_sel:[0,1,0] op_sel_hi:[1,1,1]\n"
    "v_pk_fma_f32 v[180:181], v[180:181], v[232:233], v[56:57] op_sel_hi:[1,0,1]\n"
    "v_pk_fma_f32 v[182:183], v[182:183], v[232:233], v[58:59] op_sel_hi:[1,0,1]\n"
    "v_pk_fma_f32 v[60:61], v[68:69], v[236:237], v[124:125] op_sel:[0,1,0] op_sel_hi:[1,1,1]\n"
    "v_pk_fma_f32 v[62:63], v[70:71], v[236:237], v[126:127] op_sel:[0,1,0] op_sel_hi:[1,1,1]\n"
    "v_pk_mul_f32 v[48:49], v[176:177], v[176:177]\n"
    "v_pk_mul_f32 v[50:51], v[178:179], v[178:179]\n"
    "v_pk_fma_f32 v[204:205], v[204:205], v[236:237], v[52:53] op_sel_hi:[1,0,1]\n"
    "v_pk_fma_f32 v[206:207], v[206:207], v[236:237], v[54:55] op_sel_hi:[1,0,1]\n"
    "v_pk_mul_f32 v[56:57], v[180:181], v[180:181]\n"
    "v_pk_mul_f32 v[58:59], v[182:183], v[182:183]\n"
    "v_pk_fma_f32 v[208:209], v[208:209], v[236:237], v[60:61] op_sel_hi:[1,0,1]\n"
    "v_pk_fma_f32 v[210:211], v[210:211], v[236:237], v[62:63] op_sel_hi:[1,0,1]\n"
    "v_pk_fma_f32 v[48:49], v[48:49], s[48:49], v[240:241] op_sel_hi:[1,1,0]\n"
    "buffer_load_dwordx4 a[224:227], v1, s[4:7], s18 offen offset:1024 sc1\n"
    "v_pk_fma_f32 v[50:51], v[50:51], s[48:49], v[240:241] op_sel_hi:[1,1,0]\n"
    "v_pk_mul_f32 v[52:53], v[204:205], v[204:205]\n"
    "v_pk_mul_f32 v[54:55], v[206:207], v[206:207]\n"
    "v_pk_fma_f32 v[56:57], v[56:57], s[48:49], v[240:241] op_sel_hi:[1,1,0]\n"
    "v_pk_fma_f32 v[58:59], v[58:59], s[48:49], v[240:241] op_sel_hi:[1,1,0]\n"
    "v_pk_mul_f32 v[60:61], v[208:209], v[208:209]\n"
    "v_pk_mul_f32 v[62:63], v[210:211], v[210:211]\n"
    "v_pk_mul_f32 v[48:49], v[48:49], v[176:177]\n"
    "v_pk_mul_f32 v[50:51], v[50:51], v[178:179]\n"
    "v_pk_fma_f32 v[52:53], v[52:53], s[48:49], v[240:241] op_sel_hi:[1,1,0]\n"
    "v_pk_fma_f32 v[54:55], v[54:55], s[48:49], v[240:241] op_sel_hi:[1,1,0]\n"
    "v_pk_mul_f32 v[56:57], v[56:57], v[180:181]\n"
    "v_pk_mul_f32 v[58:59], v[58:59], v[182:183]\n"
    "v_pk_fma_f32 v[60:61], v[60:61], s[48:49], v[240:241] op_sel_hi:[1,1,0]\n"
    "v_pk_fma_f32 v[62:63], v[62:63], s[48:49], v[240:241] op_sel_hi:[1,1,0]\n"
    "v_exp_f32 v48, v48\n"
    "v_exp_f32 v49, v49\n"
    "v_exp_f32_e64 v50, v50\n"
    "s_add_u32 s18, s16, 40960\n"
    "buffer_load_dwordx4 a[0:3], v1, s[4:7], s18 offen offset:0 sc1\n"
    "v_exp_f32_e64 v51, v51\n"
    "v_pk_mul_f32 v[52:53], v[52:53], v[204:205]\n"
    "v_pk_mul_f32 v[54:55], v[54:55], v[206:207]\n"
    "v_exp_f32 v56, v56\n"
    "v_exp_f32 v57, v57\n"
    "v_exp_f32 v58, v58\n"
    "v_exp_f32 v59, v59\n"
    "v_pk_mul_f32 v[60:61], v[60:61], v[208:209]\n"
    "v_pk_mul_f32 v[62:63], v[62:63], v[210:211]\n"
    "v_exp_f32 v52, v52\n"
    "v_exp_f32 v53, v53\n"
    "v_exp_f32_e64 v54, v54\n"
    "buffer_load_dwordx4 a[4:7], v1, s[4:7], s18 offen offset:1024 sc1\n"
    "v_exp_f32 v55, v55\n"
    "v_exp_f32 v60, v60\n"
    "v_exp_f32 v61, v61\n"
    "v_exp_f32 v62, v62\n"
    "v_exp_f32_e64 v63, v63\n"
    "v_pk_add_f32 v[48:49], v[48:49], 1.0 op_sel_hi:[1,0]\n"
    "v_pk_add_f32 v[50:51], v[50:51], 1.0 op_sel_hi:[1,0]\n"
    "v_pk_add_f32 v[56:57], v[56:57], 1.0 op_sel_hi:[1,0]\n"
    "v_pk_add_f32 v[58:59], v[58:59], 1.0 op_sel_hi:[1,0]\n"
    "v_rcp_f32 v48, v48\n"
    "v_rcp_f32 v49, v49\n"
    "v_rcp_f32_e64 v50, v50\n"
    "buffer_load_dwordx4 a[8:11], v1, s[4:7], s18 offen offset:2048 sc1\n"
    "v_rcp_f32_e64 v51, v51\n"
    "v_pk_add_f32 v[52:53], v[52:53], 1.0 op_sel_hi:[1,0]\n"
    "v_pk_add_f32 v[54:55], v[54:55], 1.0 op_sel_hi:[1,0]\n"
    "v_rcp_f32 v56, v56\n"
    "v_rcp_f32 v57, v57\n"
    "v_rcp_f32 v58, v58\n"
    "v_rcp_f32 v59, v59\n"
    "v_pk_add_f32 v[60:61], v[60:61], 1.0 op_sel_hi:[1,0]\n"
    "v_pk_add_f32 v[62:63], v[62:63], 1.0 op_sel_hi:[1,0]\n"
    "v_rcp_f32 v52, v52\n"
    "v_rcp_f32 v53, v53\n"
    "v_rcp_f32_e64 v54, v54\n"
    "buffer_load_dwordx4 a[12:15], v1, s[4:7], s18 offen offset:3072 sc1\n"
    "v_rcp_f32 v55, v55\n"
    "v_rcp_f32 v60, v60\n"
    "v_rcp_f32 v61, v61\n"
    "v_rcp_f32 v62, v62\n"
    "v_rcp_f32_e64 v63, v63\n"
    "v_pk_mul_f32 v[176:177], v[176:177], v[48:49]\n"
    "v_pk_mul_f32 v[178:179], v[178:179], v[50:51]\n"
    "v_pk_mul_f32 v[180:181], v[180:181], v[56:57]\n"
    "v_pk_mul_f32 v[182:183], v[182:183], v[58:59]\n"
    "v_cvt_pk_bf16_f32 v16, v176, v177\n"
    "v_cvt_pk_bf16_f32 v17, v178, v179\n"
    "v_pk_mul_f32 v[204:205], v[204:205], v[52:53]\n"
    "v_pk_mul_f32 v[206:207], v[206:207], v[54:55]\n"
    "v_cvt_pk_bf16_f32 v18, v180, v181\n"
    "v_cvt_pk_bf16_f32 v19, v182, v183\n"
    "s_add_u32 s18, s16, 45056\n"
    "buffer_load_dwordx4 a[16:19], v1, s[4:7], s18 offen offset:0 sc1\n"
    "v_pk_mul_f32 v[208:209], v[208:209], v[60:61]\n"
    "v_pk_mul_f32 v[210:211], v[210:211], v[62:63]\n"
    "v_min_u32 v16, -2, v16\n"
    "v_min_u32 v17, -2, v17\n"
    "v_cvt_pk_bf16_f32 v28, v204, v205\n"
    "v_cvt_pk_bf16_f32 v29, v206, v207\n"
    "v_min_u32 v18, -2, v18\n"
    "v_min_u32 v19, -2, v19\n"
    "v_cvt_pk_bf16_f32 v30, v208, v209\n"
    "v_cvt_pk_bf16_f32 v31, v210, v211\n"
    "v_min_u32 v28, -2, v28\n"
    "v_min_u32 v29, -2, v29\n"
    "buffer_store_dwordx4 v[16:19], v1, s[4:7], s62 offen offset:0 sc1\n"
    "v_min_u32 v30, -2, v30\n"
    "v_min_u32 v31, -2, v31\n"
    "buffer_store_dwordx4 v[28:31], v1, s[4:7], s64 offen offset:0 sc1\n"
    "s_waitcnt lgkmcnt(4)\n"
    "v_pk_fma_f32 v[48:49], v[72:73], v[232:233], v[128:129] op_sel:[0,1,0] op_sel_hi:[1,1,1]\n"
    "s_add_u32 s18, s16, 53248\n"
    "buffer_load_dwordx4 a[20:23], v1, s[4:7], s18 offen offset:3072 sc1\n"
    "v_pk_fma_f32 v[50:51], v[74:75], v[232:233], v[130:131] op_sel:[0,1,0] op_sel_hi:[1,1,1]\n"
    "s_waitcnt lgkmcnt(3)\n"
    "v_pk_fma_f32 v[56:57], v[76:77], v[232:233], v[132:133] op_sel:[0,1,0] op_sel_hi:[1,1,1]\n"
    "v_pk_fma_f32 v[58:59], v[78:79], v[232:233], v[134:135] op_sel:[0,1,0] op_sel_hi:[1,1,1]\n"
    "v_pk_fma_f32 v[184:185], v[184:185], v[232:233], v[48:49] op_sel_hi:[1,0,1]\n"
    "v_pk_fma_f32 v[186:187], v[186:187], v[232:233], v[50:51] op_sel_hi:[1,0,1]\n"
    "v_pk_fma_f32 v[52:53], v[72:73], v[236:237], v[128:129] op_sel:[0,1,0] op_sel_hi:[1,1,1]\n"
    "v_pk_fma_f32 v[54:55], v[74:75], v[236:237], v[130:131] op_sel:[0,1,0] op_sel_hi:[1,1,1]\n"
    "v_pk_fma_f32 v[188:189], v[188:189], v[232:233], v[56:57] op_sel_hi:[1,0,1]\n"
    "v_pk_fma_f32 v[190:191], v[190:191], v[232:233], v[58:59] op_sel_hi:[1,0,1]\n"
    "v_pk_fma_f32 v[60:61], v[76:77], v[236:237], v[132:133] op_sel:[0,1,0] op_sel_hi:[1,1,1]\n"
    "v_pk_fma_f32 v[62:63], v[78:79], v[236:237], v[134:135] op_sel:[0,1,0] op_sel_hi:[1,1,1]\n"
    "v_pk_mul_f32 v[48:49], v[184:185], v[184:185]\n"
    "v_pk_mul_f32 v[50:51], v[186:187], v[186:187]\n"
    "v_pk_fma_f32 v[212:213], v[212:213], v[236:237], v[52:53] op_sel_hi:[1,0,1]\n"
    "v_pk_fma_f32 v[214:215], v[214:215], v[236:237], v[54:55] op_sel_hi:[1,0,1]\n"
    "v_pk_mul_f32 v[56:57], v[188:189], v[188:189]\n"
    "v_pk_mul_f32 v[58:59], v[190:191], v[190:191]\n"
    "v_pk_fma_f32 v[216:217], v[216:217], v[236:237], v[60:61] op_sel_hi:[1,0,1]\n"
    "v_pk_fma_f32 v[218:219], v[218:219], v[236:237], v[62:63] op_sel_hi:[1,0,1]\n"
    "v_pk_fma_f32 v[48:49], v[48:49], s[48:49], v[240:241] op_sel_hi:[1,1,0]\n"
    "v_pk_fma_f32 v[50:51], v[50:51], s[48:49], v[240:241] op_sel_hi:[1,1,0]\n"
    "v_pk_mul_f32 v[52:53], v[212:213], v[212:213]\n"
    "v_pk_mul_f32 v[54:55], v[214:215], v[214:215]\n"
    "v_pk_fma_f32 v[56:57], v[56:57], s[48:49], v[240:241] op_sel_hi:[1,1,0]\n"
    "v_pk_fma_f32 v[58:59], v[58:59], s[48:49], v[240:241] op_sel_hi:[1,1,0]\n"
    "v_pk_mul_f32 v[60:61], v[216:217], v[216:217]\n"
    "v_pk_mul_f32 v[62:63], v[218:219], v[218:219]\n"
    "v_pk_mul_f32 v[48:49], v[48:49], v[184:185]\n"
    "v_pk_mul_f32 v[50:51], v[50:51], v[186:187]\n"
    "v_pk_fma_f32 v[52:53], v[52:53], s[48:49], v[240:241] op_sel_hi:[1,1,0]\n"
    "v_pk_fma_f32 v[54:55], v[54:55], s[48:49], v[240:241] op_sel_hi:[1,1,0]\n"
    "v_pk_mul_f32 v[56:57], v[56:57], v[188:189]\n"
    "s_add_u32 s18, s16, 57344\n"
    "buffer_load_dwordx4 a[24:27], v1, s[4:7], s18 offen offset:0 sc1\n"
    "v_pk_mul_f32 v[58:59], v[58:59], v[190:191]\n"
    "v_pk_fma_f32 v[60:61], v[60:61], s[48:49], v[240:241] op_sel_hi:[1,1,0]\n"
    "v_pk_fma_f32 v[62:63], v[62:63], s[48:49], v[240:241] op_sel_hi:[1,1,0]\n"
    "v_exp_f32 v48, v48\n"
    "v_exp_f32 v49, v49\n"
    "v_exp_f32 v50, v50\n"
    "v_exp_f32 v51, v51\n"
    "v_pk_mul_f32 v[52:53], v[52:53], v[212:213]\n"
    "v_pk_mul_f32 v[54:55], v[54:55], v[214:215]\n"
    "v_exp_f32 v56, v56\n"
    "v_exp_f32 v57, v57\n"
    "v_exp_f32 v58, v58\n"
    "v_exp_f32 v59, v59\n"
    "v_pk_mul_f32 v[60:61], v[60:61], v[216:217]\n"
    "v_pk_mul_f32 v[62:63], v[62:63], v[218:219]\n"
    "v_exp_f32 v52, v52\n"
    "v_exp_f32 v53, v53\n"
    "v_exp_f32 v54, v54\n"
    "v_exp_f32 v55, v55\n"
    "v_exp_f32_e64 v60, v60\n"
    "buffer_load_dwordx4 a[28:31], v1, s[4:7], s18 offen offset:1024 sc1\n"
    "v_exp_f32 v61, v61\n"
    "v_exp_f32 v62, v62\n"
    "v_exp_f32_e64 v63, v63\n"
    "v_pk_add_f32 v[48:49], v[48:49], 1.0 op_sel_hi:[1,0]\n"
    "v_pk_add_f32 v[50:51], v[50:51], 1.0 op_sel_hi:[1,0]\n"
    "v_pk_add_f32 v[56:57], v[56:57], 1.0 op_sel_hi:[1,0]\n"
    "v_pk_add_f32 v[58:59], v[58:59], 1.0 op_sel_hi:[1,0]\n"
    "v_rcp_f32 v48, v48\n"
    "v_rcp_f32 v49, v49\n"
    "v_rcp_f32 v50, v50\n"
    "v_rcp_f32 v51, v51\n"
    "v_pk_add_f32 v[52:53], v[52:53], 1.0 op_sel_hi:[1,0]\n"
    "v_pk_add_f32 v[54:55], v[54:55], 1.0 op_sel_hi:[1,0]\n"
    "v_rcp_f32 v56, v56\n"
    "v_rcp_f32 v57, v57\n"
    "v_rcp_f32 v58, v58\n"
    "v_rcp_f32 v59, v59\n"
    "v_pk_add_f32 v[60:61], v[60:61], 1.0 op_sel_hi:[1,0]\n"
    "v_pk_add_f32 v[62:63], v[62:63], 1.0 op_sel_hi:[1,0]\n"
    "v_rcp_f32_e64 v52, v52\n"
    "buffer_load_dwordx4 a[32:35], v1, s[4:7], s18 offen offset:2048 sc1\n"
    "v_rcp_f32 v53, v53\n"
    "v_rcp_f32 v54, v54\n"
    "v_rcp_f32 v55, v55\n"
    "v_rcp_f32 v60, v60\n"
    "v_rcp_f32 v61, v61\n"
    "v_rcp_f32 v62, v62\n"
    "v_rcp_f32_e64 v63, v63\n"
    "v_pk_mul_f32 v[184:185], v[184:185], v[48:49]\n"
    "v_pk_mul_f32 v[186:187], v[186:187], v[50:51]\n"
    "v_pk_mul_f32 v[188:189], v[188:189], v[56:57]\n"
    "v_pk_mul_f32 v[190:191], v[190:191], v[58:59]\n"
    "v_cvt_pk_bf16_f32 v20, v184, v185\n"
    "v_cvt_pk_bf16_f32 v21, v186, v187\n"
    "v_pk_mul_f32 v[212:213], v[212:213], v[52:53]\n"
    "v_pk_mul_f32 v[214:215], v[214:215], v[54:55]\n"
    "v_cvt_pk_bf16_f32 v22, v188, v189\n"
    "v_cvt_pk_bf16_f32 v23, v190, v191\n"
    "v_pk_mul_f32 v[216:217], v[216:217], v[60:61]\n"
    "v_pk_mul_f32 v[218:219], v[218:219], v[62:63]\n"
    "v_min_u32 v20, -2, v20\n"
    "v_min_u32 v21, -2, v21\n"
    "v_cvt_pk_bf16_f32 v32, v212, v213\n"
    "v_cvt_pk_bf16_f32 v33, v214, v215\n"
    "v_min_u32 v22, -2, v22\n"
    "v_min_u32 v23, -2, v23\n"
    "buffer_load_dwordx4 a[36:39], v1, s[4:7], s18 offen offset:3072 sc1\n"
    "v_cvt_pk_bf16_f32 v34, v216, v217\n"
    "v_cvt_pk_bf16_f32 v35, v218, v219\n"
    "v_min_u32 v32, -2, v32\n"
    "v_min_u32 v33, -2, v33\n"
    "buffer_store_dwordx4 v[20:23], v1, s[4:7], s62 offen offset:1024 sc1\n"
    "v_min_u32 v34, -2, v34\n"
    "v_min_u32 v35, -2, v35\n"
    "buffer_store_dwordx4 v[32:35], v1, s[4:7], s64 offen offset:1024 sc1\n"
    "s_waitcnt lgkmcnt(2)\n"
    "v_pk_fma_f32 v[48:49], v[80:81], v[232:233], v[136:137] op_sel:[0,1,0] op_sel_hi:[1,1,1]\n"
    "v_pk_fma_f32 v[50:51], v[82:83], v[232:233], v[138:139] op_sel:[0,1,0] op_sel_hi:[1,1,1]\n"
    "s_waitcnt lgkmcnt(1)\n"
    "v_pk_fma_f32 v[56:57], v[84:85], v[232:233], v[140:141] op_sel:[0,1,0] op_sel_hi:[1,1,1]\n"
    "v_pk_fma_f32 v[58:59], v[86:87], v[232:233], v[142:143] op_sel:[0,1,0] op_sel_hi:[1,1,1]\n"
    "v_pk_fma_f32 v[192:193], v[192:193], v[232:233], v[48:49] op_sel_hi:[1,0,1]\n"
    "v_pk_fma_f32 v[194:195], v[194:195], v[232:233], v[50:51] op_sel_hi:[1,0,1]\n"
    "v_pk_fma_f32 v[52:53], v[80:81], v[236:237], v[136:137] op_sel:[0,1,0] op_sel_hi:[1,1,1]\n"
    "v_pk_fma_f32 v[54:55], v[82:83], v[236:237], v[138:139] op_sel:[0,1,0] op_sel_hi:[1,1,1]\n"
    "v_pk_fma_f32 v[196:197], v[196:197], v[232:233], v[56:57] op_sel_hi:[1,0,1]\n"
    "v_pk_fma_f32 v[198:199], v[198:199], v[232:233], v[58:59] op_sel_hi:[1,0,1]\n"
    "v_pk_fma_f32 v[60:61], v[84:85], v[236:237], v[140:141] op_sel:[0,1,0] op_sel_hi:[1,1,1]\n"
    "v_pk_fma_f32 v[62:63], v[86:87], v[236:237], v[142:143] op_sel:[0,1,0] op_sel_hi:[1,1,1]\n"
    "v_pk_mul_f32 v[48:49], v[192:193], v[192:193]\n"
    "v_pk_mul_f32 v[50:51], v[194:195], v[194:195]\n"
    "v_pk_fma_f32 v[220:221], v[220:221], v[236:237], v[52:53] op_sel_hi:[1,0,1]\n"
    "v_pk_fma_f32 v[222:223], v[222:223], v[236:237], v[54:55] op_sel_hi:[1,0,1]\n"
    "v_pk_mul_f32 v[56:57], v[196:197], v[196:197]\n"
    "v_pk_mul_f32 v[58:59], v[198:199], v[198:199]\n"
    "v_pk_fma_f32 v[224:225], v[224:225], v[236:237], v[60:61] op_sel_hi:[1,0,1]\n"
    "v_pk_fma_f32 v[226:227], v[226:227], v[236:237], v[62:63] op_sel_hi:[1,0,1]\n"
    "v_pk_fma_f32 v[48:49], v[48:49], s[48:49], v[240:241] op_sel_hi:[1,1,0]\n"
    "v_pk_fma_f32 v[50:51], v[50:51], s[48:49], v[240:241] op_sel_hi:[1,1,0]\n"
    "v_pk_mul_f32 v[52:53], v[220:221], v[220:221]\n"
    "v_pk_mul_f32 v[54:55], v[222:223], v[222:223]\n"
    "v_pk_fma_f32 v[56:57], v[56:57], s[48:49], v[240:241] op_sel_hi:[1,1,0]\n"
    "v_pk_fma_f32 v[58:59], v[58:59], s[48:49], v[240:241] op_sel_hi:[1,1,0]\n"
    "v_pk_mul_f32 v[60:61], v[224:225], v[224:225]\n"
    "v_pk_mul_f32 v[62:63], v[226:227], v[226:227]\n"
    "v_pk_mul_f32 v[48:49], v[48:49], v[192:193]\n"
    "v_pk_mul_f32 v[50:51], v[50:51], v[194:195]\n"
    "v_pk_fma_f32 v[52:53], v[52:53], s[48:49], v[240:241] op_sel_hi:[1,1,0]\n"
    "v_pk_fma_f32 v[54:55], v[54:55], s[48:49], v[240:241] op_sel_hi:[1,1,0]\n"
    "v_pk_mul_f32 v[56:57], v[56:57], v[196:197]\n"
    "v_pk_mul_f32 v[58:59], v[58:59], v[198:199]\n"
    "v_pk_fma_f32 v[60:61], v[60:61], s[48:49], v[240:241] op_sel_hi:[1,1,0]\n"
    "v_pk_fma_f32 v[62:63], v[62:63], s[48:49], v[240:241] op_sel_hi:[1,1,0]\n"
    "v_exp_f32 v48, v48\n"
    "v_exp_f32 v49, v49\n"
    "v_exp_f32 v50, v50\n"
    "v_exp_f32 v51, v51\n"
    "v_pk_mul_f32 v[52:53], v[52:53], v[220:221]\n"
    "v_pk_mul_f32 v[54:55], v[54:55], v[222:223]\n"
    "v_exp_f32 v56, v56\n"
    "v_exp_f32 v57, v57\n"
    "v_exp_f32 v58, v58\n"
    "v_exp_f32 v59, v59\n"
    "v_pk_mul_f32 v[60:61], v[60:61], v[224:225]\n"
    "v_pk_mul_f32 v[62:63], v[62:63], v[226:227]\n"
    "v_exp_f32 v52, v52\n"
    "v_exp_f32 v53, v53\n"
    "v_exp_f32 v54, v54\n"
    "v_exp_f32 v55, v55\n"
    "v_exp_f32 v60, v60\n"
    "v_exp_f32 v61, v61\n"
    "v_exp_f32 v62, v62\n"
    "v_exp_f32 v63, v63\n"
    "v_pk_add_f32 v[48:49], v[48:49], 1.0 op_sel_hi:[1,0]\n"
    "v_pk_add_f32 v[50:51], v[50:51], 1.0 op_sel_hi:[1,0]\n"
    "v_pk_add_f32 v[56:57], v[56:57], 1.0 op_sel_hi:[1,0]\n"
    "v_pk_add_f32 v[58:59], v[58:59], 1.0 op_sel_hi:[1,0]\n"
    "v_rcp_f32 v48, v48\n"
    "v_rcp_f32 v49, v49\n"
    "v_rcp_f32 v50, v50\n"
    "v_rcp_f32 v51, v51\n"
    "v_pk_add_f32 v[52:53], v[52:53], 1.0 op_sel_hi:[1,0]\n"
    "v_pk_add_f32 v[54:55], v[54:55], 1.0 op_sel_hi:[1,0]\n"
    "v_rcp_f32 v56, v56\n"
    "v_rcp_f32 v57, v57\n"
    "v_rcp_f32 v58, v58\n"
    "v_rcp_f32 v59, v59\n"
    "v_pk_add_f32 v[60:61], v[60:61], 1.0 op_sel_hi:[1,0]\n"
    "v_pk_add_f32 v[62:63], v[62:63], 1.0 op_sel_hi:[1,0]\n"
    "v_rcp_f32 v52, v52\n"
    "v_rcp_f32 v53, v53\n"
    "v_rcp_f32 v54, v54\n"
    "v_rcp_f32 v55, v55\n"
    "v_rcp_f32 v60, v60\n"
    "v_rcp_f32 v61, v61\n"
    "v_rcp_f32 v62, v62\n"
    "v_rcp_f32 v63, v63\n"
    "v_pk_mul_f32 v[192:193], v[192:193], v[48:49]\n"
    "s_add_u32 s46, s44, 0\n"
    "buffer_load_dwordx4 v[120:123], v252, s[4:7], s46 offen offset:0 sc1\n"
    "v_pk_mul_f32 v[194:195], v[194:195], v[50:51]\n"
    "v_pk_mul_f32 v[196:197], v[196:197], v[56:57]\n"
    "v_pk_mul_f32 v[198:199], v[198:199], v[58:59]\n"
    "v_cvt_pk_bf16_f32 v24, v192, v193\n"
    "v_cvt_pk_bf16_f32 v25, v194, v195\n"
    "v_pk_mul_f32 v[220:221], v[220:221], v[52:53]\n"
    "v_pk_mul_f32 v[222:223], v[222:223], v[54:55]\n"
    "v_cvt_pk_bf16_f32 v26, v196, v197\n"
    "v_cvt_pk_bf16_f32 v27, v198, v199\n"
    "v_pk_mul_f32 v[224:225], v[224:225], v[60:61]\n"
    "v_pk_mul_f32 v[226:227], v[226:227], v[62:63]\n"
    "v_min_u32 v24, -2, v24\n"
    "v_min_u32_e64 v25, -2, v25\n"
    "v_cvt_pk_bf16_f32 v36, v220, v221\n"
    "v_cvt_pk_bf16_f32 v37, v222, v223\n"
    "v_min_u32_e64 v26, -2, v26\n"
    "buffer_load_dwordx4 v[124:127], v253, s[4:7], s46 offen offset:0 sc1\n"
    "v_min_u32_e64 v27, -2, v27\n"
    "v_cvt_pk_bf16_f32 v38, v224, v225\n"
    "v_cvt_pk_bf16_f32 v39, v226, v227\n"
    "v_min_u32 v36, -2, v36\n"
    "v_min_u32 v37, -2, v37\n"
    "buffer_store_dwordx4 v[24:27], v1, s[4:7], s62 offen offset:2048 sc1\n"
    "v_min_u32 v38, -2, v38\n"
    "v_min_u32 v39, -2, v39\n"
    "buffer_store_dwordx4 v[36:39], v1, s[4:7], s64 offen offset:2048 sc1\n"
    "s_waitcnt lgkmcnt(0)\n"
    "v_pk_fma_f32 v[48:49], v[88:89], v[232:233], v[144:145] op_sel:[0,1,0] op_sel_hi:[1,1,1]\n"
    "v_pk_fma_f32 v[50:51], v[90:91], v[232:233], v[146:147] op_sel:[0,1,0] op_sel_hi:[1,1,1]\n"
    "v_pk_fma_f32 v[52:53], v[88:89], v[236:237], v[144:145] op_sel:[0,1,0] op_sel_hi:[1,1,1]\n"
    "v_pk_fma_f32 v[54:55], v[90:91], v[236:237], v[146:147] op_sel:[0,1,0] op_sel_hi:[1,1,1]\n"
    "v_pk_fma_f32 v[200:201], v[200:201], v[232:233], v[48:49] op_sel_hi:[1,0,1]\n"
    "v_pk_fma_f32 v[202:203], v[202:203], v[232:233], v[50:51] op_sel_hi:[1,0,1]\n"
    "v_pk_fma_f32 v[228:229], v[228:229], v[236:237], v[52:53] op_sel_hi:[1,0,1]\n"
    "buffer_load_dwordx4 v[128:131], v254, s[4:7], s46 offen offset:0 sc1\n"
    "v_pk_fma_f32 v[230:231], v[230:231], v[236:237], v[54:55] op_sel_hi:[1,0,1]\n"
    "v_pk_mul_f32 v[48:49], v[200:201], v[200:201]\n"
    "v_pk_mul_f32 v[50:51], v[202:203], v[202:203]\n"
    "v_pk_mul_f32 v[52:53], v[228:229], v[228:229]\n"
    "v_pk_mul_f32 v[54:55], v[230:231], v[230:231]\n"
    "v_pk_fma_f32 v[48:49], v[48:49], s[48:49], v[240:241] op_sel_hi:[1,1,0]\n"
    "v_pk_fma_f32 v[50:51], v[50:51], s[48:49], v[240:241] op_sel_hi:[1,1,0]\n"
    "v_pk_fma_f32 v[52:53], v[52:53], s[48:49], v[240:241] op_sel_hi:[1,1,0]\n"
    "v_pk_fma_f32 v[54:55], v[54:55], s[48:49], v[240:241] op_sel_hi:[1,1,0]\n"
    "v_pk_mul_f32 v[48:49], v[48:49], v[200:201]\n"
    "v_pk_mul_f32 v[50:51], v[50:51], v[202:203]\n"
    "v_pk_mul_f32 v[52:53], v[52:53], v[228:229]\n"
    "v_pk_mul_f32 v[54:55], v[54:55], v[230:231]\n"
    "v_exp_f32 v48, v48\n"
    "v_exp_f32_e64 v49, v49\n"
    "buffer_load_dwordx4 v[132:135], v255, s[4:7], s46 offen offset:0 sc1\n"
    "v_exp_f32 v50, v50\n"
    "v_exp_f32 v51, v51\n"
    "v_exp_f32 v52, v52\n"
    "v_exp_f32 v53, v53\n"
    "v_exp_f32 v54, v54\n"
    "v_exp_f32 v55, v55\n"
    "v_pk_add_f32 v[48:49], v[48:49], 1.0 op_sel_hi:[1,0]\n"
    "v_pk_add_f32 v[50:51], v[50:51], 1.0 op_sel_hi:[1,0]\n"
    "v_pk_add_f32 v[52:53], v[52:53], 1.0 op_sel_hi:[1,0]\n"
    "v_pk_add_f32 v[54:55], v[54:55], 1.0 op_sel_hi:[1,0]\n"
    "s_add_u32 s46, s44, 0\n"
    "buffer_load_dwordx4 v[148:151], v252, s[4:7], s46 offen offset:3072 sc1\n"
    "v_rcp_f32 v48, v48\n"
    "v_rcp_f32 v49, v49\n"
    "v_rcp_f32 v50, v50\n"
    "v_rcp_f32 v51, v51\n"
    "v_rcp_f32 v52, v52\n"
    "v_rcp_f32 v53, v53\n"
    "v_rcp_f32 v54, v54\n"
    "v_rcp_f32_e64 v55, v55\n"
    "buffer_load_dwordx4 v[152:155], v253, s[4:7], s46 offen offset:3072 sc1\n"
    "v_pk_mul_f32 v[200:201], v[200:201], v[48:49]\n"
    "v_pk_mul_f32 v[202:203], v[202:203], v[50:51]\n"
    "v_pk_mul_f32 v[228:229], v[228:229], v[52:53]\n"
    "v_pk_mul_f32 v[230:231], v[230:231], v[54:55]\n"
    "v_cvt_pk_bf16_f32 v44, v200, v201\n"
    "v_cvt_pk_bf16_f32 v45, v202, v203\n"
    "v_cvt_pk_bf16_f32 v46, v228, v229\n"
    "v_cvt_pk_bf16_f32 v47, v230, v231\n"
    "v_min_u32 v44, -2, v44\n"
    "v_min_u32 v45, -2, v45\n"
    "v_min_u32 v46, -2, v46\n"
    "v_min_u32 v47, -2, v47\n"
    "s_nop 0\n"
    "v_permlane16_swap_b32 v44, v46\n"
    "v_permlane16_swap_b32_e64 v45, v47\n"
    "buffer_load_dwordx4 v[156:159], v254, s[4:7], s46 offen offset:3072 sc1\n"
    "buffer_store_dwordx4 v[44:47], v14, s[4:7], s63 offen sc1\n"
    "buffer_load_dwordx4 v[160:163], v255, s[4:7], s46 offen offset:3072 sc1\n"
    "s_add_u32 s46, s44, 4096\n"
    "buffer_load_dwordx4 v[176:179], v252, s[4:7], s46 offen offset:2048 sc1\n"
    "buffer_load_dwordx4 v[180:183], v253, s[4:7], s46 offen offset:2048 sc1\n"
    "buffer_load_dwordx4 v[184:187], v254, s[4:7], s46 offen offset:2048 sc1\n"
    "buffer_load_dwordx4 v[188:191], v255, s[4:7], s46 offen offset:2048 sc1\n"
    "s_waitcnt vmcnt(14)\n"
    "v_max3_u32 v15, v120, v121, v122\n"
    "s_waitcnt vmcnt(13)\n"
    "v_max3_u32 v15, v15, v123, v124\n"
    "v_max3_u32 v15, v15, v125, v126\n"
    "s_waitcnt vmcnt(10)\n"
    "v_max3_u32 v15, v15, v127, v128\n"
    "v_max3_u32 v15, v15, v129, v130\n"
    "s_waitcnt vmcnt(9)\n"
    "v_max3_u32 v15, v15, v131, v132\n"
    "v_max3_u32 v15, v15, v133, v134\n"
    "v_max_u32 v15, v15, v135\n"
    "v_cmp_eq_u32 vcc, -1, v15\n"
    "s_nop 0\n"
    "s_cbranch_vccnz Lrt87_%=\n"
    "Lbk88_%=:\n"
    "v_mfma_f32_16x16x32_bf16 v[16:19], a[56:59], v[120:123], 0\n"
    "s_add_u32 s46, s44, 8192\n"
    "buffer_load_dwordx4 v[204:207], v252, s[4:7], s46 offen offset:1024 sc1\n"
    "v_mfma_f32_16x16x32_bf16 v[20:23], a[60:63], v[120:123], 0\n"
    "v_lshlrev_b32_e64 v76, 16, v96\n"
    "v_and_b32 v77, 0xffff0000, v96\n"
    "v_mfma_f32_16x16x32_bf16 v[24:27], a[64:67], v[120:123], 0\n"
    "v_lshlrev_b32 v11, 16, v106\n"
    "v_add_f32 v76, v76, v11\n"
    "v_mfma_f32_16x16x32_bf16 v[28:31], a[68:71], v[120:123], 0\n"
    "buffer_load_dwordx4 v[208:211], v253, s[4:7], s46 offen offset:1024 sc1\n"
    "v_and_b32 v11, 0xffff0000, v106\n"
    "v_mfma_f32_16x16x32_bf16 v[32:35], a[72:75], v[120:123], 0\n"
    "v_add_f32 v77, v77, v11\n"
    "v_lshlrev_b32 v78, 16, v97\n"
    "v_mfma_f32_16x16x32_bf16 v[36:39], a[56:59], v[124:127], 0\n"
    "v_and_b32 v79, 0xffff0000, v97\n"
    "v_lshlrev_b32_e64 v11, 16, v107\n"
    "v_mfma_f32_16x16x32_bf16 v[40:43], a[60:63], v[124:127], 0\n"
    "buffer_load_dwordx4 v[212:215], v254, s[4:7], s46 offen offset:1024 sc1\n"
    "v_add_f32_e64 v78, v78, v11\n"
    "v_mfma_f32_16x16x32_bf16 v[44:47], a[64:67], v[124:127], 0\n"
    "v_and_b32 v11, 0xffff0000, v107\n"
    "v_add_f32_e64 v79, v79, v11\n"
    "v_mfma_f32_16x16x32_bf16 v[48:51], a[68:71], v[124:127], 0\n"
    "v_lshlrev_b32_e64 v80, 16, v98\n"
    "v_and_b32 v81, 0xffff0000, v98\n"
    "v_mfma_f32_16x16x32_bf16 v[52:55], a[72:75], v[124:127], 0\n"
    "buffer_load_dwordx4 v[216:219], v255, s[4:7], s46 offen offset:1024 sc1\n"
    "s_waitcnt vmcnt(12)\n"
    "v_max3_u32 v15, v148, v149, v150\n"
    "v_mfma_f32_16x16x32_bf16 v[56:59], a[56:59], v[128:131], 0\n"
    "s_waitcnt vmcnt(11)\n"
    "v_max3_u32 v15, v15, v151, v152\n"
    "v_max3_u32 v15, v15, v153, v154\n"
    "v_mfma_f32_16x16x32_bf16 v[60:63], a[60:63], v[128:131], 0\n"
    "s_waitcnt vmcnt(10)\n"
    "v_max3_u32 v15, v15, v155, v156\n"
    "v_max3_u32 v15, v15, v157, v158\n"
    "v_mfma_f32_16x16x32_bf16 v[64:67], a[64:67], v[128:131], 0\n"
    "s_add_u32 s46, s44, 0\n"
    "buffer_load_dwordx4 v[136:139], v252, s[4:7], s46 offen offset:1024 sc1\n"
    "v_mfma_f32_16x16x32_bf16 v[68:71], a[68:71], v[128:131], 0\n"
    "s_waitcnt vmcnt(9)\n"
    "v_max3_u32 v15, v15, v159, v160\n"
    "v_max3_u32 v15, v15, v161, v162\n"
    "v_mfma_f32_16x16x32_bf16 v[72:75], a[72:75], v[128:131], 0\n"
    "v_max_u32 v15, v15, v163\n"
    "v_cmp_eq_u32 vcc, -1, v15\n"
    "s_nop 0\n"
    "s_cbranch_vccnz Lrt89_%=\n"
    "Lbk90_%=:\n"
    "v_mfma_f32_16x16x32_bf16 v[16:19], a[76:79], v[148:151], v[16:19]\n"
    "buffer_load_dwordx4 v[140:143], v253, s[4:7], s46 offen offset:1024 sc1\n"
    "v_lshlrev_b32 v11, 16, v108\n"
    "v_mfma_f32_16x16x32_bf16 v[20:23], a[80:83], v[148:151], v[20:23]\n"
    "v_add_f32_e64 v80, v80, v11\n"
    "v_and_b32 v11, 0xffff0000, v108\n"
    "v_mfma_f32_16x16x32_bf16 v[24:27], a[84:87], v[148:151], v[24:27]\n"
    "v_add_f32 v81, v81, v11\n"
    "v_lshlrev_b32 v82, 16, v99\n"
    "v_mfma_f32_16x16x32_bf16 v[28:31], a[88:91], v[148:151], v[28:31]\n"
    "buffer_load_dwordx4 v[144:147], v254, s[4:7], s46 offen offset:1024 sc1\n"
    "v_and_b32 v83, 0xffff0000, v99\n"
    "v_mfma_f32_16x16x32_bf16 v[32:35], a[92:95], v[148:151], v[32:35]\n"
    "v_lshlrev_b32 v11, 16, v109\n"
    "v_add_f32 v82, v82, v11\n"
    "v_mfma_f32_16x16x32_bf16 v[36:39], a[76:79], v[152:155], v[36:39]\n"
    "v_and_b32 v11, 0xffff0000, v109\n"
    "v_add_f32_e64 v83, v83, v11\n"
    "v_mfma_f32_16x16x32_bf16 v[40:43], a[80:83], v[152:155], v[40:43]\n"
    "buffer_load_dwordx4 v[164:167], v255, s[4:7], s46 offen offset:1024 sc1\n"
    "v_lshlrev_b32_e64 v84, 16, v100\n"
    "v_mfma_f32_16x16x32_bf16 v[44:47], a[84:87], v[152:155], v[44:47]\n"
    "v_and_b32 v85, 0xffff0000, v100\n"
    "v_lshlrev_b32_e64 v11, 16, v110\n"
    "v_mfma_f32_16x16x32_bf16 v[48:51], a[88:91], v[152:155], v[48:51]\n"
    "v_add_f32_e64 v84, v84, v11\n"
    "v_and_b32 v11, 0xffff0000, v110\n"
    "v_mfma_f32_16x16x32_bf16 v[52:55], a[92:95], v[152:155], v[52:55]\n"
    "s_add_u32 s46, s44, 4096\n"
    "buffer_load_dwordx4 v[168:171], v252, s[4:7], s46 offen offset:0 sc1\n"
    "v_mfma_f32_16x16x32_bf16 v[56:59], a[76:79], v[156:159], v[56:59]\n"
    "v_add_f32 v85, v85, v11\n"
    "v_lshlrev_b32 v86, 16, v101\n"
    "v_mfma_f32_16x16x32_bf16 v[60:63], a[80:83], v[156:159], v[60:63]\n"
    "v_and_b32 v87, 0xffff0000, v101\n"
    "v_lshlrev_b32_e64 v11, 16, v111\n"
    "v_mfma_f32_16x16x32_bf16 v[64:67], a[84:87], v[156:159], v[64:67]\n"
    "buffer_load_dwordx4 v[172:175], v253, s[4:7], s46 offen offset:0 sc1\n"
    "v_add_f32_e64 v86, v86, v11\n"
    "v_mfma_f32_16x16x32_bf16 v[68:71], a[88:91], v[156:159], v[68:71]\n"
    "v_and_b32 v11, 0xffff0000, v111\n"
    "v_add_f32_e64 v87, v87, v11\n"
    "v_mfma_f32_16x16x32_bf16 v[72:75], a[92:95], v[156:159], v[72:75]\n"
    "v_lshlrev_b32_e64 v88, 16, v102\n"
    "v_and_b32 v89, 0xffff0000, v102\n"
    "v_lshlrev_b32 v11, 16, v112\n"
    "v_add_f32 v88, v88, v11\n"
    "v_and_b32 v11, 0xffff0000, v112\n"
    "v_add_f32 v89, v89, v11\n"
    "v_lshlrev_b32 v90, 16, v103\n"
    "v_and_b32 v91, 0xffff0000, v103\n"
    "v_lshlrev_b32 v11, 16, v113\n"
    "v_add_f32 v90, v90, v11\n"
    "v_and_b32 v11, 0xffff0000, v113\n"
    "v_add_f32 v91, v91, v11\n"
    "v_lshlrev_b32 v92, 16, v104\n"
    "v_and_b32 v93, 0xffff0000, v104\n"
    "v_lshlrev_b32 v11, 16, v114\n"
    "v_add_f32 v92, v92, v11\n"
    "v_and_b32 v11, 0xffff0000, v114\n"
    "v_add_f32 v93, v93, v11\n"
    "v_lshlrev_b32 v94, 16, v105\n"
    "v_and_b32 v95, 0xffff0000, v105\n"
    "v_lshlrev_b32 v11, 16, v115\n"
    "v_add_f32 v94, v94, v11\n"
    "v_and_b32 v11, 0xffff0000, v115\n"
    "v_add_f32_e64 v95, v95, v11\n"
    "v_mfma_f32_16x16x32_bf16 v[76:79], a[56:59], v[132:135], v[76:79]\n"
    "buffer_load_dwordx4 v[192:195], v254, s[4:7], s46 offen offset:0 sc1\n"
    "s_waitcnt vmcnt(14)\n"
    "v_max3_u32 v15, v176, v177, v178\n"
    "v_mfma_f32_16x16x32_bf16 v[80:83], a[60:63], v[132:135], v[80:83]\n"
    "s_waitcnt vmcnt(13)\n"
    "v_max3_u32 v15, v15, v179, v180\n"
    "v_max3_u32 v15, v15, v181, v182\n"
    "v_mfma_f32_16x16x32_bf16 v[84:87], a[64:67], v[132:135], v[84:87]\n"
    "s_waitcnt vmcnt(12)\n"
    "v_max3_u32 v15, v15, v183, v184\n"
    "v_max3_u32 v15, v15, v185, v186\n"
    "v_mfma_f32_16x16x32_bf16 v[88:91], a[68:71], v[132:135], v[88:91]\n"
    "buffer_load_dwordx4 v[196:199], v255, s[4:7], s46 offen offset:0 sc1\n"
    "s_waitcnt vmcnt(12)\n"
    "v_max3_u32 v15, v15, v187, v188\n"
    "v_mfma_f32_16x16x32_bf16 v[92:95], a[72:75], v[132:135], v[92:95]\n"
    "v_max3_u32 v15, v15, v189, v190\n"
    "v_max_u32_e64 v15, v15, v191\n"
    "v_mfma_f32_16x16x32_bf16 v[76:79], a[76:79], v[160:163], v[76:79]\n"
    "v_cmp_eq_u32_e64 vcc, -1, v15\n"
    "v_mfma_f32_16x16x32_bf16 v[80:83], a[80:83], v[160:163], v[80:83]\n"
    "s_add_u32 s46, s44, 4096\n"
    "buffer_load_dwordx4 v[200:203], v252, s[4:7], s46 offen offset:3072 sc1\n"
    "v_mfma_f32_16x16x32_bf16 v[84:87], a[84:87], v[160:163], v[84:87]\n"
    "s_cbranch_vccnz Lrt91_%=\n"
    "Lbk92_%=:\n"
    "v_mfma_f32_16x16x32_bf16 v[88:91], a[88:91], v[160:163], v[88:91]\n"
    "v_mfma_f32_16x16x32_bf16 v[92:95], a[92:95], v[160:163], v[92:95]\n"
    "buffer_load_dwordx4 v[220:223], v253, s[4:7], s46 offen offset:3072 sc1\n"
    "v_mfma_f32_16x16x32_bf16 v[16:19], a[96:99], v[176:179], v[16:19]\n"
    "s_mov_b32 s58, s25\n"
    "v_mul_u32_u24 v5, 2560, v8\n"
    "v_mfma_f32_16x16x32_bf16 v[20:23], a[100:103], v[176:179], v[20:23]\n"
    "v_lshrrev_b32_e64 v6, 5, v0\n"
    "v_lshl_add_u32 v5, v6, 4, v5\n"
    "v_mfma_f32_16x16x32_bf16 v[24:27], a[104:107], v[176:179], v[24:27]\n"
    "buffer_load_dwordx4 v[224:227], v254, s[4:7], s46 offen offset:3072 sc1\n"
    "v_bfe_u32 v6, v0, 4, 1\n"
    "v_mfma_f32_16x16x32_bf16 v[28:31], a[108:111], v[176:179], v[28:31]\n"
    "v_lshl_add_u32 v14, v6, 5, v5\n"
    "v_mfma_f32_16x16x32_bf16 v[32:35], a[112:115], v[176:179], v[32:35]\n"
    "v_mfma_f32_16x16x32_bf16 v[36:39], a[96:99], v[180:183], v[36:39]\n"
    "buffer_load_dwordx4 v[228:231], v255, s[4:7], s46 offen offset:3072 sc1\n"
    "v_mfma_f32_16x16x32_bf16 v[40:43], a[100:103], v[180:183], v[40:43]\n"
    "v_mfma_f32_16x16x32_bf16 v[44:47], a[104:107], v[180:183], v[44:47]\n"
    "v_mfma_f32_16x16x32_bf16 v[48:51], a[108:111], v[180:183], v[48:51]\n"
    "s_add_u32 s46, s44, 8192\n"
    "buffer_load_dwordx4 v[232:235], v252, s[4:7], s46 offen offset:2048 sc1\n"
    "v_mfma_f32_16x16x32_bf16 v[52:55], a[112:115], v[180:183], v[52:55]\n"
    "s_waitcnt vmcnt(16)\n"
    "v_max3_u32 v15, v204, v205, v206\n"
    "s_waitcnt vmcnt(15)\n"
    "v_max3_u32 v15, v15, v207, v208\n"
    "v_mfma_f32_16x16x32_bf16 v[56:59], a[96:99], v[184:187], v[56:59]\n"
    "v_max3_u32 v15, v15, v209, v210\n"
    "s_waitcnt vmcnt(14)\n"
    "v_max3_u32 v15, v15, v211, v212\n"
    "v_mfma_f32_16x16x32_bf16 v[60:63], a[100:103], v[184:187], v[60:63]\n"
    "buffer_load_dwordx4 v[236:239], v253, s[4:7], s46 offen offset:2048 sc1\n"
    "v_max3_u32 v15, v15, v213, v214\n"
    "v_mfma_f32_16x16x32_bf16 v[64:67], a[104:107], v[184:187], v[64:67]\n"
    "s_waitcnt vmcnt(14)\n"
    "v_max3_u32 v15, v15, v215, v216\n"
    "v_max3_u32 v15, v15, v217, v218\n"
    "v_mfma_f32_16x16x32_bf16 v[68:71], a[108:111], v[184:187], v[68:71]\n"
    "v_max_u32 v15, v15, v219\n"
    "v_cmp_eq_u32 vcc, -1, v15\n"
    "v_mfma_f32_16x16x32_bf16 v[72:75], a[112:115], v[184:187], v[72:75]\n"
    "buffer_load_dwordx4 v[240:243], v254, s[4:7], s46 offen offset:2048 sc1\n"
    "v_mfma_f32_16x16x32_bf16 v[76:79], a[96:99], v[188:191], v[76:79]\n"
    "s_cbranch_vccnz Lrt93_%=\n"
    "Lbk94_%=:\n"
    "v_mfma_f32_16x16x32_bf16 v[80:83], a[100:103], v[188:191], v[80:83]\n"
    "v_mfma_f32_16x16x32_bf16 v[84:87], a[104:107], v[188:191], v[84:87]\n"
    "buffer_load_dwordx4 v[244:247], v255, s[4:7], s46 offen offset:2048 sc1\n"
    "v_mfma_f32_16x16x32_bf16 v[88:91], a[108:111], v[188:191], v[88:91]\n"
    "v_mfma_f32_16x16x32_bf16 v[92:95], a[112:115], v[188:191], v[92:95]\n"
    "v_mfma_f32_16x16x32_bf16 v[16:19], a[116:119], v[204:207], v[16:19]\n"
    "v_cmp_eq_u32 s[62:63], 0, v6\n"
    "v_mov_b32 v6, 1073741824\n"
    "v_mfma_f32_16x16x32_bf16 v[20:23], a[120:123], v[204:207], v[20:23]\n"
    "s_add_u32 s18, s17, 0\n"
    "buffer_load_dwordx4 a[40:43], v1, s[4:7], s18 offen offset:0 sc1\n"
    "v_mfma_f32_16x16x32_bf16 v[24:27], a[124:127], v[204:207], v[24:27]\n"
    "v_cndmask_b32 v3, v6, v5, s[62:63]\n"
    "s_add_u32 s49, s36, 1\n"
    "v_mfma_f32_16x16x32_bf16 v[28:31], a[228:231], v[204:207], v[28:31]\n"
    "s_and_b32 s49, s49, 3\n"
    "s_mul_i32 s49, s49, 15360\n"
    "v_mfma_f32_16x16x32_bf16 v[32:35], a[232:235], v[204:207], v[32:35]\n"
    "v_mfma_f32_16x16x32_bf16 v[36:39], a[116:119], v[208:211], v[36:39]\n"
    "buffer_load_dwordx4 a[44:47], v1, s[4:7], s18 offen offset:1024 sc1\n"
    "v_mfma_f32_16x16x32_bf16 v[40:43], a[120:123], v[208:211], v[40:43]\n"
    "v_mfma_f32_16x16x32_bf16 v[44:47], a[124:127], v[208:211], v[44:47]\n"
    "v_mfma_f32_16x16x32_bf16 v[48:51], a[228:231], v[208:211], v[48:51]\n"
    "s_waitcnt vmcnt(17)\n"
    "v_max3_u32 v15, v136, v137, v138\n"
    "s_waitcnt vmcnt(16)\n"
    "v_max3_u32 v15, v15, v139, v140\n"
    "v_mfma_f32_16x16x32_bf16 v[52:55], a[232:235], v[208:211], v[52:55]\n"
    "buffer_load_dwordx4 a[48:51], v1, s[4:7], s18 offen offset:2048 sc1\n"
    "v_max3_u32 v15, v15, v141, v142\n"
    "v_mfma_f32_16x16x32_bf16 v[56:59], a[116:119], v[212:215], v[56:59]\n"
    "s_waitcnt vmcnt(16)\n"
    "v_max3_u32 v15, v15, v143, v144\n"
    "v_max3_u32 v15, v15, v145, v146\n"
    "v_mfma_f32_16x16x32_bf16 v[60:63], a[120:123], v[212:215], v[60:63]\n"
    "s_waitcnt vmcnt(15)\n"
    "v_max3_u32 v15, v15, v147, v164\n"
    "v_max3_u32 v15, v15, v165, v166\n"
    "v_mfma_f32_16x16x32_bf16 v[64:67], a[124:127], v[212:215], v[64:67]\n"
    "v_max_u32 v15, v15, v167\n"
    "v_cmp_eq_u32_e64 vcc, -1, v15\n"
    "v_mfma_f32_16x16x32_bf16 v[68:71], a[228:231], v[212:215], v[68:71]\n"
    "buffer_load_dwordx4 a[52:55], v1, s[4:7], s18 offen offset:3072 sc1\n"
    "v_mfma_f32_16x16x32_bf16 v[72:75], a[232:235], v[212:215], v[72:75]\n"
    "s_cbranch_vccnz Lrt95_%=\n"
    "Lbk96_%=:\n"
    "v_mfma_f32_16x16x32_bf16 v[76:79], a[116:119], v[216:219], v[76:79]\n"
    "v_mfma_f32_16x16x32_bf16 v[80:83], a[120:123], v[216:219], v[80:83]\n"
    "v_mfma_f32_16x16x32_bf16 v[84:87], a[124:127], v[216:219], v[84:87]\n"
    "s_add_u32 s46, s44, 0\n"
    "buffer_load_dwordx4 v[120:123], v252, s[4:7], s46 offen offset:2048 sc1\n"
    "v_mfma_f32_16x16x32_bf16 v[88:91], a[228:231], v[216:219], v[88:91]\n"
    "v_mfma_f32_16x16x32_bf16 v[92:95], a[232:235], v[216:219], v[92:95]\n"
    "v_mfma_f32_16x16x32_bf16 v[16:19], a[236:239], v[136:139], v[16:19]\n"
    "s_add_u32 s49, s49, 0\n"
    "v_add_u32 v4, s49, v1\n"
    "v_mfma_f32_16x16x32_bf16 v[20:23], a[240:243], v[136:139], v[20:23]\n"
    "buffer_load_dwordx4 v[124:127], v253, s[4:7], s46 offen offset:2048 sc1\n"
    "s_add_u32 s49, s36, 2\n"
    "v_mfma_f32_16x16x32_bf16 v[24:27], a[244:247], v[136:139], v[24:27]\n"
    "s_and_b32 s49, s49, 3\n"
    "s_mul_i32 s49, s49, 15360\n"
    "v_mfma_f32_16x16x32_bf16 v[28:31], a[248:251], v[136:139], v[28:31]\n"
    "s_add_u32 s49, s49, 5120\n"
    "v_mfma_f32_16x16x32_bf16 v[32:35], a[252:255], v[136:139], v[32:35]\n"
    "v_mfma_f32_16x16x32_bf16 v[36:39], a[236:239], v[140:143], v[36:39]\n"
    "buffer_load_dwordx4 v[128:131], v254, s[4:7], s46 offen offset:2048 sc1\n"
    "v_mfma_f32_16x16x32_bf16 v[40:43], a[240:243], v[140:143], v[40:43]\n"
    "v_mfma_f32_16x16x32_bf16 v[44:47], a[244:247], v[140:143], v[44:47]\n"
    "v_mfma_f32_16x16x32_bf16 v[48:51], a[248:251], v[140:143], v[48:51]\n"
    "s_waitcnt vmcnt(18)\n"
    "v_max3_u32 v15, v168, v169, v170\n"
    "s_waitcnt vmcnt(17)\n"
    "v_max3_u32 v15, v15, v171, v172\n"
    "v_mfma_f32_16x16x32_bf16 v[52:55], a[252:255], v[140:143], v[52:55]\n"
    "buffer_load_dwordx4 v[132:135], v255, s[4:7], s46 offen offset:2048 sc1\n"
    "v_max3_u32 v15, v15, v173, v174\n"
    "v_mfma_f32_16x16x32_bf16 v[56:59], a[236:239], v[144:147], v[56:59]\n"
    "s_waitcnt vmcnt(17)\n"
    "v_max3_u32 v15, v15, v175, v192\n"
    "v_max3_u32 v15, v15, v193, v194\n"
    "v_mfma_f32_16x16x32_bf16 v[60:63], a[240:243], v[144:147], v[60:63]\n"
    "s_waitcnt vmcnt(16)\n"
    "v_max3_u32 v15, v15, v195, v196\n"
    "v_max3_u32 v15, v15, v197, v198\n"
    "v_mfma_f32_16x16x32_bf16 v[64:67], a[244:247], v[144:147], v[64:67]\n"
    "v_max_u32 v15, v15, v199\n"
    "v_cmp_eq_u32 vcc, -1, v15\n"
    "v_mfma_f32_16x16x32_bf16 v[68:71], a[248:251], v[144:147], v[68:71]\n"
    "s_add_u32 s46, s44, 4096\n"
    "buffer_load_dwordx4 v[148:151], v252, s[4:7], s46 offen offset:1024 sc1\n"
    "v_mfma_f32_16x16x32_bf16 v[72:75], a[252:255], v[144:147], v[72:75]\n"
    "s_cbranch_vccnz Lrt97_%=\n"
    "Lbk98_%=:\n"
    "v_mfma_f32_16x16x32_bf16 v[76:79], a[236:239], v[164:167], v[76:79]\n"
    "v_mfma_f32_16x16x32_bf16 v[80:83], a[240:243], v[164:167], v[80:83]\n"
    "v_mfma_f32_16x16x32_bf16 v[84:87], a[244:247], v[164:167], v[84:87]\n"
    "buffer_load_dwordx4 v[152:155], v253, s[4:7], s46 offen offset:1024 sc1\n"
    "v_mfma_f32_16x16x32_bf16 v[88:91], a[248:251], v[164:167], v[88:91]\n"
    "v_mfma_f32_16x16x32_bf16 v[92:95], a[252:255], v[164:167], v[92:95]\n"
    "v_mfma_f32_16x16x32_bf16 v[16:19], a[128:131], v[168:171], v[16:19]\n"
    "v_add_u32 v5, s49, v1\n"
    "s_add_u32 s49, s36, 3\n"
    "v_mfma_f32_16x16x32_bf16 v[20:23], a[132:135], v[168:171], v[20:23]\n"
    "buffer_load_dwordx4 v[156:159], v254, s[4:7], s46 offen offset:1024 sc1\n"
    "s_and_b32 s49, s49, 3\n"
    "v_mfma_f32_16x16x32_bf16 v[24:27], a[136:139], v[168:171], v[24:27]\n"
    "s_mul_i32 s49, s49, 15360\n"
    "s_add_u32 s49, s49, 10240\n"
    "v_mfma_f32_16x16x32_bf16 v[28:31], a[140:143], v[168:171], v[28:31]\n"
    "v_add_u32_e64 v6, s49, v1\n"
    "v_mfma_f32_16x16x32_bf16 v[32:35], a[144:147], v[168:171], v[32:35]\n"
    "v_mfma_f32_16x16x32_bf16 v[36:39], a[128:131], v[172:175], v[36:39]\n"
    "buffer_load_dwordx4 v[160:163], v255, s[4:7], s46 offen offset:1024 sc1\n"
    "v_mfma_f32_16x16x32_bf16 v[40:43], a[132:135], v[172:175], v[40:43]\n"
    "v_mfma_f32_16x16x32_bf16 v[44:47], a[136:139], v[172:175], v[44:47]\n"
    "v_mfma_f32_16x16x32_bf16 v[48:51], a[140:143], v[172:175], v[48:51]\n"
    "s_waitcnt vmcnt(19)\n"
    "v_max3_u32 v15, v200, v201, v202\n"
    "s_waitcnt vmcnt(18)\n"
    "v_max3_u32 v15, v15, v203, v220\n"
    "v_mfma_f32_16x16x32_bf16 v[52:55], a[144:147], v[172:175], v[52:55]\n"
    "s_add_u32 s18, s17, 4096\n"
    "buffer_load_dwordx4 v[96:99], v1, s[4:7], s18 offen offset:0 sc1\n"
    "v_mfma_f32_16x16x32_bf16 v[56:59], a[128:131], v[192:195], v[56:59]\n"
    "v_max3_u32 v15, v15, v221, v222\n"
    "s_waitcnt vmcnt(18)\n"
    "v_max3_u32 v15, v15, v223, v224\n"
    "v_mfma_f32_16x16x32_bf16 v[60:63], a[132:135], v[192:195], v[60:63]\n"
    "v_max3_u32 v15, v15, v225, v226\n"
    "s_waitcnt vmcnt(17)\n"
    "v_max3_u32 v15, v15, v227, v228\n"
    "v_mfma_f32_16x16x32_bf16 v[64:67], a[136:139], v[192:195], v[64:67]\n"
    "v_max3_u32 v15, v15, v229, v230\n"
    "v_max_u32_e64 v15, v15, v231\n"
    "v_mfma_f32_16x16x32_bf16 v[68:71], a[140:143], v[192:195], v[68:71]\n"
    "buffer_load_dwordx4 v[100:103], v1, s[4:7], s18 offen offset:1024 sc1\n"
    "v_cmp_eq_u32_e64 vcc, -1, v15\n"
    "v_mfma_f32_16x16x32_bf16 v[72:75], a[144:147], v[192:195], v[72:75]\n"
    "s_cbranch_vccnz Lrt99_%=\n"
    "Lbk100_%=:\n"
    "v_mfma_f32_16x16x32_bf16 v[76:79], a[128:131], v[196:199], v[76:79]\n"
    "v_mfma_f32_16x16x32_bf16 v[80:83], a[132:135], v[196:199], v[80:83]\n"
    "v_mfma_f32_16x16x32_bf16 v[84:87], a[136:139], v[196:199], v[84:87]\n"
    "s_add_u32 s46, s44, 8192\n"
    "buffer_load_dwordx4 v[176:179], v252, s[4:7], s46 offen offset:0 sc1\n"
    "v_mfma_f32_16x16x32_bf16 v[88:91], a[140:143], v[196:199], v[88:91]\n"
    "v_mfma_f32_16x16x32_bf16 v[92:95], a[144:147], v[196:199], v[92:95]\n"
    "v_mfma_f32_16x16x32_bf16 v[16:19], a[148:151], v[200:203], v[16:19]\n"
    "s_mul_i32 s49, s36, 15360\n"
    "s_add_u32 s49, s49, 0\n"
    "v_mfma_f32_16x16x32_bf16 v[20:23], a[152:155], v[200:203], v[20:23]\n"
    "buffer_load_dwordx4 v[180:183], v253, s[4:7], s46 offen offset:0 sc1\n"
    "v_add_u32_e64 v7, s49, v1\n"
    "v_mfma_f32_16x16x32_bf16 v[24:27], a[156:159], v[200:203], v[24:27]\n"
    "v_mfma_f32_16x16x32_bf16 v[28:31], a[160:163], v[200:203], v[28:31]\n"
    "v_mfma_f32_16x16x32_bf16 v[32:35], a[164:167], v[200:203], v[32:35]\n"
    "v_mfma_f32_16x16x32_bf16 v[36:39], a[148:151], v[220:223], v[36:39]\n"
    "buffer_load_dwordx4 v[184:187], v254, s[4:7], s46 offen offset:0 sc1\n"
    "v_mfma_f32_16x16x32_bf16 v[40:43], a[152:155], v[220:223], v[40:43]\n"
    "v_mfma_f32_16x16x32_bf16 v[44:47], a[156:159], v[220:223], v[44:47]\n"
    "v_mfma_f32_16x16x32_bf16 v[48:51], a[160:163], v[220:223], v[48:51]\n"
    "s_waitcnt vmcnt(20)\n"
    "v_max3_u32 v15, v232, v233, v234\n"
    "s_waitcnt vmcnt(19)\n"
    "v_max3_u32 v15, v15, v235, v236\n"
    "v_mfma_f32_16x16x32_bf16 v[52:55], a[164:167], v[220:223], v[52:55]\n"
    "buffer_load_dwordx4 v[188:191], v255, s[4:7], s46 offen offset:0 sc1\n"
    "v_max3_u32 v15, v15, v237, v238\n"
    "v_mfma_f32_16x16x32_bf16 v[56:59], a[148:151], v[224:227], v[56:59]\n"
    "s_waitcnt vmcnt(19)\n"
    "v_max3_u32 v15, v15, v239, v240\n"
    "v_max3_u32 v15, v15, v241, v242\n"
    "v_mfma_f32_16x16x32_bf16 v[60:63], a[152:155], v[224:227], v[60:63]\n"
    "s_waitcnt vmcnt(18)\n"
    "v_max3_u32 v15, v15, v243, v244\n"
    "v_max3_u32 v15, v15, v245, v246\n"
    "v_mfma_f32_16x16x32_bf16 v[64:67], a[156:159], v[224:227], v[64:67]\n"
    "v_max_u32 v15, v15, v247\n"
    "v_cmp_eq_u32 vcc, -1, v15\n"
    "v_mfma_f32_16x16x32_bf16 v[68:71], a[160:163], v[224:227], v[68:71]\n"
    "buffer_load_dwordx4 v[104:107], v1, s[4:7], s18 offen offset:2048 sc1\n"
    "v_mfma_f32_16x16x32_bf16 v[72:75], a[164:167], v[224:227], v[72:75]\n"
    "s_cbranch_vccnz Lrt101_%=\n"
    "Lbk102_%=:\n"
    "v_mfma_f32_16x16x32_bf16 v[76:79], a[148:151], v[228:231], v[76:79]\n"
    "v_mfma_f32_16x16x32_bf16 v[80:83], a[152:155], v[228:231], v[80:83]\n"
    "v_mfma_f32_16x16x32_bf16 v[84:87], a[156:159], v[228:231], v[84:87]\n"
    "buffer_load_dwordx4 v[108:111], v1, s[4:7], s18 offen offset:3072 sc1\n"
    "v_mfma_f32_16x16x32_bf16 v[88:91], a[160:163], v[228:231], v[88:91]\n"
    "v_mfma_f32_16x16x32_bf16 v[92:95], a[164:167], v[228:231], v[92:95]\n"
    "v_mfma_f32_16x16x32_bf16 v[16:19], a[168:171], v[232:235], v[16:19]\n"
    "v_mfma_f32_16x16x32_bf16 v[20:23], a[172:175], v[232:235], v[20:23]\n"
    "s_add_u32 s46, s44, 8192\n"
    "buffer_load_dwordx4 v[204:207], v252, s[4:7], s46 offen offset:3072 sc1\n"
    "v_mfma_f32_16x16x32_bf16 v[24:27], a[176:179], v[232:235], v[24:27]\n"
    "v_mfma_f32_16x16x32_bf16 v[28:31], a[180:183], v[232:235], v[28:31]\n"
    "v_mfma_f32_16x16x32_bf16 v[32:35], a[184:187], v[232:235], v[32:35]\n"
    "v_mfma_f32_16x16x32_bf16 v[36:39], a[168:171], v[236:239], v[36:39]\n"
    "buffer_load_dwordx4 v[208:211], v253, s[4:7], s46 offen offset:3072 sc1\n"
    "v_mfma_f32_16x16x32_bf16 v[40:43], a[172:175], v[236:239], v[40:43]\n"
    "v_mfma_f32_16x16x32_bf16 v[44:47], a[176:179], v[236:239], v[44:47]\n"
    "v_mfma_f32_16x16x32_bf16 v[48:51], a[180:183], v[236:239], v[48:51]\n"
    "s_waitcnt vmcnt(17)\n"
    "v_max3_u32 v15, v120, v121, v122\n"
    "s_waitcnt vmcnt(16)\n"
    "v_max3_u32 v15, v15, v123, v124\n"
    "v_mfma_f32_16x16x32_bf16 v[52:55], a[184:187], v[236:239], v[52:55]\n"
    "buffer_load_dwordx4 v[212:215], v254, s[4:7], s46 offen offset:3072 sc1\n"
    "v_max3_u32 v15, v15, v125, v126\n"
    "v_mfma_f32_16x16x32_bf16 v[56:59], a[168:171], v[240:243], v[56:59]\n"
    "s_waitcnt vmcnt(16)\n"
    "v_max3_u32 v15, v15, v127, v128\n"
    "v_max3_u32 v15, v15, v129, v130\n"
    "v_mfma_f32_16x16x32_bf16 v[60:63], a[172:175], v[240:243], v[60:63]\n"
    "s_waitcnt vmcnt(15)\n"
    "v_max3_u32 v15, v15, v131, v132\n"
    "v_max3_u32 v15, v15, v133, v134\n"
    "v_mfma_f32_16x16x32_bf16 v[64:67], a[176:179], v[240:243], v[64:67]\n"
    "v_max_u32 v15, v15, v135\n"
    "v_cmp_eq_u32_e64 vcc, -1, v15\n"
    "v_mfma_f32_16x16x32_bf16 v[68:71], a[180:183], v[240:243], v[68:71]\n"
    "buffer_load_dwordx4 v[216:219], v255, s[4:7], s46 offen offset:3072 sc1\n"
    "v_mfma_f32_16x16x32_bf16 v[72:75], a[184:187], v[240:243], v[72:75]\n"
    "s_cbranch_vccnz Lrt103_%=\n"
    "Lbk104_%=:\n"
    "v_mfma_f32_16x16x32_bf16 v[76:79], a[168:171], v[244:247], v[76:79]\n"
    "v_mfma_f32_16x16x32_bf16 v[80:83], a[172:175], v[244:247], v[80:83]\n"
    "v_mfma_f32_16x16x32_bf16 v[84:87], a[176:179], v[244:247], v[84:87]\n"
    "s_add_u32 s18, s17, 8192\n"
    "buffer_load_dwordx4 v[112:115], v1, s[4:7], s18 offen offset:0 sc1\n"
    "v_mfma_f32_16x16x32_bf16 v[88:91], a[180:183], v[244:247], v[88:91]\n"
    "v_mfma_f32_16x16x32_bf16 v[92:95], a[184:187], v[244:247], v[92:95]\n"
    "v_mfma_f32_16x16x32_bf16 v[16:19], a[188:191], v[120:123], v[16:19]\n"
    "v_mfma_f32_16x16x32_bf16 v[20:23], a[192:195], v[120:123], v[20:23]\n"
    "buffer_load_dwordx4 v[116:119], v1, s[4:7], s18 offen offset:1024 sc1\n"
    "v_mfma_f32_16x16x32_bf16 v[24:27], a[196:199], v[120:123], v[24:27]\n"
    "v_mfma_f32_16x16x32_bf16 v[28:31], a[200:203], v[120:123], v[28:31]\n"
    "v_mfma_f32_16x16x32_bf16 v[32:35], a[204:207], v[120:123], v[32:35]\n"
    "v_mfma_f32_16x16x32_bf16 v[36:39], a[188:191], v[124:127], v[36:39]\n"
    "s_add_u32 s46, s45, 0\n"
    "buffer_load_dwordx4 v[136:139], v252, s[4:7], s46 offen offset:0 sc1\n"
    "v_mfma_f32_16x16x32_bf16 v[40:43], a[192:195], v[124:127], v[40:43]\n"
    "v_mfma_f32_16x16x32_bf16 v[44:47], a[196:199], v[124:127], v[44:47]\n"
    "v_mfma_f32_16x16x32_bf16 v[48:51], a[200:203], v[124:127], v[48:51]\n"
    "s_waitcnt vmcnt(18)\n"
    "v_max3_u32 v15, v148, v149, v150\n"
    "s_waitcnt vmcnt(17)\n"
    "v_max3_u32 v15, v15, v151, v152\n"
    "v_mfma_f32_16x16x32_bf16 v[52:55], a[204:207], v[124:127], v[52:55]\n"
    "buffer_load_dwordx4 v[140:143], v253, s[4:7], s46 offen offset:0 sc1\n"
    "v_max3_u32 v15, v15, v153, v154\n"
    "v_mfma_f32_16x16x32_bf16 v[56:59], a[188:191], v[128:131], v[56:59]\n"
    "s_waitcnt vmcnt(17)\n"
    "v_max3_u32 v15, v15, v155, v156\n"
    "v_max3_u32 v15, v15, v157, v158\n"
    "v_mfma_f32_16x16x32_bf16 v[60:63], a[192:195], v[128:131], v[60:63]\n"
    "s_waitcnt vmcnt(16)\n"
    "v_max3_u32 v15, v15, v159, v160\n"
    "v_max3_u32 v15, v15, v161, v162\n"
    "v_mfma_f32_16x16x32_bf16 v[64:67], a[196:199], v[128:131], v[64:67]\n"
    "v_max_u32 v15, v15, v163\n"
    "v_cmp_eq_u32 vcc, -1, v15\n"
    "v_mfma_f32_16x16x32_bf16 v[68:71], a[200:203], v[128:131], v[68:71]\n"
    "buffer_load_dwordx4 v[144:147], v254, s[4:7], s46 offen offset:0 sc1\n"
    "v_mfma_f32_16x16x32_bf16 v[72:75], a[204:207], v[128:131], v[72:75]\n"
    "s_cbranch_vccnz Lrt105_%=\n"
    "Lbk106_%=:\n"
    "v_mfma_f32_16x16x32_bf16 v[76:79], a[188:191], v[132:135], v[76:79]\n"
    "v_mfma_f32_16x16x32_bf16 v[80:83], a[192:195], v[132:135], v[80:83]\n"
    "v_mfma_f32_16x16x32_bf16 v[84:87], a[196:199], v[132:135], v[84:87]\n"
    "buffer_load_dwordx4 v[164:167], v255, s[4:7], s46 offen offset:0 sc1\n"
    "v_mfma_f32_16x16x32_bf16 v[88:91], a[200:203], v[132:135], v[88:91]\n"
    "v_mfma_f32_16x16x32_bf16 v[92:95], a[204:207], v[132:135], v[92:95]\n"
    "s_add_u32 s46, s45, 0\n"
    "buffer_load_dwordx4 v[168:171], v252, s[4:7], s46 offen offset:1024 sc1\n"
    "buffer_load_dwordx4 v[172:175], v253, s[4:7], s46 offen offset:1024 sc1\n"
    "buffer_load_dwordx4 v[192:195], v254, s[4:7], s46 offen offset:1024 sc1\n"
    "buffer_load_dwordx4 v[196:199], v255, s[4:7], s46 offen offset:1024 sc1\n"
    "v_mfma_f32_16x16x32_bf16 v[16:19], a[208:211], v[148:151], v[16:19]\n"
    "s_waitcnt vmcnt(19)\n"
    "v_max3_u32 v15, v176, v177, v178\n"
    "s_waitcnt vmcnt(18)\n"
    "v_max3_u32 v15, v15, v179, v180\n"
    "v_mfma_f32_16x16x32_bf16 v[20:23], a[212:215], v[148:151], v[20:23]\n"
    "v_max3_u32 v15, v15, v181, v182\n"
    "s_waitcnt vmcnt(17)\n"
    "v_max3_u32 v15, v15, v183, v184\n"
    "v_mfma_f32_16x16x32_bf16 v[24:27], a[216:219], v[148:151], v[24:27]\n"
    "v_max3_u32 v15, v15, v185, v186\n"
    "s_waitcnt vmcnt(16)\n"
    "v_max3_u32 v15, v15, v187, v188\n"
    "v_mfma_f32_16x16x32_bf16 v[28:31], a[220:223], v[148:151], v[28:31]\n"
    "v_max3_u32 v15, v15, v189, v190\n"
    "v_max_u32_e64 v15, v15, v191\n"
    "v_mfma_f32_16x16x32_bf16 v[32:35], a[224:227], v[148:151], v[32:35]\n"
    "v_cmp_eq_u32_e64 vcc, -1, v15\n"
    "v_mfma_f32_16x16x32_bf16 v[36:39], a[208:211], v[152:155], v[36:39]\n"
    "s_cbranch_vccnz Lrt107_%=\n"
    "Lbk108_%=:\n"
    "v_mfma_f32_16x16x32_bf16 v[40:43], a[212:215], v[152:155], v[40:43]\n"
    "s_waitcnt vmcnt(13)\n"
    "v_max3_u32 v15, v204, v205, v206\n"
    "s_waitcnt vmcnt(12)\n"
    "v_max3_u32 v15, v15, v207, v208\n"
    "v_mfma_f32_16x16x32_bf16 v[44:47], a[216:219], v[152:155], v[44:47]\n"
    "v_max3_u32 v15, v15, v209, v210\n"
    "s_waitcnt vmcnt(11)\n"
    "v_max3_u32 v15, v15, v211, v212\n"
    "v_mfma_f32_16x16x32_bf16 v[48:51], a[220:223], v[152:155], v[48:51]\n"
    "v_max3_u32 v15, v15, v213, v214\n"
    "s_waitcnt vmcnt(10)\n"
    "v_max3_u32 v15, v15, v215, v216\n"
    "v_mfma_f32_16x16x32_bf16 v[52:55], a[224:227], v[152:155], v[52:55]\n"
    "v_max3_u32 v15, v15, v217, v218\n"
    "v_max_u32 v15, v15, v219\n"
    "v_mfma_f32_16x16x32_bf16 v[56:59], a[208:211], v[156:159], v[56:59]\n"
    "v_cmp_eq_u32_e64 vcc, -1, v15\n"
    "v_mfma_f32_16x16x32_bf16 v[60:63], a[212:215], v[156:159], v[60:63]\n"
    "s_cbranch_vccnz Lrt109_%=\n"
    "Lbk110_%=:\n"
    "v_mfma_f32_16x16x32_bf16 v[64:67], a[216:219], v[156:159], v[64:67]\n"
    "s_waitcnt vmcnt(7)\n"
    "v_max3_u32 v15, v136, v137, v138\n"
    "s_waitcnt vmcnt(6)\n"
    "v_max3_u32 v15, v15, v139, v140\n"
    "v_mfma_f32_16x16x32_bf16 v[68:71], a[220:223], v[156:159], v[68:71]\n"
    "v_max3_u32 v15, v15, v141, v142\n"
    "s_waitcnt vmcnt(5)\n"
    "v_max3_u32 v15, v15, v143, v144\n"
    "v_mfma_f32_16x16x32_bf16 v[72:75], a[224:227], v[156:159], v[72:75]\n"
    "v_max3_u32 v15, v15, v145, v146\n"
    "s_waitcnt vmcnt(4)\n"
    "v_max3_u32 v15, v15, v147, v164\n"
    "v_mfma_f32_16x16x32_bf16 v[76:79], a[208:211], v[160:163], v[76:79]\n"
    "v_max3_u32 v15, v15, v165, v166\n"
    "v_max_u32 v15, v15, v167\n"
    "v_mfma_f32_16x16x32_bf16 v[80:83], a[212:215], v[160:163], v[80:83]\n"
    "v_cmp_eq_u32_e64 vcc, -1, v15\n"
    "v_mfma_f32_16x16x32_bf16 v[84:87], a[216:219], v[160:163], v[84:87]\n"
    "s_cbranch_vccnz Lrt111_%=\n"
    "Lbk112_%=:\n"
    "v_mfma_f32_16x16x32_bf16 v[88:91], a[220:223], v[160:163], v[88:91]\n"
    "s_waitcnt vmcnt(3)\n"
    "v_max3_u32 v15, v168, v169, v170\n"
    "s_waitcnt vmcnt(2)\n"
    "v_max3_u32 v15, v15, v171, v172\n"
    "v_mfma_f32_16x16x32_bf16 v[92:95], a[224:227], v[160:163], v[92:95]\n"
    "v_max3_u32 v15, v15, v173, v174\n"
    "s_waitcnt vmcnt(1)\n"
    "v_max3_u32 v15, v15, v175, v192\n"
    "v_max3_u32 v15, v15, v193, v194\n"
    "s_waitcnt vmcnt(0)\n"
    "v_max3_u32 v15, v15, v195, v196\n"
    "v_max3_u32 v15, v15, v197, v198\n"
    "v_max_u32 v15, v15, v199\n"
    "v_cmp_eq_u32 vcc, -1, v15\n"
    "s_nop 0\n"
    "s_cbranch_vccnz Lrt113_%=\n"
    "Lbk114_%=:\n"
    "v_mfma_f32_16x16x32_bf16 v[16:19], a[0:3], v[176:179], v[16:19]\n"
    "v_mfma_f32_16x16x32_bf16 v[16:19], a[20:23], v[204:207], v[16:19]\n"
    "v_mfma_f32_16x16x32_bf16 v[16:19], a[40:43], v[136:139], v[16:19]\n"
    "v_mfma_f32_16x16x32_bf16 v[16:19], v[100:103], v[168:171], v[16:19]\n"
    "v_mfma_f32_16x16x32_bf16 v[20:23], a[4:7], v[176:179], v[20:23]\n"
    "v_mfma_f32_16x16x32_bf16 v[20:23], a[24:27], v[204:207], v[20:23]\n"
    "v_mfma_f32_16x16x32_bf16 v[20:23], a[44:47], v[136:139], v[20:23]\n"
    "v_mfma_f32_16x16x32_bf16 v[20:23], v[104:107], v[168:171], v[20:23]\n"
    "v_mfma_f32_16x16x32_bf16 v[24:27], a[8:11], v[176:179], v[24:27]\n"
    "v_mfma_f32_16x16x32_bf16 v[24:27], a[28:31], v[204:207], v[24:27]\n"
    "v_mfma_f32_16x16x32_bf16 v[24:27], a[48:51], v[136:139], v[24:27]\n"
    "v_mfma_f32_16x16x32_bf16 v[24:27], v[108:111], v[168:171], v[24:27]\n"
    "v_mfma_f32_16x16x32_bf16 v[28:31], a[12:15], v[176:179], v[28:31]\n"
    "v_mfma_f32_16x16x32_bf16 v[28:31], a[32:35], v[204:207], v[28:31]\n"
    "v_mfma_f32_16x16x32_bf16 v[28:31], a[52:55], v[136:139], v[28:31]\n"
    "v_mfma_f32_16x16x32_bf16 v[28:31], v[112:115], v[168:171], v[28:31]\n"
    "v_mfma_f32_16x16x32_bf16 v[32:35], a[16:19], v[176:179], v[32:35]\n"
    "v_mfma_f32_16x16x32_bf16 v[32:35], a[36:39], v[204:207], v[32:35]\n"
    "v_mfma_f32_16x16x32_bf16 v[32:35], v[96:99], v[136:139], v[32:35]\n"
    "v_mfma_f32_16x16x32_bf16 v[32:35], v[116:119], v[168:171], v[32:35]\n"
    "v_mfma_f32_16x16x32_bf16 v[36:39], a[0:3], v[180:183], v[36:39]\n"
    "v_mfma_f32_16x16x32_bf16 v[36:39], a[20:23], v[208:211], v[36:39]\n"
    "v_mfma_f32_16x16x32_bf16 v[36:39], a[40:43], v[140:143], v[36:39]\n"
    "ds_write_b128 v4, v[16:19] offset:0\n"
    "v_mfma_f32_16x16x32_bf16 v[36:39], v[100:103], v[172:175], v[36:39]\n"
    "v_mfma_f32_16x16x32_bf16 v[40:43], a[4:7], v[180:183], v[40:43]\n"
    "ds_write_b128 v4, v[20:23] offset:1024\n"
    "v_mfma_f32_16x16x32_bf16 v[40:43], a[24:27], v[208:211], v[40:43]\n"
    "v_mfma_f32_16x16x32_bf16 v[40:43], a[44:47], v[140:143], v[40:43]\n"
    "ds_write_b128 v4, v[24:27] offset:2048\n"
    "v_mfma_f32_16x16x32_bf16 v[40:43], v[104:107], v[172:175], v[40:43]\n"
    "v_mfma_f32_16x16x32_bf16 v[44:47], a[8:11], v[180:183], v[44:47]\n"
    "ds_write_b128 v4, v[28:31] offset:3072\n"
    "v_mfma_f32_16x16x32_bf16 v[44:47], a[28:31], v[208:211], v[44:47]\n"
    "v_mfma_f32_16x16x32_bf16 v[44:47], a[48:51], v[140:143], v[44:47]\n"
    "ds_write_b128 v4, v[32:35] offset:4096\n"
    "v_mfma_f32_16x16x32_bf16 v[44:47], v[108:111], v[172:175], v[44:47]\n"
    "v_mfma_f32_16x16x32_bf16 v[48:51], a[12:15], v[180:183], v[48:51]\n"
    "v_mfma_f32_16x16x32_bf16 v[48:51], a[32:35], v[208:211], v[48:51]\n"
    "v_mfma_f32_16x16x32_bf16 v[48:51], a[52:55], v[140:143], v[48:51]\n"
    "v_mfma_f32_16x16x32_bf16 v[48:51], v[112:115], v[172:175], v[48:51]\n"
    "v_mfma_f32_16x16x32_bf16 v[52:55], a[16:19], v[180:183], v[52:55]\n"
    "v_mfma_f32_16x16x32_bf16 v[52:55], a[36:39], v[208:211], v[52:55]\n"
    "v_mfma_f32_16x16x32_bf16 v[52:55], v[96:99], v[140:143], v[52:55]\n"
    "v_mfma_f32_16x16x32_bf16 v[52:55], v[116:119], v[172:175], v[52:55]\n"
    "v_mfma_f32_16x16x32_bf16 v[56:59], a[0:3], v[184:187], v[56:59]\n"
    "v_mfma_f32_16x16x32_bf16 v[56:59], a[20:23], v[212:215], v[56:59]\n"
    "v_mfma_f32_16x16x32_bf16 v[56:59], a[40:43], v[144:147], v[56:59]\n"
    "ds_write_b128 v5, v[36:39] offset:0\n"
    "v_mfma_f32_16x16x32_bf16 v[56:59], v[100:103], v[192:195], v[56:59]\n"
    "v_mfma_f32_16x16x32_bf16 v[60:63], a[4:7], v[184:187], v[60:63]\n"
    "ds_write_b128 v5, v[40:43] offset:1024\n"
    "v_mfma_f32_16x16x32_bf16 v[60:63], a[24:27], v[212:215], v[60:63]\n"
    "v_mfma_f32_16x16x32_bf16 v[60:63], a[44:47], v[144:147], v[60:63]\n"
    "ds_write_b128 v5, v[44:47] offset:2048\n"
    "v_mfma_f32_16x16x32_bf16 v[60:63], v[104:107], v[192:195], v[60:63]\n"
    "v_mfma_f32_16x16x32_bf16 v[64:67], a[8:11], v[184:187], v[64:67]\n"
    "ds_write_b128 v5, v[48:51] offset:3072\n"
    "v_mfma_f32_16x16x32_bf16 v[64:67], a[28:31], v[212:215], v[64:67]\n"
    "v_mfma_f32_16x16x32_bf16 v[64:67], a[48:51], v[144:147], v[64:67]\n"
    "ds_write_b128 v5, v[52:55] offset:4096\n"
    "v_mfma_f32_16x16x32_bf16 v[64:67], v[108:111], v[192:195], v[64:67]\n"
    "v_mfma_f32_16x16x32_bf16 v[68:71], a[12:15], v[184:187], v[68:71]\n"
    "v_mfma_f32_16x16x32_bf16 v[68:71], a[32:35], v[212:215], v[68:71]\n"
    "v_mfma_f32_16x16x32_bf16 v[68:71], a[52:55], v[144:147], v[68:71]\n"
    "v_mfma_f32_16x16x32_bf16 v[68:71], v[112:115], v[192:195], v[68:71]\n"
    "v_mfma_f32_16x16x32_bf16 v[72:75], a[16:19], v[184:187], v[72:75]\n"
    "v_mfma_f32_16x16x32_bf16 v[72:75], a[36:39], v[212:215], v[72:75]\n"
    "v_mfma_f32_16x16x32_bf16 v[72:75], v[96:99], v[144:147], v[72:75]\n"
    "v_mfma_f32_16x16x32_bf16 v[72:75], v[116:119], v[192:195], v[72:75]\n"
    "v_mfma_f32_16x16x32_bf16 v[76:79], a[0:3], v[188:191], v[76:79]\n"
    "v_mfma_f32_16x16x32_bf16 v[76:79], a[20:23], v[216:219], v[76:79]\n"
    "v_mfma_f32_16x16x32_bf16 v[76:79], a[40:43], v[164:167], v[76:79]\n"
    "ds_write_b128 v6, v[56:59] offset:0\n"
    "v_mfma_f32_16x16x32_bf16 v[76:79], v[100:103], v[196:199], v[76:79]\n"
    "v_mfma_f32_16x16x32_bf16 v[80:83], a[4:7], v[188:191], v[80:83]\n"
    "ds_write_b128 v6, v[60:63] offset:1024\n"
    "v_mfma_f32_16x16x32_bf16 v[80:83], a[24:27], v[216:219], v[80:83]\n"
    "v_mfma_f32_16x16x32_bf16 v[80:83], a[44:47], v[164:167], v[80:83]\n"
    "ds_write_b128 v6, v[64:67] offset:2048\n"
    "v_mfma_f32_16x16x32_bf16 v[80:83], v[104:107], v[196:199], v[80:83]\n"
    "v_mfma_f32_16x16x32_bf16 v[84:87], a[8:11], v[188:191], v[84:87]\n"
    "ds_write_b128 v6, v[68:71] offset:3072\n"
    "v_mfma_f32_16x16x32_bf16 v[84:87], a[28:31], v[216:219], v[84:87]\n"
    "v_mfma_f32_16x16x32_bf16 v[84:87], a[48:51], v[164:167], v[84:87]\n"
    "ds_write_b128 v6, v[72:75] offset:4096\n"
    "v_mfma_f32_16x16x32_bf16 v[84:87], v[108:111], v[196:199], v[84:87]\n"
    "s_waitcnt lgkmcnt(0)\n"
    "s_barrier\n"
    "v_mfma_f32_16x16x32_bf16 v[88:91], a[12:15], v[188:191], v[88:91]\n"
    "ds_read_b128 v[200:203], v7 offset:0\n"
    "ds_read_b128 v[236:239], v7 offset:5120\n"
    "v_mfma_f32_16x16x32_bf16 v[88:91], a[32:35], v[216:219], v[88:91]\n"
    "ds_read_b128 v[220:223], v7 offset:1024\n"
    "ds_read_b128 v[240:243], v7 offset:6144\n"
    "v_mfma_f32_16x16x32_bf16 v[88:91], a[52:55], v[164:167], v[88:91]\n"
    "ds_read_b128 v[224:227], v7 offset:2048\n"
    "ds_read_b128 v[244:247], v7 offset:7168\n"
    "v_mfma_f32_16x16x32_bf16 v[88:91], v[112:115], v[196:199], v[88:91]\n"
    "ds_read_b128 v[228:231], v7 offset:3072\n"
    "ds_read_b128 v[120:123], v7 offset:8192\n"
    "v_mfma_f32_16x16x32_bf16 v[92:95], a[16:19], v[188:191], v[92:95]\n"
    "ds_read_b128 v[232:235], v7 offset:4096\n"
    "ds_read_b128 v[124:127], v7 offset:9216\n"
    "v_mfma_f32_16x16x32_bf16 v[92:95], a[36:39], v[216:219], v[92:95]\n"
    "ds_read_b128 v[128:131], v7 offset:10240\n"
    "ds_read_b128 v[132:135], v7 offset:11264\n"
    "v_mfma_f32_16x16x32_bf16 v[92:95], v[96:99], v[164:167], v[92:95]\n"
    "ds_read_b128 v[148:151], v7 offset:12288\n"
    "ds_read_b128 v[152:155], v7 offset:13312\n"
    "v_mfma_f32_16x16x32_bf16 v[92:95], v[116:119], v[196:199], v[92:95]\n"
    "ds_read_b128 v[156:159], v7 offset:14336\n"
    "s_waitcnt lgkmcnt(13)\n"
    "v_pk_add_f32 v[200:201], v[200:201], v[236:237]\n"
    "s_waitcnt lgkmcnt(4)\n"
    "v_pk_add_f32 v[76:77], v[76:77], v[128:129]\n"
    "v_pk_add_f32 v[202:203], v[202:203], v[238:239]\n"
    "v_pk_add_f32 v[78:79], v[78:79], v[130:131]\n"
    "v_pk_add_f32 v[220:221], v[220:221], v[240:241]\n"
    "s_waitcnt lgkmcnt(3)\n"
    "v_pk_add_f32 v[80:81], v[80:81], v[132:133]\n"
    "v_pk_add_f32 v[222:223], v[222:223], v[242:243]\n"
    "v_pk_add_f32 v[82:83], v[82:83], v[134:135]\n"
    "v_pk_add_f32 v[76:77], v[76:77], v[200:201]\n"
    "v_pk_add_f32 v[78:79], v[78:79], v[202:203]\n"
    "v_pk_add_f32 v[80:81], v[80:81], v[220:221]\n"
    "v_pk_add_f32 v[82:83], v[82:83], v[222:223]\n"
    "v_pk_add_f32 v[224:225], v[224:225], v[244:245]\n"
    "s_waitcnt lgkmcnt(2)\n"
    "v_pk_add_f32 v[84:85], v[84:85], v[148:149]\n"
    "v_pk_add_f32 v[226:227], v[226:227], v[246:247]\n"
    "v_pk_add_f32 v[86:87], v[86:87], v[150:151]\n"
    "v_pk_add_f32 v[228:229], v[228:229], v[120:121]\n"
    "s_waitcnt lgkmcnt(1)\n"
    "v_pk_add_f32 v[88:89], v[88:89], v[152:153]\n"
    "v_pk_add_f32 v[230:231], v[230:231], v[122:123]\n"
    "v_pk_add_f32 v[90:91], v[90:91], v[154:155]\n"
    "v_cvt_pk_bf16_f32 v16, v76, v77\n"
    "v_cvt_pk_bf16_f32 v17, v78, v79\n"
    "v_cvt_pk_bf16_f32 v18, v80, v81\n"
    "v_cvt_pk_bf16_f32 v19, v82, v83\n"
    "v_pk_add_f32 v[84:85], v[84:85], v[224:225]\n"
    "v_pk_add_f32 v[86:87], v[86:87], v[226:227]\n"
    "v_pk_add_f32 v[88:89], v[88:89], v[228:229]\n"
    "v_pk_add_f32 v[90:91], v[90:91], v[230:231]\n"
    "v_pk_add_f32 v[232:233], v[232:233], v[124:125]\n"
    "s_waitcnt lgkmcnt(0)\n"
    "v_pk_add_f32 v[92:93], v[92:93], v[156:157]\n"
    "v_pk_add_f32 v[234:235], v[234:235], v[126:127]\n"
    "v_pk_add_f32 v[94:95], v[94:95], v[158:159]\n"
    "v_cvt_pk_bf16_f32 v20, v84, v85\n"
    "v_cvt_pk_bf16_f32 v21, v86, v87\n"
    "v_cvt_pk_bf16_f32 v22, v88, v89\n"
    "v_cvt_pk_bf16_f32 v23, v90, v91\n"
    "v_pk_add_f32 v[92:93], v[92:93], v[232:233]\n"
    "v_pk_add_f32 v[94:95], v[94:95], v[234:235]\n"
    "v_cvt_pk_bf16_f32 v24, v92, v93\n"
    "v_cvt_pk_bf16_f32 v26, v92, v93\n"
    "v_cvt_pk_bf16_f32 v25, v94, v95\n"
    "v_cvt_pk_bf16_f32 v27, v94, v95\n"
    "v_permlane16_swap_b32 v16, v18\n"
    "v_permlane16_swap_b32_e64 v17, v19\n"
    "buffer_store_dwordx4 v[16:19], v14, s[28:31], s58 offen offset:0\n"
    "v_permlane16_swap_b32 v20, v22\n"
    "v_permlane16_swap_b32 v21, v23\n"
    "buffer_store_dwordx4 v[20:23], v14, s[28:31], s58 offen offset:64\n"
    "v_permlane16_swap_b32 v24, v26\n"
    "v_permlane16_swap_b32 v25, v27\n"
    "buffer_store_dwordx4 v[24:27], v3, s[28:31], s58 offen offset:128\n"
    "s_endpgm\n"
    "Lf2r8_%=:\n"
    "s_mov_b32 s66, 65536\n"
    "Lf2p10_%=:\n"
    "global_load_dwordx2 v[12:13], v112, s[4:5] offset:0 sc1\n"
    "s_waitcnt vmcnt(0)\n"
    "v_cmp_ne_u64 vcc, s[42:43], v[12:13]\n"
    "s_nop 0\n"
    "s_cbranch_vccz Lf2b9_%=\n"
    "s_sub_u32 s66, s66, 1\n"
    "s_cbranch_scc0 Lf2p10_%=\n"
    "s_branch Lf2b9_%=\n"
    "Lf2r12_%=:\n"
    "s_mov_b32 s66, 65536\n"
    "Lf2p14_%=:\n"
    "global_load_dwordx2 v[252:253], v122, s[4:5] offset:0 sc1\n"
    "s_waitcnt vmcnt(0)\n"
    "v_cmp_ne_u64 vcc, s[42:43], v[252:253]\n"
    "s_nop 0\n"
    "s_cbranch_vccz Lf2b13_%=\n"
    "s_sub_u32 s66, s66, 1\n"
    "s_cbranch_scc0 Lf2p14_%=\n"
    "s_branch Lf2b13_%=\n"
    "Lrt18_%=:\n"
    "s_mov_b32 s66, 65536\n"
    "Lrp20_%=:\n"
    "s_add_u32 s67, s53, 0\n"
    "buffer_load_dwordx2 v[52:53], v2, s[4:7], s67 offen offset:0 sc1\n"
    "s_add_u32 s67, s53, 0\n"
    "buffer_load_dwordx2 v[54:55], v2, s[4:7], s67 offen offset:512 sc1\n"
    "s_add_u32 s67, s61, 0\n"
    "buffer_load_dwordx4 v[36:39], v1, s[4:7], s67 offen offset:0 sc1\n"
    "s_add_u32 s67, s61, 0\n"
    "buffer_load_dwordx4 v[40:43], v1, s[4:7], s67 offen offset:1024 sc1\n"
    "s_add_u32 s67, s61, 0\n"
    "buffer_load_dwordx4 v[44:47], v1, s[4:7], s67 offen offset:2048 sc1\n"
    "s_add_u32 s67, s61, 0\n"
    "buffer_load_dwordx4 v[48:51], v1, s[4:7], s67 offen offset:3072 sc1\n"
    "s_waitcnt vmcnt(0)\n"
    "v_max3_u32 v15, v52, v53, v54\n"
    "v_max3_u32 v15, v15, v55, v36\n"
    "v_max3_u32 v15, v15, v37, v38\n"
    "v_max3_u32 v15, v15, v39, v40\n"
    "v_max3_u32 v15, v15, v41, v42\n"
    "v_max3_u32 v15, v15, v43, v44\n"
    "v_max3_u32 v15, v15, v45, v46\n"
    "v_max3_u32 v15, v15, v47, v48\n"
    "v_max3_u32 v15, v15, v49, v50\n"
    "v_max_u32 v15, v15, v51\n"
    "v_cmp_eq_u32 vcc, -1, v15\n"
    "s_nop 0\n"
    "s_cbranch_vccz Lbk19_%=\n"
    "s_sub_u32 s66, s66, 1\n"
    "s_cbranch_scc0 Lrp20_%=\n"
    "s_branch Lbk19_%=\n"
    "Lrt21_%=:\n"
    "s_mov_b32 s66, 65536\n"
    "Lrp23_%=:\n"
    "s_add_u32 s67, s54, 0\n"
    "buffer_load_dwordx2 v[72:73], v2, s[4:7], s67 offen offset:0 sc1\n"
    "s_add_u32 s67, s54, 0\n"
    "buffer_load_dwordx2 v[74:75], v2, s[4:7], s67 offen offset:512 sc1\n"
    "s_add_u32 s67, s62, 0\n"
    "buffer_load_dwordx4 v[56:59], v1, s[4:7], s67 offen offset:0 sc1\n"
    "s_add_u32 s67, s62, 0\n"
    "buffer_load_dwordx4 v[60:63], v1, s[4:7], s67 offen offset:1024 sc1\n"
    "s_add_u32 s67, s62, 0\n"
    "buffer_load_dwordx4 v[64:67], v1, s[4:7], s67 offen offset:2048 sc1\n"
    "s_add_u32 s67, s62, 0\n"
    "buffer_load_dwordx4 v[68:71], v1, s[4:7], s67 offen offset:3072 sc1\n"
    "s_waitcnt vmcnt(0)\n"
    "v_max3_u32 v15, v72, v73, v74\n"
    "v_max3_u32 v15, v15, v75, v56\n"
    "v_max3_u32 v15, v15, v57, v58\n"
    "v_max3_u32 v15, v15, v59, v60\n"
    "v_max3_u32 v15, v15, v61, v62\n"
    "v_max3_u32 v15, v15, v63, v64\n"
    "v_max3_u32 v15, v15, v65, v66\n"
    "v_max3_u32 v15, v15, v67, v68\n"
    "v_max3_u32 v15, v15, v69, v70\n"
    "v_max_u32 v15, v15, v71\n"
    "v_cmp_eq_u32 vcc, -1, v15\n"
    "s_nop 0\n"
    "s_cbranch_vccz Lbk22_%=\n"
    "s_sub_u32 s66, s66, 1\n"
    "s_cbranch_scc0 Lrp23_%=\n"
    "s_branch Lbk22_%=\n"
    "Lrt24_%=:\n"
    "s_mov_b32 s66, 65536\n"
    "Lrp26_%=:\n"
    "s_add_u32 s67, s55, 0\n"
    "buffer_load_dwordx2 v[92:93], v2, s[4:7], s67 offen offset:0 sc1\n"
    "s_add_u32 s67, s55, 0\n"
    "buffer_load_dwordx2 v[94:95], v2, s[4:7], s67 offen offset:512 sc1\n"
    "s_add_u32 s67, s63, 0\n"
    "buffer_load_dwordx4 v[76:79], v1, s[4:7], s67 offen offset:0 sc1\n"
    "s_add_u32 s67, s63, 0\n"
    "buffer_load_dwordx4 v[80:83], v1, s[4:7], s67 offen offset:1024 sc1\n"
    "s_add_u32 s67, s63, 0\n"
    "buffer_load_dwordx4 v[84:87], v1, s[4:7], s67 offen offset:2048 sc1\n"
    "s_add_u32 s67, s63, 0\n"
    "buffer_load_dwordx4 v[88:91], v1, s[4:7], s67 offen offset:3072 sc1\n"
    "s_waitcnt vmcnt(0)\n"
    "v_max3_u32 v15, v92, v93, v94\n"
    "v_max3_u32 v15, v15, v95, v76\n"
    "v_max3_u32 v15, v15, v77, v78\n"
    "v_max3_u32 v15, v15, v79, v80\n"
    "v_max3_u32 v15, v15, v81, v82\n"
    "v_max3_u32 v15, v15, v83, v84\n"
    "v_max3_u32 v15, v15, v85, v86\n"
    "v_max3_u32 v15, v15, v87, v88\n"
    "v_max3_u32 v15, v15, v89, v90\n"
    "v_max_u32 v15, v15, v91\n"
    "v_cmp_eq_u32 vcc, -1, v15\n"
    "s_nop 0\n"
    "s_cbranch_vccz Lbk25_%=\n"
    "s_sub_u32 s66, s66, 1\n"
    "s_cbranch_scc0 Lrp26_%=\n"
    "s_branch Lbk25_%=\n"
    "Lrt30_%=:\n"
    "s_mov_b32 s66, 65536\n"
    "Lrp32_%=:\n"
    "s_add_u32 s67, s60, 0\n"
    "buffer_load_dwordx4 v[24:27], v1, s[4:7], s67 offen offset:1024 sc1\n"
    "s_add_u32 s67, s60, 0\n"
    "buffer_load_dwordx4 v[28:31], v3, s[4:7], s67 offen offset:1024 sc1\n"
    "s_waitcnt vmcnt(0)\n"
    "v_max3_u32 v15, v24, v25, v26\n"
    "v_max3_u32 v15, v15, v27, v28\n"
    "v_max3_u32 v15, v15, v29, v30\n"
    "v_max_u32 v15, v15, v31\n"
    "v_cmp_eq_u32 vcc, -1, v15\n"
    "s_nop 0\n"
    "s_cbranch_vccz Lbk31_%=\n"
    "s_sub_u32 s66, s66, 1\n"
    "s_cbranch_scc0 Lrp32_%=\n"
    "s_branch Lbk31_%=\n"
    "Lrt33_%=:\n"
    "s_mov_b32 s66, 65536\n"
    "Lrp35_%=:\n"
    "s_add_u32 s67, s60, 0\n"
    "buffer_load_dwordx4 v[32:35], v1, s[4:7], s67 offen offset:2048 sc1\n"
    "s_add_u32 s67, s60, 0\n"
    "buffer_load_dwordx4 v[36:39], v3, s[4:7], s67 offen offset:2048 sc1\n"
    "s_waitcnt vmcnt(0)\n"
    "v_max3_u32 v15, v32, v33, v34\n"
    "v_max3_u32 v15, v15, v35, v36\n"
    "v_max3_u32 v15, v15, v37, v38\n"
    "v_max_u32 v15, v15, v39\n"
    "v_cmp_eq_u32 vcc, -1, v15\n"
    "s_nop 0\n"
    "s_cbranch_vccz Lbk34_%=\n"
    "s_sub_u32 s66, s66, 1\n"
    "s_cbranch_scc0 Lrp35_%=\n"
    "s_branch Lbk34_%=\n"
    "Lrt36_%=:\n"
    "s_mov_b32 s66, 65536\n"
    "Lrp38_%=:\n"
    "s_add_u32 s67, s60, 0\n"
    "buffer_load_dwordx4 v[40:43], v1, s[4:7], s67 offen offset:3072 sc1\n"
    "s_add_u32 s67, s60, 0\n"
    "buffer_load_dwordx4 v[44:47], v3, s[4:7], s67 offen offset:3072 sc1\n"
    "s_waitcnt vmcnt(0)\n"
    "v_max3_u32 v15, v40, v41, v42\n"
    "v_max3_u32 v15, v15, v43, v44\n"
    "v_max3_u32 v15, v15, v45, v46\n"
    "v_max_u32 v15, v15, v47\n"
    "v_cmp_eq_u32 vcc, -1, v15\n"
    "s_nop 0\n"
    "s_cbranch_vccz Lbk37_%=\n"
    "s_sub_u32 s66, s66, 1\n"
    "s_cbranch_scc0 Lrp38_%=\n"
    "s_branch Lbk37_%=\n"
    "Lrt39_%=:\n"
    "s_mov_b32 s66, 65536\n"
    "Lrp41_%=:\n"
    "s_add_u32 s67, s60, 4096\n"
    "buffer_load_dwordx4 v[48:51], v1, s[4:7], s67 offen offset:0 sc1\n"
    "s_add_u32 s67, s60, 4096\n"
    "buffer_load_dwordx4 v[52:55], v3, s[4:7], s67 offen offset:0 sc1\n"
    "s_waitcnt vmcnt(0)\n"
    "v_max3_u32 v15, v48, v49, v50\n"
    "v_max3_u32 v15, v15, v51, v52\n"
    "v_max3_u32 v15, v15, v53, v54\n"
    "v_max_u32 v15, v15, v55\n"
    "v_cmp_eq_u32 vcc, -1, v15\n"
    "s_nop 0\n"
    "s_cbranch_vccz Lbk40_%=\n"
    "s_sub_u32 s66, s66, 1\n"
    "s_cbranch_scc0 Lrp41_%=\n"
    "s_branch Lbk40_%=\n"
    "Lrt42_%=:\n"
    "s_mov_b32 s66, 65536\n"
    "Lrp44_%=:\n"
    "s_add_u32 s67, s60, 4096\n"
    "buffer_load_dwordx4 v[56:59], v1, s[4:7], s67 offen offset:1024 sc1\n"
    "s_add_u32 s67, s60, 4096\n"
    "buffer_load_dwordx4 v[60:63], v3, s[4:7], s67 offen offset:1024 sc1\n"
    "s_waitcnt vmcnt(0)\n"
    "v_max3_u32 v15, v56, v57, v58\n"
    "v_max3_u32 v15, v15, v59, v60\n"
    "v_max3_u32 v15, v15, v61, v62\n"
    "v_max_u32 v15, v15, v63\n"
    "v_cmp_eq_u32 vcc, -1, v15\n"
    "s_nop 0\n"
    "s_cbranch_vccz Lbk43_%=\n"
    "s_sub_u32 s66, s66, 1\n"
    "s_cbranch_scc0 Lrp44_%=\n"
    "s_branch Lbk43_%=\n"
    "Lrt45_%=:\n"
    "s_mov_b32 s66, 65536\n"
    "Lrp47_%=:\n"
    "s_add_u32 s67, s60, 4096\n"
    "buffer_load_dwordx4 v[64:67], v1, s[4:7], s67 offen offset:2048 sc1\n"
    "s_add_u32 s67, s60, 4096\n"
    "buffer_load_dwordx4 v[68:71], v3, s[4:7], s67 offen offset:2048 sc1\n"
    "s_waitcnt vmcnt(0)\n"
    "v_max3_u32 v15, v64, v65, v66\n"
    "v_max3_u32 v15, v15, v67, v68\n"
    "v_max3_u32 v15, v15, v69, v70\n"
    "v_max_u32 v15, v15, v71\n"
    "v_cmp_eq_u32 vcc, -1, v15\n"
    "s_nop 0\n"
    "s_cbranch_vccz Lbk46_%=\n"
    "s_sub_u32 s66, s66, 1\n"
    "s_cbranch_scc0 Lrp47_%=\n"
    "s_branch Lbk46_%=\n"
    "Lrt48_%=:\n"
    "s_mov_b32 s66, 65536\n"
    "Lrp50_%=:\n"
    "s_add_u32 s67, s60, 4096\n"
    "buffer_load_dwordx4 v[72:75], v1, s[4:7], s67 offen offset:3072 sc1\n"
    "s_add_u32 s67, s60, 4096\n"
    "buffer_load_dwordx4 v[76:79], v3, s[4:7], s67 offen offset:3072 sc1\n"
    "s_waitcnt vmcnt(0)\n"
    "v_max3_u32 v15, v72, v73, v74\n"
    "v_max3_u32 v15, v15, v75, v76\n"
    "v_max3_u32 v15, v15, v77, v78\n"
    "v_max_u32 v15, v15, v79\n"
    "v_cmp_eq_u32 vcc, -1, v15\n"
    "s_nop 0\n"
    "s_cbranch_vccz Lbk49_%=\n"
    "s_sub_u32 s66, s66, 1\n"
    "s_cbranch_scc0 Lrp50_%=\n"
    "s_branch Lbk49_%=\n"
    "Lrt51_%=:\n"
    "s_mov_b32 s66, 65536\n"
    "Lrp53_%=:\n"
    "s_add_u32 s67, s60, 8192\n"
    "buffer_load_dwordx4 v[80:83], v1, s[4:7], s67 offen offset:0 sc1\n"
    "s_add_u32 s67, s60, 8192\n"
    "buffer_load_dwordx4 v[84:87], v3, s[4:7], s67 offen offset:0 sc1\n"
    "s_waitcnt vmcnt(0)\n"
    "v_max3_u32 v15, v80, v81, v82\n"
    "v_max3_u32 v15, v15, v83, v84\n"
    "v_max3_u32 v15, v15, v85, v86\n"
    "v_max_u32 v15, v15, v87\n"
    "v_cmp_eq_u32 vcc, -1, v15\n"
    "s_nop 0\n"
    "s_cbranch_vccz Lbk52_%=\n"
    "s_sub_u32 s66, s66, 1\n"
    "s_cbranch_scc0 Lrp53_%=\n"
    "s_branch Lbk52_%=\n"
    "Lrt54_%=:\n"
    "s_mov_b32 s66, 65536\n"
    "Lrp56_%=:\n"
    "s_add_u32 s67, s60, 8192\n"
    "buffer_load_dwordx4 v[88:91], v1, s[4:7], s67 offen offset:1024 sc1\n"
    "s_add_u32 s67, s60, 8192\n"
    "buffer_load_dwordx4 v[92:95], v3, s[4:7], s67 offen offset:1024 sc1\n"
    "s_waitcnt vmcnt(0)\n"
    "v_max3_u32 v15, v88, v89, v90\n"
    "v_max3_u32 v15, v15, v91, v92\n"
    "v_max3_u32 v15, v15, v93, v94\n"
    "v_max_u32 v15, v15, v95\n"
    "v_cmp_eq_u32 vcc, -1, v15\n"
    "s_nop 0\n"
    "s_cbranch_vccz Lbk55_%=\n"
    "s_sub_u32 s66, s66, 1\n"
    "s_cbranch_scc0 Lrp56_%=\n"
    "s_branch Lbk55_%=\n"
    "Lrt57_%=:\n"
    "s_mov_b32 s66, 65536\n"
    "Lrp59_%=:\n"
    "s_add_u32 s67, s60, 8192\n"
    "buffer_load_dwordx4 v[96:99], v1, s[4:7], s67 offen offset:2048 sc1\n"
    "s_add_u32 s67, s60, 8192\n"
    "buffer_load_dwordx4 v[100:103], v3, s[4:7], s67 offen offset:2048 sc1\n"
    "s_waitcnt vmcnt(0)\n"
    "v_max3_u32 v15, v96, v97, v98\n"
    "v_max3_u32 v15, v15, v99, v100\n"
    "v_max3_u32 v15, v15, v101, v102\n"
    "v_max_u32 v15, v15, v103\n"
    "v_cmp_eq_u32 vcc, -1, v15\n"
    "s_nop 0\n"
    "s_cbranch_vccz Lbk58_%=\n"
    "s_sub_u32 s66, s66, 1\n"
    "s_cbranch_scc0 Lrp59_%=\n"
    "s_branch Lbk58_%=\n"
    "Lrt60_%=:\n"
    "s_mov_b32 s66, 65536\n"
    "Lrp62_%=:\n"
    "s_add_u32 s67, s60, 8192\n"
    "buffer_load_dwordx4 v[104:107], v1, s[4:7], s67 offen offset:3072 sc1\n"
    "s_add_u32 s67, s60, 8192\n"
    "buffer_load_dwordx4 v[108:111], v3, s[4:7], s67 offen offset:3072 sc1\n"
    "s_waitcnt vmcnt(0)\n"
    "v_max3_u32 v15, v104, v105, v106\n"
    "v_max3_u32 v15, v15, v107, v108\n"
    "v_max3_u32 v15, v15, v109, v110\n"
    "v_max_u32 v15, v15, v111\n"
    "v_cmp_eq_u32 vcc, -1, v15\n"
    "s_nop 0\n"
    "s_cbranch_vccz Lbk61_%=\n"
    "s_sub_u32 s66, s66, 1\n"
    "s_cbranch_scc0 Lrp62_%=\n"
    "s_branch Lbk61_%=\n"
    "Lrt63_%=:\n"
    "s_mov_b32 s66, 65536\n"
    "Lrp65_%=:\n"
    "s_add_u32 s67, s60, 12288\n"
    "buffer_load_dwordx4 v[112:115], v1, s[4:7], s67 offen offset:0 sc1\n"
    "s_add_u32 s67, s60, 12288\n"
    "buffer_load_dwordx4 v[116:119], v3, s[4:7], s67 offen offset:0 sc1\n"
    "s_waitcnt vmcnt(0)\n"
    "v_max3_u32 v15, v112, v113, v114\n"
    "v_max3_u32 v15, v15, v115, v116\n"
    "v_max3_u32 v15, v15, v117, v118\n"
    "v_max_u32 v15, v15, v119\n"
    "v_cmp_eq_u32 vcc, -1, v15\n"
    "s_nop 0\n"
    "s_cbranch_vccz Lbk64_%=\n"
    "s_sub_u32 s66, s66, 1\n"
    "s_cbranch_scc0 Lrp65_%=\n"
    "s_branch Lbk64_%=\n"
    "Lrt66_%=:\n"
    "s_mov_b32 s66, 65536\n"
    "Lrp68_%=:\n"
    "s_add_u32 s67, s60, 12288\n"
    "buffer_load_dwordx4 v[120:123], v1, s[4:7], s67 offen offset:1024 sc1\n"
    "s_add_u32 s67, s60, 12288\n"
    "buffer_load_dwordx4 v[124:127], v3, s[4:7], s67 offen offset:1024 sc1\n"
    "s_waitcnt vmcnt(0)\n"
    "v_max3_u32 v15, v120, v121, v122\n"
    "v_max3_u32 v15, v15, v123, v124\n"
    "v_max3_u32 v15, v15, v125, v126\n"
    "v_max_u32 v15, v15, v127\n"
    "v_cmp_eq_u32 vcc, -1, v15\n"
    "s_nop 0\n"
    "s_cbranch_vccz Lbk67_%=\n"
    "s_sub_u32 s66, s66, 1\n"
    "s_cbranch_scc0 Lrp68_%=\n"
    "s_branch Lbk67_%=\n"
    "Lrt69_%=:\n"
    "s_mov_b32 s66, 65536\n"
    "Lrp71_%=:\n"
    "s_add_u32 s67, s60, 12288\n"
    "buffer_load_dwordx4 v[128:131], v1, s[4:7], s67 offen offset:2048 sc1\n"
    "s_add_u32 s67, s60, 12288\n"
    "buffer_load_dwordx4 v[132:135], v3, s[4:7], s67 offen offset:2048 sc1\n"
    "s_waitcnt vmcnt(0)\n"
    "v_max3_u32 v15, v128, v129, v130\n"
    "v_max3_u32 v15, v15, v131, v132\n"
    "v_max3_u32 v15, v15, v133, v134\n"
    "v_max_u32 v15, v15, v135\n"
    "v_cmp_eq_u32 vcc, -1, v15\n"
    "s_nop 0\n"
    "s_cbranch_vccz Lbk70_%=\n"
    "s_sub_u32 s66, s66, 1\n"
    "s_cbranch_scc0 Lrp71_%=\n"
    "s_branch Lbk70_%=\n"
    "Lrt72_%=:\n"
    "s_mov_b32 s66, 65536\n"
    "Lrp74_%=:\n"
    "s_add_u32 s67, s60, 12288\n"
    "buffer_load_dwordx4 v[136:139], v1, s[4:7], s67 offen offset:3072 sc1\n"
    "s_add_u32 s67, s60, 12288\n"
    "buffer_load_dwordx4 v[140:143], v3, s[4:7], s67 offen offset:3072 sc1\n"
    "s_waitcnt vmcnt(0)\n"
    "v_max3_u32 v15, v136, v137, v138\n"
    "v_max3_u32 v15, v15, v139, v140\n"
    "v_max3_u32 v15, v15, v141, v142\n"
    "v_max_u32 v15, v15, v143\n"
    "v_cmp_eq_u32 vcc, -1, v15\n"
    "s_nop 0\n"
    "s_cbranch_vccz Lbk73_%=\n"
    "s_sub_u32 s66, s66, 1\n"
    "s_cbranch_scc0 Lrp74_%=\n"
    "s_branch Lbk73_%=\n"
    "Lrt75_%=:\n"
    "s_mov_b32 s66, 65536\n"
    "Lrp77_%=:\n"
    "s_add_u32 s67, s60, 16384\n"
    "buffer_load_dwordx4 v[144:147], v1, s[4:7], s67 offen offset:0 sc1\n"
    "s_add_u32 s67, s60, 16384\n"
    "buffer_load_dwordx4 v[148:151], v3, s[4:7], s67 offen offset:0 sc1\n"
    "s_waitcnt vmcnt(0)\n"
    "v_max3_u32 v15, v144, v145, v146\n"
    "v_max3_u32 v15, v15, v147, v148\n"
    "v_max3_u32 v15, v15, v149, v150\n"
    "v_max_u32 v15, v15, v151\n"
    "v_cmp_eq_u32 vcc, -1, v15\n"
    "s_nop 0\n"
    "s_cbranch_vccz Lbk76_%=\n"
    "s_sub_u32 s66, s66, 1\n"
    "s_cbranch_scc0 Lrp77_%=\n"
    "s_branch Lbk76_%=\n"
    "Lrt78_%=:\n"
    "s_mov_b32 s66, 65536\n"
    "Lrp80_%=:\n"
    "s_add_u32 s67, s60, 16384\n"
    "buffer_load_dwordx4 v[152:155], v1, s[4:7], s67 offen offset:1024 sc1\n"
    "s_add_u32 s67, s60, 16384\n"
    "buffer_load_dwordx4 v[156:159], v3, s[4:7], s67 offen offset:1024 sc1\n"
    "s_waitcnt vmcnt(0)\n"
    "v_max3_u32 v15, v152, v153, v154\n"
    "v_max3_u32 v15, v15, v155, v156\n"
    "v_max3_u32 v15, v15, v157, v158\n"
    "v_max_u32 v15, v15, v159\n"
    "v_cmp_eq_u32 vcc, -1, v15\n"
    "s_nop 0\n"
    "s_cbranch_vccz Lbk79_%=\n"
    "s_sub_u32 s66, s66, 1\n"
    "s_cbranch_scc0 Lrp80_%=\n"
    "s_branch Lbk79_%=\n"
    "Lrt81_%=:\n"
    "s_mov_b32 s66, 65536\n"
    "Lrp83_%=:\n"
    "s_add_u32 s67, s60, 16384\n"
    "buffer_load_dwordx4 v[160:163], v1, s[4:7], s67 offen offset:2048 sc1\n"
    "s_add_u32 s67, s60, 16384\n"
    "buffer_load_dwordx4 v[164:167], v3, s[4:7], s67 offen offset:2048 sc1\n"
    "s_waitcnt vmcnt(0)\n"
    "v_max3_u32 v15, v160, v161, v162\n"
    "v_max3_u32 v15, v15, v163, v164\n"
    "v_max3_u32 v15, v15, v165, v166\n"
    "v_max_u32 v15, v15, v167\n"
    "v_cmp_eq_u32 vcc, -1, v15\n"
    "s_nop 0\n"
    "s_cbranch_vccz Lbk82_%=\n"
    "s_sub_u32 s66, s66, 1\n"
    "s_cbranch_scc0 Lrp83_%=\n"
    "s_branch Lbk82_%=\n"
    "Lrt84_%=:\n"
    "s_mov_b32 s66, 65536\n"
    "Lrp86_%=:\n"
    "s_add_u32 s67, s60, 16384\n"
    "buffer_load_dwordx4 v[168:171], v1, s[4:7], s67 offen offset:3072 sc1\n"
    "s_add_u32 s67, s60, 16384\n"
    "buffer_load_dwordx4 v[172:175], v3, s[4:7], s67 offen offset:3072 sc1\n"
    "s_waitcnt vmcnt(0)\n"
    "v_max3_u32 v15, v168, v169, v170\n"
    "v_max3_u32 v15, v15, v171, v172\n"
    "v_max3_u32 v15, v15, v173, v174\n"
    "v_max_u32 v15, v15, v175\n"
    "v_cmp_eq_u32 vcc, -1, v15\n"
    "s_nop 0\n"
    "s_cbranch_vccz Lbk85_%=\n"
    "s_sub_u32 s66, s66, 1\n"
    "s_cbranch_scc0 Lrp86_%=\n"
    "s_branch Lbk85_%=\n"
    "Lrt87_%=:\n"
    "s_mov_b32 s66, 65536\n"
    "Lrp116_%=:\n"
    "s_add_u32 s67, s44, 0\n"
    "buffer_load_dwordx4 v[120:123], v252, s[4:7], s67 offen offset:0 sc1\n"
    "s_add_u32 s67, s44, 0\n"
    "buffer_load_dwordx4 v[124:127], v253, s[4:7], s67 offen offset:0 sc1\n"
    "s_add_u32 s67, s44, 0\n"
    "buffer_load_dwordx4 v[128:131], v254, s[4:7], s67 offen offset:0 sc1\n"
    "s_add_u32 s67, s44, 0\n"
    "buffer_load_dwordx4 v[132:135], v255, s[4:7], s67 offen offset:0 sc1\n"
    "s_waitcnt vmcnt(0)\n"
    "v_max3_u32 v15, v120, v121, v122\n"
    "v_max3_u32 v15, v15, v123, v124\n"
    "v_max3_u32 v15, v15, v125, v126\n"
    "v_max3_u32 v15, v15, v127, v128\n"
    "v_max3_u32 v15, v15, v129, v130\n"
    "v_max3_u32 v15, v15, v131, v132\n"
    "v_max3_u32 v15, v15, v133, v134\n"
    "v_max_u32 v15, v15, v135\n"
    "v_cmp_eq_u32 vcc, -1, v15\n"
    "s_nop 0\n"
    "s_cbranch_vccz Lsg115_%=\n"
    "s_sub_u32 s66, s66, 1\n"
    "s_cbranch_scc0 Lrp116_%=\n"
    "Lsg115_%=:\n"
    "s_mov_b32 s66, 1073741824\n"
    "s_add_u32 s67, s44, 0\n"
    "buffer_load_dwordx4 v[148:151], v252, s[4:7], s67 offen offset:3072 sc1\n"
    "s_add_u32 s67, s44, 0\n"
    "buffer_load_dwordx4 v[152:155], v253, s[4:7], s67 offen offset:3072 sc1\n"
    "s_add_u32 s67, s44, 0\n"
    "buffer_load_dwordx4 v[156:159], v254, s[4:7], s67 offen offset:3072 sc1\n"
    "buffer_load_dword v9, v1, s[4:7], s66 offen\n"
    "s_add_u32 s67, s44, 0\n"
    "buffer_load_dwordx4 v[160:163], v255, s[4:7], s67 offen offset:3072 sc1\n"
    "s_add_u32 s67, s44, 4096\n"
    "buffer_load_dwordx4 v[176:179], v252, s[4:7], s67 offen offset:2048 sc1\n"
    "s_add_u32 s67, s44, 4096\n"
    "buffer_load_dwordx4 v[180:183], v253, s[4:7], s67 offen offset:2048 sc1\n"
    "s_add_u32 s67, s44, 4096\n"
    "buffer_load_dwordx4 v[184:187], v254, s[4:7], s67 offen offset:2048 sc1\n"
    "s_add_u32 s67, s44, 4096\n"
    "buffer_load_dwordx4 v[188:191], v255, s[4:7], s67 offen offset:2048 sc1\n"
    "s_branch Lbk88_%=\n"
    "Lrt89_%=:\n"
    "s_mov_b32 s66, 65536\n"
    "Lrp118_%=:\n"
    "s_add_u32 s67, s44, 0\n"
    "buffer_load_dwordx4 v[148:151], v252, s[4:7], s67 offen offset:3072 sc1\n"
    "s_add_u32 s67, s44, 0\n"
    "buffer_load_dwordx4 v[152:155], v253, s[4:7], s67 offen offset:3072 sc1\n"
    "s_add_u32 s67, s44, 0\n"
    "buffer_load_dwordx4 v[156:159], v254, s[4:7], s67 offen offset:3072 sc1\n"
    "s_add_u32 s67, s44, 0\n"
    "buffer_load_dwordx4 v[160:163], v255, s[4:7], s67 offen offset:3072 sc1\n"
    "s_waitcnt vmcnt(0)\n"
    "v_max3_u32 v15, v148, v149, v150\n"
    "v_max3_u32 v15, v15, v151, v152\n"
    "v_max3_u32 v15, v15, v153, v154\n"
    "v_max3_u32 v15, v15, v155, v156\n"
    "v_max3_u32 v15, v15, v157, v158\n"
    "v_max3_u32 v15, v15, v159, v160\n"
    "v_max3_u32 v15, v15, v161, v162\n"
    "v_max_u32 v15, v15, v163\n"
    "v_cmp_eq_u32 vcc, -1, v15\n"
    "s_nop 0\n"
    "s_cbranch_vccz Lsg117_%=\n"
    "s_sub_u32 s66, s66, 1\n"
    "s_cbranch_scc0 Lrp118_%=\n"
    "Lsg117_%=:\n"
    "s_mov_b32 s66, 1073741824\n"
    "s_add_u32 s67, s44, 4096\n"
    "buffer_load_dwordx4 v[176:179], v252, s[4:7], s67 offen offset:2048 sc1\n"
    "s_add_u32 s67, s44, 4096\n"
    "buffer_load_dwordx4 v[180:183], v253, s[4:7], s67 offen offset:2048 sc1\n"
    "s_add_u32 s67, s44, 4096\n"
    "buffer_load_dwordx4 v[184:187], v254, s[4:7], s67 offen offset:2048 sc1\n"
    "s_add_u32 s67, s44, 4096\n"
    "buffer_load_dwordx4 v[188:191], v255, s[4:7], s67 offen offset:2048 sc1\n"
    "s_add_u32 s67, s44, 8192\n"
    "buffer_load_dwordx4 v[204:207], v252, s[4:7], s67 offen offset:1024 sc1\n"
    "s_add_u32 s67, s44, 8192\n"
    "buffer_load_dwordx4 v[208:211], v253, s[4:7], s67 offen offset:1024 sc1\n"
    "s_add_u32 s67, s44, 8192\n"
    "buffer_load_dwordx4 v[212:215], v254, s[4:7], s67 offen offset:1024 sc1\n"
    "s_add_u32 s67, s44, 8192\n"
    "buffer_load_dwordx4 v[216:219], v255, s[4:7], s67 offen offset:1024 sc1\n"
    "s_add_u32 s67, s44, 0\n"
    "buffer_load_dwordx4 v[136:139], v252, s[4:7], s67 offen offset:1024 sc1\n"
    "s_branch Lbk90_%=\n"
    "Lrt91_%=:\n"
    "s_mov_b32 s66, 65536\n"
    "Lrp120_%=:\n"
    "s_add_u32 s67, s44, 4096\n"
    "buffer_load_dwordx4 v[176:179], v252, s[4:7], s67 offen offset:2048 sc1\n"
    "s_add_u32 s67, s44, 4096\n"
    "buffer_load_dwordx4 v[180:183], v253, s[4:7], s67 offen offset:2048 sc1\n"
    "s_add_u32 s67, s44, 4096\n"
    "buffer_load_dwordx4 v[184:187], v254, s[4:7], s67 offen offset:2048 sc1\n"
    "s_add_u32 s67, s44, 4096\n"
    "buffer_load_dwordx4 v[188:191], v255, s[4:7], s67 offen offset:2048 sc1\n"
    "s_waitcnt vmcnt(0)\n"
    "v_max3_u32 v15, v176, v177, v178\n"
    "v_max3_u32 v15, v15, v179, v180\n"
    "v_max3_u32 v15, v15, v181, v182\n"
    "v_max3_u32 v15, v15, v183, v184\n"
    "v_max3_u32 v15, v15, v185, v186\n"
    "v_max3_u32 v15, v15, v187, v188\n"
    "v_max3_u32 v15, v15, v189, v190\n"
    "v_max_u32 v15, v15, v191\n"
    "v_cmp_eq_u32 vcc, -1, v15\n"
    "s_nop 0\n"
    "s_cbranch_vccz Lsg119_%=\n"
    "s_sub_u32 s66, s66, 1\n"
    "s_cbranch_scc0 Lrp120_%=\n"
    "Lsg119_%=:\n"
    "s_mov_b32 s66, 1073741824\n"
    "s_add_u32 s67, s44, 8192\n"
    "buffer_load_dwordx4 v[204:207], v252, s[4:7], s67 offen offset:1024 sc1\n"
    "s_add_u32 s67, s44, 8192\n"
    "buffer_load_dwordx4 v[208:211], v253, s[4:7], s67 offen offset:1024 sc1\n"
    "s_add_u32 s67, s44, 8192\n"
    "buffer_load_dwordx4 v[212:215], v254, s[4:7], s67 offen offset:1024 sc1\n"
    "s_add_u32 s67, s44, 8192\n"
    "buffer_load_dwordx4 v[216:219], v255, s[4:7], s67 offen offset:1024 sc1\n"
    "s_add_u32 s67, s44, 0\n"
    "buffer_load_dwordx4 v[136:139], v252, s[4:7], s67 offen offset:1024 sc1\n"
    "s_add_u32 s67, s44, 0\n"
    "buffer_load_dwordx4 v[140:143], v253, s[4:7], s67 offen offset:1024 sc1\n"
    "s_add_u32 s67, s44, 0\n"
    "buffer_load_dwordx4 v[144:147], v254, s[4:7], s67 offen offset:1024 sc1\n"
    "s_add_u32 s67, s44, 0\n"
    "buffer_load_dwordx4 v[164:167], v255, s[4:7], s67 offen offset:1024 sc1\n"
    "s_add_u32 s67, s44, 4096\n"
    "buffer_load_dwordx4 v[168:171], v252, s[4:7], s67 offen offset:0 sc1\n"
    "s_add_u32 s67, s44, 4096\n"
    "buffer_load_dwordx4 v[172:175], v253, s[4:7], s67 offen offset:0 sc1\n"
    "s_add_u32 s67, s44, 4096\n"
    "buffer_load_dwordx4 v[192:195], v254, s[4:7], s67 offen offset:0 sc1\n"
    "s_add_u32 s67, s44, 4096\n"
    "buffer_load_dwordx4 v[196:199], v255, s[4:7], s67 offen offset:0 sc1\n"
    "s_add_u32 s67, s44, 4096\n"
    "buffer_load_dwordx4 v[200:203], v252, s[4:7], s67 offen offset:3072 sc1\n"
    "s_branch Lbk92_%=\n"
    "Lrt93_%=:\n"
    "s_mov_b32 s66, 65536\n"
    "Lrp122_%=:\n"
    "s_add_u32 s67, s44, 8192\n"
    "buffer_load_dwordx4 v[204:207], v252, s[4:7], s67 offen offset:1024 sc1\n"
    "s_add_u32 s67, s44, 8192\n"
    "buffer_load_dwordx4 v[208:211], v253, s[4:7], s67 offen offset:1024 sc1\n"
    "s_add_u32 s67, s44, 8192\n"
    "buffer_load_dwordx4 v[212:215], v254, s[4:7], s67 offen offset:1024 sc1\n"
    "s_add_u32 s67, s44, 8192\n"
    "buffer_load_dwordx4 v[216:219], v255, s[4:7], s67 offen offset:1024 sc1\n"
    "s_waitcnt vmcnt(0)\n"
    "v_max3_u32 v15, v204, v205, v206\n"
    "v_max3_u32 v15, v15, v207, v208\n"
    "v_max3_u32 v15, v15, v209, v210\n"
    "v_max3_u32 v15, v15, v211, v212\n"
    "v_max3_u32 v15, v15, v213, v214\n"
    "v_max3_u32 v15, v15, v215, v216\n"
    "v_max3_u32 v15, v15, v217, v218\n"
    "v_max_u32 v15, v15, v219\n"
    "v_cmp_eq_u32 vcc, -1, v15\n"
    "s_nop 0\n"
    "s_cbranch_vccz Lsg121_%=\n"
    "s_sub_u32 s66, s66, 1\n"
    "s_cbranch_scc0 Lrp122_%=\n"
    "Lsg121_%=:\n"
    "s_mov_b32 s66, 1073741824\n"
    "s_add_u32 s67, s44, 0\n"
    "buffer_load_dwordx4 v[136:139], v252, s[4:7], s67 offen offset:1024 sc1\n"
    "s_add_u32 s67, s44, 0\n"
    "buffer_load_dwordx4 v[140:143], v253, s[4:7], s67 offen offset:1024 sc1\n"
    "s_add_u32 s67, s44, 0\n"
    "buffer_load_dwordx4 v[144:147], v254, s[4:7], s67 offen offset:1024 sc1\n"
    "s_add_u32 s67, s44, 0\n"
    "buffer_load_dwordx4 v[164:167], v255, s[4:7], s67 offen offset:1024 sc1\n"
    "s_add_u32 s67, s44, 4096\n"
    "buffer_load_dwordx4 v[168:171], v252, s[4:7], s67 offen offset:0 sc1\n"
    "s_add_u32 s67, s44, 4096\n"
    "buffer_load_dwordx4 v[172:175], v253, s[4:7], s67 offen offset:0 sc1\n"
    "s_add_u32 s67, s44, 4096\n"
    "buffer_load_dwordx4 v[192:195], v254, s[4:7], s67 offen offset:0 sc1\n"
    "s_add_u32 s67, s44, 4096\n"
    "buffer_load_dwordx4 v[196:199], v255, s[4:7], s67 offen offset:0 sc1\n"
    "s_add_u32 s67, s44, 4096\n"
    "buffer_load_dwordx4 v[200:203], v252, s[4:7], s67 offen offset:3072 sc1\n"
    "s_add_u32 s67, s44, 4096\n"
    "buffer_load_dwordx4 v[220:223], v253, s[4:7], s67 offen offset:3072 sc1\n"
    "s_add_u32 s67, s44, 4096\n"
    "buffer_load_dwordx4 v[224:227], v254, s[4:7], s67 offen offset:3072 sc1\n"
    "s_add_u32 s67, s44, 4096\n"
    "buffer_load_dwordx4 v[228:231], v255, s[4:7], s67 offen offset:3072 sc1\n"
    "s_add_u32 s67, s44, 8192\n"
    "buffer_load_dwordx4 v[232:235], v252, s[4:7], s67 offen offset:2048 sc1\n"
    "s_add_u32 s67, s44, 8192\n"
    "buffer_load_dwordx4 v[236:239], v253, s[4:7], s67 offen offset:2048 sc1\n"
    "s_add_u32 s67, s44, 8192\n"
    "buffer_load_dwordx4 v[240:243], v254, s[4:7], s67 offen offset:2048 sc1\n"
    "s_branch Lbk94_%=\n"
    "Lrt95_%=:\n"
    "s_mov_b32 s66, 65536\n"
    "Lrp124_%=:\n"
    "s_add_u32 s67, s44, 0\n"
    "buffer_load_dwordx4 v[136:139], v252, s[4:7], s67 offen offset:1024 sc1\n"
    "s_add_u32 s67, s44, 0\n"
    "buffer_load_dwordx4 v[140:143], v253, s[4:7], s67 offen offset:1024 sc1\n"
    "s_add_u32 s67, s44, 0\n"
    "buffer_load_dwordx4 v[144:147], v254, s[4:7], s67 offen offset:1024 sc1\n"
    "s_add_u32 s67, s44, 0\n"
    "buffer_load_dwordx4 v[164:167], v255, s[4:7], s67 offen offset:1024 sc1\n"
    "s_waitcnt vmcnt(0)\n"
    "v_max3_u32 v15, v136, v137, v138\n"
    "v_max3_u32 v15, v15, v139, v140\n"
    "v_max3_u32 v15, v15, v141, v142\n"
    "v_max3_u32 v15, v15, v143, v144\n"
    "v_max3_u32 v15, v15, v145, v146\n"
    "v_max3_u32 v15, v15, v147, v164\n"
    "v_max3_u32 v15, v15, v165, v166\n"
    "v_max_u32 v15, v15, v167\n"
    "v_cmp_eq_u32 vcc, -1, v15\n"
    "s_nop 0\n"
    "s_cbranch_vccz Lsg123_%=\n"
    "s_sub_u32 s66, s66, 1\n"
    "s_cbranch_scc0 Lrp124_%=\n"
    "Lsg123_%=:\n"
    "s_mov_b32 s66, 1073741824\n"
    "s_add_u32 s67, s44, 4096\n"
    "buffer_load_dwordx4 v[168:171], v252, s[4:7], s67 offen offset:0 sc1\n"
    "s_add_u32 s67, s44, 4096\n"
    "buffer_load_dwordx4 v[172:175], v253, s[4:7], s67 offen offset:0 sc1\n"
    "s_add_u32 s67, s44, 4096\n"
    "buffer_load_dwordx4 v[192:195], v254, s[4:7], s67 offen offset:0 sc1\n"
    "s_add_u32 s67, s44, 4096\n"
    "buffer_load_dwordx4 v[196:199], v255, s[4:7], s67 offen offset:0 sc1\n"
    "s_add_u32 s67, s44, 4096\n"
    "buffer_load_dwordx4 v[200:203], v252, s[4:7], s67 offen offset:3072 sc1\n"
    "s_add_u32 s67, s44, 4096\n"
    "buffer_load_dwordx4 v[220:223], v253, s[4:7], s67 offen offset:3072 sc1\n"
    "s_add_u32 s67, s44, 4096\n"
    "buffer_load_dwordx4 v[224:227], v254, s[4:7], s67 offen offset:3072 sc1\n"
    "s_add_u32 s67, s44, 4096\n"
    "buffer_load_dwordx4 v[228:231], v255, s[4:7], s67 offen offset:3072 sc1\n"
    "s_add_u32 s67, s44, 8192\n"
    "buffer_load_dwordx4 v[232:235], v252, s[4:7], s67 offen offset:2048 sc1\n"
    "s_add_u32 s67, s44, 8192\n"
    "buffer_load_dwordx4 v[236:239], v253, s[4:7], s67 offen offset:2048 sc1\n"
    "s_add_u32 s67, s44, 8192\n"
    "buffer_load_dwordx4 v[240:243], v254, s[4:7], s67 offen offset:2048 sc1\n"
    "s_add_u32 s67, s44, 8192\n"
    "buffer_load_dwordx4 v[244:247], v255, s[4:7], s67 offen offset:2048 sc1\n"
    "buffer_load_dword v9, v1, s[4:7], s66 offen\n"
    "buffer_load_dword v9, v1, s[4:7], s66 offen\n"
    "buffer_load_dword v9, v1, s[4:7], s66 offen\n"
    "buffer_load_dword v9, v1, s[4:7], s66 offen\n"
    "s_branch Lbk96_%=\n"
    "Lrt97_%=:\n"
    "s_mov_b32 s66, 65536\n"
    "Lrp126_%=:\n"
    "s_add_u32 s67, s44, 4096\n"
    "buffer_load_dwordx4 v[168:171], v252, s[4:7], s67 offen offset:0 sc1\n"
    "s_add_u32 s67, s44, 4096\n"
    "buffer_load_dwordx4 v[172:175], v253, s[4:7], s67 offen offset:0 sc1\n"
    "s_add_u32 s67, s44, 4096\n"
    "buffer_load_dwordx4 v[192:195], v254, s[4:7], s67 offen offset:0 sc1\n"
    "s_add_u32 s67, s44, 4096\n"
    "buffer_load_dwordx4 v[196:199], v255, s[4:7], s67 offen offset:0 sc1\n"
    "s_waitcnt vmcnt(0)\n"
    "v_max3_u32 v15, v168, v169, v170\n"
    "v_max3_u32 v15, v15, v171, v172\n"
    "v_max3_u32 v15, v15, v173, v174\n"
    "v_max3_u32 v15, v15, v175, v192\n"
    "v_max3_u32 v15, v15, v193, v194\n"
    "v_max3_u32 v15, v15, v195, v196\n"
    "v_max3_u32 v15, v15, v197, v198\n"
    "v_max_u32 v15, v15, v199\n"
    "v_cmp_eq_u32 vcc, -1, v15\n"
    "s_nop 0\n"
    "s_cbranch_vccz Lsg125_%=\n"
    "s_sub_u32 s66, s66, 1\n"
    "s_cbranch_scc0 Lrp126_%=\n"
    "Lsg125_%=:\n"
    "s_mov_b32 s66, 1073741824\n"
    "s_add_u32 s67, s44, 4096\n"
    "buffer_load_dwordx4 v[200:203], v252, s[4:7], s67 offen offset:3072 sc1\n"
    "s_add_u32 s67, s44, 4096\n"
    "buffer_load_dwordx4 v[220:223], v253, s[4:7], s67 offen offset:3072 sc1\n"
    "s_add_u32 s67, s44, 4096\n"
    "buffer_load_dwordx4 v[224:227], v254, s[4:7], s67 offen offset:3072 sc1\n"
    "s_add_u32 s67, s44, 4096\n"
    "buffer_load_dwordx4 v[228:231], v255, s[4:7], s67 offen offset:3072 sc1\n"
    "s_add_u32 s67, s44, 8192\n"
    "buffer_load_dwordx4 v[232:235], v252, s[4:7], s67 offen offset:2048 sc1\n"
    "s_add_u32 s67, s44, 8192\n"
    "buffer_load_dwordx4 v[236:239], v253, s[4:7], s67 offen offset:2048 sc1\n"
    "s_add_u32 s67, s44, 8192\n"
    "buffer_load_dwordx4 v[240:243], v254, s[4:7], s67 offen offset:2048 sc1\n"
    "s_add_u32 s67, s44, 8192\n"
    "buffer_load_dwordx4 v[244:247], v255, s[4:7], s67 offen offset:2048 sc1\n"
    "buffer_load_dword v9, v1, s[4:7], s66 offen\n"
    "buffer_load_dword v9, v1, s[4:7], s66 offen\n"
    "buffer_load_dword v9, v1, s[4:7], s66 offen\n"
    "buffer_load_dword v9, v1, s[4:7], s66 offen\n"
    "s_add_u32 s67, s44, 0\n"
    "buffer_load_dwordx4 v[120:123], v252, s[4:7], s67 offen offset:2048 sc1\n"
    "s_add_u32 s67, s44, 0\n"
    "buffer_load_dwordx4 v[124:127], v253, s[4:7], s67 offen offset:2048 sc1\n"
    "s_add_u32 s67, s44, 0\n"
    "buffer_load_dwordx4 v[128:131], v254, s[4:7], s67 offen offset:2048 sc1\n"
    "s_add_u32 s67, s44, 0\n"
    "buffer_load_dwordx4 v[132:135], v255, s[4:7], s67 offen offset:2048 sc1\n"
    "s_add_u32 s67, s44, 4096\n"
    "buffer_load_dwordx4 v[148:151], v252, s[4:7], s67 offen offset:1024 sc1\n"
    "s_branch Lbk98_%=\n"
    "Lrt99_%=:\n"
    "s_mov_b32 s66, 65536\n"
    "Lrp128_%=:\n"
    "s_add_u32 s67, s44, 4096\n"
    "buffer_load_dwordx4 v[200:203], v252, s[4:7], s67 offen offset:3072 sc1\n"
    "s_add_u32 s67, s44, 4096\n"
    "buffer_load_dwordx4 v[220:223], v253, s[4:7], s67 offen offset:3072 sc1\n"
    "s_add_u32 s67, s44, 4096\n"
    "buffer_load_dwordx4 v[224:227], v254, s[4:7], s67 offen offset:3072 sc1\n"
    "s_add_u32 s67, s44, 4096\n"
    "buffer_load_dwordx4 v[228:231], v255, s[4:7], s67 offen offset:3072 sc1\n"
    "s_waitcnt vmcnt(0)\n"
    "v_max3_u32 v15, v200, v201, v202\n"
    "v_max3_u32 v15, v15, v203, v220\n"
    "v_max3_u32 v15, v15, v221, v222\n"
    "v_max3_u32 v15, v15, v223, v224\n"
    "v_max3_u32 v15, v15, v225, v226\n"
    "v_max3_u32 v15, v15, v227, v228\n"
    "v_max3_u32 v15, v15, v229, v230\n"
    "v_max_u32 v15, v15, v231\n"
    "v_cmp_eq_u32 vcc, -1, v15\n"
    "s_nop 0\n"
    "s_cbranch_vccz Lsg127_%=\n"
    "s_sub_u32 s66, s66, 1\n"
    "s_cbranch_scc0 Lrp128_%=\n"
    "Lsg127_%=:\n"
    "s_mov_b32 s66, 1073741824\n"
    "s_add_u32 s67, s44, 8192\n"
    "buffer_load_dwordx4 v[232:235], v252, s[4:7], s67 offen offset:2048 sc1\n"
    "s_add_u32 s67, s44, 8192\n"
    "buffer_load_dwordx4 v[236:239], v253, s[4:7], s67 offen offset:2048 sc1\n"
    "s_add_u32 s67, s44, 8192\n"
    "buffer_load_dwordx4 v[240:243], v254, s[4:7], s67 offen offset:2048 sc1\n"
    "s_add_u32 s67, s44, 8192\n"
    "buffer_load_dwordx4 v[244:247], v255, s[4:7], s67 offen offset:2048 sc1\n"
    "buffer_load_dword v9, v1, s[4:7], s66 offen\n"
    "buffer_load_dword v9, v1, s[4:7], s66 offen\n"
    "buffer_load_dword v9, v1, s[4:7], s66 offen\n"
    "buffer_load_dword v9, v1, s[4:7], s66 offen\n"
    "s_add_u32 s67, s44, 0\n"
    "buffer_load_dwordx4 v[120:123], v252, s[4:7], s67 offen offset:2048 sc1\n"
    "s_add_u32 s67, s44, 0\n"
    "buffer_load_dwordx4 v[124:127], v253, s[4:7], s67 offen offset:2048 sc1\n"
    "s_add_u32 s67, s44, 0\n"
    "buffer_load_dwordx4 v[128:131], v254, s[4:7], s67 offen offset:2048 sc1\n"
    "s_add_u32 s67, s44, 0\n"
    "buffer_load_dwordx4 v[132:135], v255, s[4:7], s67 offen offset:2048 sc1\n"
    "s_add_u32 s67, s44, 4096\n"
    "buffer_load_dwordx4 v[148:151], v252, s[4:7], s67 offen offset:1024 sc1\n"
    "s_add_u32 s67, s44, 4096\n"
    "buffer_load_dwordx4 v[152:155], v253, s[4:7], s67 offen offset:1024 sc1\n"
    "s_add_u32 s67, s44, 4096\n"
    "buffer_load_dwordx4 v[156:159], v254, s[4:7], s67 offen offset:1024 sc1\n"
    "s_add_u32 s67, s44, 4096\n"
    "buffer_load_dwordx4 v[160:163], v255, s[4:7], s67 offen offset:1024 sc1\n"
    "buffer_load_dword v9, v1, s[4:7], s66 offen\n"
    "buffer_load_dword v9, v1, s[4:7], s66 offen\n"
    "s_branch Lbk100_%=\n"
    "Lrt101_%=:\n"
    "s_mov_b32 s66, 65536\n"
    "Lrp130_%=:\n"
    "s_add_u32 s67, s44, 8192\n"
    "buffer_load_dwordx4 v[232:235], v252, s[4:7], s67 offen offset:2048 sc1\n"
    "s_add_u32 s67, s44, 8192\n"
    "buffer_load_dwordx4 v[236:239], v253, s[4:7], s67 offen offset:2048 sc1\n"
    "s_add_u32 s67, s44, 8192\n"
    "buffer_load_dwordx4 v[240:243], v254, s[4:7], s67 offen offset:2048 sc1\n"
    "s_add_u32 s67, s44, 8192\n"
    "buffer_load_dwordx4 v[244:247], v255, s[4:7], s67 offen offset:2048 sc1\n"
    "s_waitcnt vmcnt(0)\n"
    "v_max3_u32 v15, v232, v233, v234\n"
    "v_max3_u32 v15, v15, v235, v236\n"
    "v_max3_u32 v15, v15, v237, v238\n"
    "v_max3_u32 v15, v15, v239, v240\n"
    "v_max3_u32 v15, v15, v241, v242\n"
    "v_max3_u32 v15, v15, v243, v244\n"
    "v_max3_u32 v15, v15, v245, v246\n"
    "v_max_u32 v15, v15, v247\n"
    "v_cmp_eq_u32 vcc, -1, v15\n"
    "s_nop 0\n"
    "s_cbranch_vccz Lsg129_%=\n"
    "s_sub_u32 s66, s66, 1\n"
    "s_cbranch_scc0 Lrp130_%=\n"
    "Lsg129_%=:\n"
    "s_mov_b32 s66, 1073741824\n"
    "buffer_load_dword v9, v1, s[4:7], s66 offen\n"
    "buffer_load_dword v9, v1, s[4:7], s66 offen\n"
    "buffer_load_dword v9, v1, s[4:7], s66 offen\n"
    "buffer_load_dword v9, v1, s[4:7], s66 offen\n"
    "s_add_u32 s67, s44, 0\n"
    "buffer_load_dwordx4 v[120:123], v252, s[4:7], s67 offen offset:2048 sc1\n"
    "s_add_u32 s67, s44, 0\n"
    "buffer_load_dwordx4 v[124:127], v253, s[4:7], s67 offen offset:2048 sc1\n"
    "s_add_u32 s67, s44, 0\n"
    "buffer_load_dwordx4 v[128:131], v254, s[4:7], s67 offen offset:2048 sc1\n"
    "s_add_u32 s67, s44, 0\n"
    "buffer_load_dwordx4 v[132:135], v255, s[4:7], s67 offen offset:2048 sc1\n"
    "s_add_u32 s67, s44, 4096\n"
    "buffer_load_dwordx4 v[148:151], v252, s[4:7], s67 offen offset:1024 sc1\n"
    "s_add_u32 s67, s44, 4096\n"
    "buffer_load_dwordx4 v[152:155], v253, s[4:7], s67 offen offset:1024 sc1\n"
    "s_add_u32 s67, s44, 4096\n"
    "buffer_load_dwordx4 v[156:159], v254, s[4:7], s67 offen offset:1024 sc1\n"
    "s_add_u32 s67, s44, 4096\n"
    "buffer_load_dwordx4 v[160:163], v255, s[4:7], s67 offen offset:1024 sc1\n"
    "buffer_load_dword v9, v1, s[4:7], s66 offen\n"
    "buffer_load_dword v9, v1, s[4:7], s66 offen\n"
    "s_add_u32 s67, s44, 8192\n"
    "buffer_load_dwordx4 v[176:179], v252, s[4:7], s67 offen offset:0 sc1\n"
    "s_add_u32 s67, s44, 8192\n"
    "buffer_load_dwordx4 v[180:183], v253, s[4:7], s67 offen offset:0 sc1\n"
    "s_add_u32 s67, s44, 8192\n"
    "buffer_load_dwordx4 v[184:187], v254, s[4:7], s67 offen offset:0 sc1\n"
    "s_add_u32 s67, s44, 8192\n"
    "buffer_load_dwordx4 v[188:191], v255, s[4:7], s67 offen offset:0 sc1\n"
    "buffer_load_dword v9, v1, s[4:7], s66 offen\n"
    "s_branch Lbk102_%=\n"
    "Lrt103_%=:\n"
    "s_mov_b32 s66, 65536\n"
    "Lrp132_%=:\n"
    "s_add_u32 s67, s44, 0\n"
    "buffer_load_dwordx4 v[120:123], v252, s[4:7], s67 offen offset:2048 sc1\n"
    "s_add_u32 s67, s44, 0\n"
    "buffer_load_dwordx4 v[124:127], v253, s[4:7], s67 offen offset:2048 sc1\n"
    "s_add_u32 s67, s44, 0\n"
    "buffer_load_dwordx4 v[128:131], v254, s[4:7], s67 offen offset:2048 sc1\n"
    "s_add_u32 s67, s44, 0\n"
    "buffer_load_dwordx4 v[132:135], v255, s[4:7], s67 offen offset:2048 sc1\n"
    "s_waitcnt vmcnt(0)\n"
    "v_max3_u32 v15, v120, v121, v122\n"
    "v_max3_u32 v15, v15, v123, v124\n"
    "v_max3_u32 v15, v15, v125, v126\n"
    "v_max3_u32 v15, v15, v127, v128\n"
    "v_max3_u32 v15, v15, v129, v130\n"
    "v_max3_u32 v15, v15, v131, v132\n"
    "v_max3_u32 v15, v15, v133, v134\n"
    "v_max_u32 v15, v15, v135\n"
    "v_cmp_eq_u32 vcc, -1, v15\n"
    "s_nop 0\n"
    "s_cbranch_vccz Lsg131_%=\n"
    "s_sub_u32 s66, s66, 1\n"
    "s_cbranch_scc0 Lrp132_%=\n"
    "Lsg131_%=:\n"
    "s_mov_b32 s66, 1073741824\n"
    "s_add_u32 s67, s44, 4096\n"
    "buffer_load_dwordx4 v[148:151], v252, s[4:7], s67 offen offset:1024 sc1\n"
    "s_add_u32 s67, s44, 4096\n"
    "buffer_load_dwordx4 v[152:155], v253, s[4:7], s67 offen offset:1024 sc1\n"
    "s_add_u32 s67, s44, 4096\n"
    "buffer_load_dwordx4 v[156:159], v254, s[4:7], s67 offen offset:1024 sc1\n"
    "s_add_u32 s67, s44, 4096\n"
    "buffer_load_dwordx4 v[160:163], v255, s[4:7], s67 offen offset:1024 sc1\n"
    "buffer_load_dword v9, v1, s[4:7], s66 offen\n"
    "buffer_load_dword v9, v1, s[4:7], s66 offen\n"
    "s_add_u32 s67, s44, 8192\n"
    "buffer_load_dwordx4 v[176:179], v252, s[4:7], s67 offen offset:0 sc1\n"
    "s_add_u32 s67, s44, 8192\n"
    "buffer_load_dwordx4 v[180:183], v253, s[4:7], s67 offen offset:0 sc1\n"
    "s_add_u32 s67, s44, 8192\n"
    "buffer_load_dwordx4 v[184:187], v254, s[4:7], s67 offen offset:0 sc1\n"
    "s_add_u32 s67, s44, 8192\n"
    "buffer_load_dwordx4 v[188:191], v255, s[4:7], s67 offen offset:0 sc1\n"
    "buffer_load_dword v9, v1, s[4:7], s66 offen\n"
    "buffer_load_dword v9, v1, s[4:7], s66 offen\n"
    "s_add_u32 s67, s44, 8192\n"
    "buffer_load_dwordx4 v[204:207], v252, s[4:7], s67 offen offset:3072 sc1\n"
    "s_add_u32 s67, s44, 8192\n"
    "buffer_load_dwordx4 v[208:211], v253, s[4:7], s67 offen offset:3072 sc1\n"
    "s_add_u32 s67, s44, 8192\n"
    "buffer_load_dwordx4 v[212:215], v254, s[4:7], s67 offen offset:3072 sc1\n"
    "s_add_u32 s67, s44, 8192\n"
    "buffer_load_dwordx4 v[216:219], v255, s[4:7], s67 offen offset:3072 sc1\n"
    "s_branch Lbk104_%=\n"
    "Lrt105_%=:\n"
    "s_mov_b32 s66, 65536\n"
    "Lrp134_%=:\n"
    "s_add_u32 s67, s44, 4096\n"
    "buffer_load_dwordx4 v[148:151], v252, s[4:7], s67 offen offset:1024 sc1\n"
    "s_add_u32 s67, s44, 4096\n"
    "buffer_load_dwordx4 v[152:155], v253, s[4:7], s67 offen offset:1024 sc1\n"
    "s_add_u32 s67, s44, 4096\n"
    "buffer_load_dwordx4 v[156:159], v254, s[4:7], s67 offen offset:1024 sc1\n"
    "s_add_u32 s67, s44, 4096\n"
    "buffer_load_dwordx4 v[160:163], v255, s[4:7], s67 offen offset:1024 sc1\n"
    "s_waitcnt vmcnt(0)\n"
    "v_max3_u32 v15, v148, v149, v150\n"
    "v_max3_u32 v15, v15, v151, v152\n"
    "v_max3_u32 v15, v15, v153, v154\n"
    "v_max3_u32 v15, v15, v155, v156\n"
    "v_max3_u32 v15, v15, v157, v158\n"
    "v_max3_u32 v15, v15, v159, v160\n"
    "v_max3_u32 v15, v15, v161, v162\n"
    "v_max_u32 v15, v15, v163\n"
    "v_cmp_eq_u32 vcc, -1, v15\n"
    "s_nop 0\n"
    "s_cbranch_vccz Lsg133_%=\n"
    "s_sub_u32 s66, s66, 1\n"
    "s_cbranch_scc0 Lrp134_%=\n"
    "Lsg133_%=:\n"
    "s_mov_b32 s66, 1073741824\n"
    "buffer_load_dword v9, v1, s[4:7], s66 offen\n"
    "buffer_load_dword v9, v1, s[4:7], s66 offen\n"
    "s_add_u32 s67, s44, 8192\n"
    "buffer_load_dwordx4 v[176:179], v252, s[4:7], s67 offen offset:0 sc1\n"
    "s_add_u32 s67, s44, 8192\n"
    "buffer_load_dwordx4 v[180:183], v253, s[4:7], s67 offen offset:0 sc1\n"
    "s_add_u32 s67, s44, 8192\n"
    "buffer_load_dwordx4 v[184:187], v254, s[4:7], s67 offen offset:0 sc1\n"
    "s_add_u32 s67, s44, 8192\n"
    "buffer_load_dwordx4 v[188:191], v255, s[4:7], s67 offen offset:0 sc1\n"
    "buffer_load_dword v9, v1, s[4:7], s66 offen\n"
    "buffer_load_dword v9, v1, s[4:7], s66 offen\n"
    "s_add_u32 s67, s44, 8192\n"
    "buffer_load_dwordx4 v[204:207], v252, s[4:7], s67 offen offset:3072 sc1\n"
    "s_add_u32 s67, s44, 8192\n"
    "buffer_load_dwordx4 v[208:211], v253, s[4:7], s67 offen offset:3072 sc1\n"
    "s_add_u32 s67, s44, 8192\n"
    "buffer_load_dwordx4 v[212:215], v254, s[4:7], s67 offen offset:3072 sc1\n"
    "s_add_u32 s67, s44, 8192\n"
    "buffer_load_dwordx4 v[216:219], v255, s[4:7], s67 offen offset:3072 sc1\n"
    "buffer_load_dword v9, v1, s[4:7], s66 offen\n"
    "buffer_load_dword v9, v1, s[4:7], s66 offen\n"
    "s_add_u32 s67, s45, 0\n"
    "buffer_load_dwordx4 v[136:139], v252, s[4:7], s67 offen offset:0 sc1\n"
    "s_add_u32 s67, s45, 0\n"
    "buffer_load_dwordx4 v[140:143], v253, s[4:7], s67 offen offset:0 sc1\n"
    "s_add_u32 s67, s45, 0\n"
    "buffer_load_dwordx4 v[144:147], v254, s[4:7], s67 offen offset:0 sc1\n"
    "s_branch Lbk106_%=\n"
    "Lrt107_%=:\n"
    "s_mov_b32 s66, 65536\n"
    "Lrp136_%=:\n"
    "s_add_u32 s67, s44, 8192\n"
    "buffer_load_dwordx4 v[176:179], v252, s[4:7], s67 offen offset:0 sc1\n"
    "s_add_u32 s67, s44, 8192\n"
    "buffer_load_dwordx4 v[180:183], v253, s[4:7], s67 offen offset:0 sc1\n"
    "s_add_u32 s67, s44, 8192\n"
    "buffer_load_dwordx4 v[184:187], v254, s[4:7], s67 offen offset:0 sc1\n"
    "s_add_u32 s67, s44, 8192\n"
    "buffer_load_dwordx4 v[188:191], v255, s[4:7], s67 offen offset:0 sc1\n"
    "s_waitcnt vmcnt(0)\n"
    "v_max3_u32 v15, v176, v177, v178\n"
    "v_max3_u32 v15, v15, v179, v180\n"
    "v_max3_u32 v15, v15, v181, v182\n"
    "v_max3_u32 v15, v15, v183, v184\n"
    "v_max3_u32 v15, v15, v185, v186\n"
    "v_max3_u32 v15, v15, v187, v188\n"
    "v_max3_u32 v15, v15, v189, v190\n"
    "v_max_u32 v15, v15, v191\n"
    "v_cmp_eq_u32 vcc, -1, v15\n"
    "s_nop 0\n"
    "s_cbranch_vccz Lsg135_%=\n"
    "s_sub_u32 s66, s66, 1\n"
    "s_cbranch_scc0 Lrp136_%=\n"
    "Lsg135_%=:\n"
    "s_mov_b32 s66, 1073741824\n"
    "buffer_load_dword v9, v1, s[4:7], s66 offen\n"
    "buffer_load_dword v9, v1, s[4:7], s66 offen\n"
    "s_add_u32 s67, s44, 8192\n"
    "buffer_load_dwordx4 v[204:207], v252, s[4:7], s67 offen offset:3072 sc1\n"
    "s_add_u32 s67, s44, 8192\n"
    "buffer_load_dwordx4 v[208:211], v253, s[4:7], s67 offen offset:3072 sc1\n"
    "s_add_u32 s67, s44, 8192\n"
    "buffer_load_dwordx4 v[212:215], v254, s[4:7], s67 offen offset:3072 sc1\n"
    "s_add_u32 s67, s44, 8192\n"
    "buffer_load_dwordx4 v[216:219], v255, s[4:7], s67 offen offset:3072 sc1\n"
    "buffer_load_dword v9, v1, s[4:7], s66 offen\n"
    "buffer_load_dword v9, v1, s[4:7], s66 offen\n"
    "s_add_u32 s67, s45, 0\n"
    "buffer_load_dwordx4 v[136:139], v252, s[4:7], s67 offen offset:0 sc1\n"
    "s_add_u32 s67, s45, 0\n"
    "buffer_load_dwordx4 v[140:143], v253, s[4:7], s67 offen offset:0 sc1\n"
    "s_add_u32 s67, s45, 0\n"
    "buffer_load_dwordx4 v[144:147], v254, s[4:7], s67 offen offset:0 sc1\n"
    "s_add_u32 s67, s45, 0\n"
    "buffer_load_dwordx4 v[164:167], v255, s[4:7], s67 offen offset:0 sc1\n"
    "s_add_u32 s67, s45, 0\n"
    "buffer_load_dwordx4 v[168:171], v252, s[4:7], s67 offen offset:1024 sc1\n"
    "s_add_u32 s67, s45, 0\n"
    "buffer_load_dwordx4 v[172:175], v253, s[4:7], s67 offen offset:1024 sc1\n"
    "s_add_u32 s67, s45, 0\n"
    "buffer_load_dwordx4 v[192:195], v254, s[4:7], s67 offen offset:1024 sc1\n"
    "s_add_u32 s67, s45, 0\n"
    "buffer_load_dwordx4 v[196:199], v255, s[4:7], s67 offen offset:1024 sc1\n"
    "s_branch Lbk108_%=\n"
    "Lrt109_%=:\n"
    "s_mov_b32 s66, 65536\n"
    "Lrp138_%=:\n"
    "s_add_u32 s67, s44, 8192\n"
    "buffer_load_dwordx4 v[204:207], v252, s[4:7], s67 offen offset:3072 sc1\n"
    "s_add_u32 s67, s44, 8192\n"
    "buffer_load_dwordx4 v[208:211], v253, s[4:7], s67 offen offset:3072 sc1\n"
    "s_add_u32 s67, s44, 8192\n"
    "buffer_load_dwordx4 v[212:215], v254, s[4:7], s67 offen offset:3072 sc1\n"
    "s_add_u32 s67, s44, 8192\n"
    "buffer_load_dwordx4 v[216:219], v255, s[4:7], s67 offen offset:3072 sc1\n"
    "s_waitcnt vmcnt(0)\n"
    "v_max3_u32 v15, v204, v205, v206\n"
    "v_max3_u32 v15, v15, v207, v208\n"
    "v_max3_u32 v15, v15, v209, v210\n"
    "v_max3_u32 v15, v15, v211, v212\n"
    "v_max3_u32 v15, v15, v213, v214\n"
    "v_max3_u32 v15, v15, v215, v216\n"
    "v_max3_u32 v15, v15, v217, v218\n"
    "v_max_u32 v15, v15, v219\n"
    "v_cmp_eq_u32 vcc, -1, v15\n"
    "s_nop 0\n"
    "s_cbranch_vccz Lsg137_%=\n"
    "s_sub_u32 s66, s66, 1\n"
    "s_cbranch_scc0 Lrp138_%=\n"
    "Lsg137_%=:\n"
    "s_mov_b32 s66, 1073741824\n"
    "buffer_load_dword v9, v1, s[4:7], s66 offen\n"
    "buffer_load_dword v9, v1, s[4:7], s66 offen\n"
    "s_add_u32 s67, s45, 0\n"
    "buffer_load_dwordx4 v[136:139], v252, s[4:7], s67 offen offset:0 sc1\n"
    "s_add_u32 s67, s45, 0\n"
    "buffer_load_dwordx4 v[140:143], v253, s[4:7], s67 offen offset:0 sc1\n"
    "s_add_u32 s67, s45, 0\n"
    "buffer_load_dwordx4 v[144:147], v254, s[4:7], s67 offen offset:0 sc1\n"
    "s_add_u32 s67, s45, 0\n"
    "buffer_load_dwordx4 v[164:167], v255, s[4:7], s67 offen offset:0 sc1\n"
    "s_add_u32 s67, s45, 0\n"
    "buffer_load_dwordx4 v[168:171], v252, s[4:7], s67 offen offset:1024 sc1\n"
    "s_add_u32 s67, s45, 0\n"
    "buffer_load_dwordx4 v[172:175], v253, s[4:7], s67 offen offset:1024 sc1\n"
    "s_add_u32 s67, s45, 0\n"
    "buffer_load_dwordx4 v[192:195], v254, s[4:7], s67 offen offset:1024 sc1\n"
    "s_add_u32 s67, s45, 0\n"
    "buffer_load_dwordx4 v[196:199], v255, s[4:7], s67 offen offset:1024 sc1\n"
    "s_branch Lbk110_%=\n"
    "Lrt111_%=:\n"
    "s_mov_b32 s66, 65536\n"
    "Lrp140_%=:\n"
    "s_add_u32 s67, s45, 0\n"
    "buffer_load_dwordx4 v[136:139], v252, s[4:7], s67 offen offset:0 sc1\n"
    "s_add_u32 s67, s45, 0\n"
    "buffer_load_dwordx4 v[140:143], v253, s[4:7], s67 offen offset:0 sc1\n"
    "s_add_u32 s67, s45, 0\n"
    "buffer_load_dwordx4 v[144:147], v254, s[4:7], s67 offen offset:0 sc1\n"
    "s_add_u32 s67, s45, 0\n"
    "buffer_load_dwordx4 v[164:167], v255, s[4:7], s67 offen offset:0 sc1\n"
    "s_waitcnt vmcnt(0)\n"
    "v_max3_u32 v15, v136, v137, v138\n"
    "v_max3_u32 v15, v15, v139, v140\n"
    "v_max3_u32 v15, v15, v141, v142\n"
    "v_max3_u32 v15, v15, v143, v144\n"
    "v_max3_u32 v15, v15, v145, v146\n"
    "v_max3_u32 v15, v15, v147, v164\n"
    "v_max3_u32 v15, v15, v165, v166\n"
    "v_max_u32 v15, v15, v167\n"
    "v_cmp_eq_u32 vcc, -1, v15\n"
    "s_nop 0\n"
    "s_cbranch_vccz Lsg139_%=\n"
    "s_sub_u32 s66, s66, 1\n"
    "s_cbranch_scc0 Lrp140_%=\n"
    "Lsg139_%=:\n"
    "s_mov_b32 s66, 1073741824\n"
    "s_add_u32 s67, s45, 0\n"
    "buffer_load_dwordx4 v[168:171], v252, s[4:7], s67 offen offset:1024 sc1\n"
    "s_add_u32 s67, s45, 0\n"
    "buffer_load_dwordx4 v[172:175], v253, s[4:7], s67 offen offset:1024 sc1\n"
    "s_add_u32 s67, s45, 0\n"
    "buffer_load_dwordx4 v[192:195], v254, s[4:7], s67 offen offset:1024 sc1\n"
    "s_add_u32 s67, s45, 0\n"
    "buffer_load_dwordx4 v[196:199], v255, s[4:7], s67 offen offset:1024 sc1\n"
    "s_branch Lbk112_%=\n"
    "Lrt113_%=:\n"
    "s_mov_b32 s66, 65536\n"
    "Lrp142_%=:\n"
    "s_add_u32 s67, s45, 0\n"
    "buffer_load_dwordx4 v[168:171], v252, s[4:7], s67 offen offset:1024 sc1\n"
    "s_add_u32 s67, s45, 0\n"
    "buffer_load_dwordx4 v[172:175], v253, s[4:7], s67 offen offset:1024 sc1\n"
    "s_add_u32 s67, s45, 0\n"
    "buffer_load_dwordx4 v[192:195], v254, s[4:7], s67 offen offset:1024 sc1\n"
    "s_add_u32 s67, s45, 0\n"
    "buffer_load_dwordx4 v[196:199], v255, s[4:7], s67 offen offset:1024 sc1\n"
    "s_waitcnt vmcnt(0)\n"
    "v_max3_u32 v15, v168, v169, v170\n"
    "v_max3_u32 v15, v15, v171, v172\n"
    "v_max3_u32 v15, v15, v173, v174\n"
    "v_max3_u32 v15, v15, v175, v192\n"
    "v_max3_u32 v15, v15, v193, v194\n"
    "v_max3_u32 v15, v15, v195, v196\n"
    "v_max3_u32 v15, v15, v197, v198\n"
    "v_max_u32 v15, v15, v199\n"
    "v_cmp_eq_u32 vcc, -1, v15\n"
    "s_nop 0\n"
    "s_cbranch_vccz Lsg141_%=\n"
    "s_sub_u32 s66, s66, 1\n"
    "s_cbranch_scc0 Lrp142_%=\n"
    "Lsg141_%=:\n"
    "s_mov_b32 s66, 1073741824\n"
    "s_branch Lbk114_%=\n"
    "Lend_%=:\n"
    : "+{v0}"(tid), "+{s2}"(bid), "+{s[0:1]}"(ka), "+{s[66:67]}"(tb)
    :
    : "v1", "v2", "v3", "v4", "v5", "v6", "v7", "v8", "v9", "v10", "v11", "v12", "v13", "v14", "v15", "v16", "v17", "v18", "v19", "v20", "v21", "v22", "v23", "v24", "v25", "v26", "v27", "v28", "v29", "v30", "v31", "v32", "v33", "v34", "v35", "v36", "v37", "v38", "v39", "v40", "v41", "v42", "v43", "v44", "v45", "v46", "v47", "v48", "v49", "v50", "v51", "v52", "v53", "v54", "v55", "v56", "v57", "v58", "v59", "v60", "v61", "v62", "v63", "v64", "v65", "v66", "v67", "v68", "v69", "v70", "v71", "v72", "v73", "v74", "v75", "v76", "v77", "v78", "v79", "v80", "v81", "v82", "v83", "v84", "v85", "v86", "v87", "v88", "v89", "v90", "v91", "v92", "v93", "v94", "v95", "v96", "v97", "v98", "v99", "v100", "v101", "v102", "v103", "v104", "v105", "v106", "v107", "v108", "v109", "v110", "v111", "v112", "v113", "v114", "v115", "v116", "v117", "v118", "v119", "v120", "v121", "v122", "v123", "v124", "v125", "v126", "v127", "v128", "v129", "v130", "v131", "v132", "v133", "v134", "v135", "v136", "v137", "v138", "v139", "v140", "v141", "v142", "v143", "v144", "v145", "v146", "v147", "v148", "v149", "v150", "v151", "v152", "v153", "v154", "v155", "v156", "v157", "v158", "v159", "v160", "v161", "v162", "v163", "v164", "v165", "v166", "v167", "v168", "v169", "v170", "v171", "v172", "v173", "v174", "v175", "v176", "v177", "v178", "v179", "v180", "v181", "v182", "v183", "v184", "v185", "v186", "v187", "v188", "v189", "v190", "v191", "v192", "v193", "v194", "v195", "v196", "v197", "v198", "v199", "v200", "v201", "v202", "v203", "v204", "v205", "v206", "v207", "v208", "v209", "v210", "v211", "v212", "v213", "v214", "v215", "v216", "v217", "v218", "v219", "v220", "v221", "v222", "v223", "v224", "v225", "v226", "v227", "v228", "v229", "v230", "v231", "v232", "v233", "v234", "v235", "v236", "v237", "v238", "v239", "v240", "v241", "v242", "v243", "v244", "v245", "v246", "v247", "v248", "v249", "v250", "v251", "v252", "v253", "v254", "v255", "a0", "a1", "a2", "a3", "a4", "a5", "a6", "a7", "a8", "a9", "a10", "a11", "a12", "a13", "a14", "a15", "a16", "a17", "a18", "a19", "a20", "a21", "a22", "a23", "a24", "a25", "a26", "a27", "a28", "a29", "a30", "a31", "a32", "a33", "a34", "a35", "a36", "a37", "a38", "a39", "a40", "a41", "a42", "a43", "a44", "a45", "a46", "a47", "a48", "a49", "a50", "a51", "a52", "a53", "a54", "a55", "a56", "a57", "a58", "a59", "a60", "a61", "a62", "a63", "a64", "a65", "a66", "a67", "a68", "a69", "a70", "a71", "a72", "a73", "a74", "a75", "a76", "a77", "a78", "a79", "a80", "a81", "a82", "a83", "a84", "a85", "a86", "a87", "a88", "a89", "a90", "a91", "a92", "a93", "a94", "a95", "a96", "a97", "a98", "a99", "a100", "a101", "a102", "a103", "a104", "a105", "a106", "a107", "a108", "a109", "a110", "a111", "a112", "a113", "a114", "a115", "a116", "a117", "a118", "a119", "a120", "a121", "a122", "a123", "a124", "a125", "a126", "a127", "a128", "a129", "a130", "a131", "a132", "a133", "a134", "a135", "a136", "a137", "a138", "a139", "a140", "a141", "a142", "a143", "a144", "a145", "a146", "a147", "a148", "a149", "a150", "a151", "a152", "a153", "a154", "a155", "a156", "a157", "a158", "a159", "a160", "a161", "a162", "a163", "a164", "a165", "a166", "a167", "a168", "a169", "a170", "a171", "a172", "a173", "a174", "a175", "a176", "a177", "a178", "a179", "a180", "a181", "a182", "a183", "a184", "a185", "a186", "a187", "a188", "a189", "a190", "a191", "a192", "a193", "a194", "a195", "a196", "a197", "a198", "a199", "a200", "a201", "a202", "a203", "a204", "a205", "a206", "a207", "a208", "a209", "a210", "a211", "a212", "a213", "a214", "a215", "a216", "a217", "a218", "a219", "a220", "a221", "a222", "a223", "a224", "a225", "a226", "a227", "a228", "a229", "a230", "a231", "a232", "a233", "a234", "a235", "a236", "a237", "a238", "a239", "a240", "a241", "a242", "a243", "a244", "a245", "a246", "a247", "a248", "a249", "a250", "a251", "a252", "a253", "a254", "a255", "s3", "s4", "s5", "s6", "s7", "s8", "s9", "s10", "s11", "s12", "s13", "s14", "s15", "s16", "s17", "s18", "s19", "s20", "s21", "s22", "s23", "s24", "s25", "s26", "s27", "s28", "s29", "s30", "s31", "s34", "s35", "s36", "s37", "s38", "s39", "s40", "s41", "s42", "s43", "s44", "s45", "s46", "s47", "s48", "s49", "s50", "s51", "s52", "s53", "s54", "s55", "s56", "s57", "s58", "s59", "s60", "s61", "s62", "s63", "s64", "s65", "s68", "s69", "s70", "s71", "s72", "s73", "s74", "s75", "s76", "s77", "s78", "s79", "s80", "s81", "s82", "s83", "s84", "s85", "s86", "s87", "s88", "s89", "s90", "s91", "s92", "s93", "s94", "s95", "s96", "s97", "s98", "s99", "vcc", "scc", "memory");
}

extern "C" void kernel_launch(void* const* d_in, const int* in_sizes, int n_in, void* d_out, int out_size, void* d_ws,
                              size_t ws_size, hipStream_t stream) {
  sequential_preLN__sliding_local_dilated__dense_activation_gelu__prompt_kernel<<<256, 256, 0, stream>>>(d_in[0], d_in[1], d_in[2], d_in[4], d_in[5], d_in[6], d_in[7], d_out, d_ws);
}
```
